```python
import torch

def _softmax(x, axis=-1):
    return torch.softmax(x, dim=axis)
def _gelu(x):
    return torch.nn.functional.gelu(x, approximate="tanh")
def _silu(x):
    return torch.nn.functional.silu(x)
def _sigmoid(x):
    return torch.sigmoid(x)
def _layernorm(x, axis=-1, eps=1e-5):
    m = x.mean(dim=axis, keepdim=True)
    v = x.var(dim=axis, keepdim=True, unbiased=False)
    return (x - m) / torch.sqrt(v + eps)
def _scatter_add(tgt, idx, src, axis=0):
    return tgt.clone().index_add_(axis, idx, src)
def _segment_sum(src, seg, n):
    out = torch.zeros((n,) + src.shape[1:], dtype=src.dtype)
    return out.index_add_(0, seg, src)
def _pad(x, pw):
    flat = tuple(v for pair in reversed(pw) for v in pair)
    return torch.nn.functional.pad(x, flat)

kernel_name = 'recurrent_scan__softmax_full_mha__dense_activation_squared_relu__prompt'

def setup_inputs():

    _w = (lambda a: (lambda t: t.to(torch.bfloat16) if t.is_floating_point() else t)(torch.as_tensor(a)))
    def _A_setup():
        B, S, D = 1, 2048, 576
        return {
            "x": _w((lambda *s: torch.randn(*s, dtype=torch.bfloat16) * 0.05)(B, S, D)),
            "wqkv": _w((lambda *s: torch.randn(*s, dtype=torch.bfloat16) * 0.05)(D, 3 * D)),
            "wo": _w((lambda *s: torch.randn(*s, dtype=torch.bfloat16) * 0.05)(D, D)),
        }
    _A_in = _A_setup()

    def _B_setup():
        B, S, D, F = 1, 2048, 576, 4608
        return {
            "x": _w((lambda *s: torch.randn(*s, dtype=torch.bfloat16) * 0.05)(B, S, D)),
            "w1": _w((lambda *s: torch.randn(*s, dtype=torch.bfloat16) * 0.05)(D, F)),
            "b1": _w((lambda *s: torch.randn(*s, dtype=torch.bfloat16) * 0.4)(F)),
            "w2": _w((lambda *s: torch.randn(*s, dtype=torch.bfloat16) * 0.05)(F, D)),
            "b2": _w((lambda *s: torch.randn(*s, dtype=torch.bfloat16) * 3.2)(D)),
        }
    _B_in = _B_setup()

    return {
        "A_x": _A_in["x"],
        "A_wqkv": _A_in["wqkv"],
        "A_wo": _A_in["wo"],
        "B_x": _B_in["x"],
        "B_w1": _B_in["w1"],
        "B_b1": _B_in["b1"],
        "B_w2": _B_in["w2"],
        "B_b2": _B_in["b2"],
    }

def reference(A_x, A_wqkv, A_wo, B_x, B_w1, B_b1, B_w2, B_b2):
    def _A_ref(x, wqkv, wo):
        B, S, D = x.shape
        H = 32; Dh = D // H
        qkv = (lambda x, *d: x.permute(*d))((x @ wqkv).reshape(B, S, 3, H, Dh), 2, 0, 3, 1, 4)
        q, k, v = qkv[0], qkv[1], qkv[2]
        scale = Dh ** -0.5
        scores = torch.einsum("bhqd,bhkd->bhqk", q, k) * scale
        p = _softmax(scores, axis=-1)
        o = torch.einsum("bhqk,bhkd->bhqd", p, v)
        return (lambda x, *d: x.permute(*d))(o, 0, 2, 1, 3).reshape(B, S, D) @ wo

    def _B_ref(x, w1, b1, w2, b2):
        h = torch.einsum("bsd,df->bsf", x, w1) + b1
        zero = torch.zeros_like(h)
        r = torch.where(h > zero, h, zero)
        h = r * r
        out = torch.einsum("bsf,fd->bsd", h, w2) + b2
        return out

    x = A_x
    xn = _layernorm(x, axis=-1)
    s = _A_ref(xn, A_wqkv, A_wo)
    g = _silu(_B_ref(xn, B_w1, B_b1, B_w2, B_b2))
    return x + s * g
```

```cpp
#include <hip/hip_runtime.h>
#include <hip/hip_bf16.h>
#include <stdint.h>
typedef __hip_bfloat16 bf16;
constexpr int S=2048, D=576, H=32, DH=18, F=4608;
__device__ __forceinline__ float b2f(bf16 v){return __bfloat162float(v);}
__device__ __forceinline__ float rbf(float v){return __bfloat162float(__float2bfloat16(v));}

__global__ void k_ln(const bf16* x, bf16* xn){
    int row=blockIdx.x; __shared__ float red[256];
    float s=0; for(int i=threadIdx.x;i<D;i+=256) s+=b2f(x[row*D+i]);
    red[threadIdx.x]=s; __syncthreads();
    for(int o=128;o>0;o>>=1){ if(threadIdx.x<o) red[threadIdx.x]+=red[threadIdx.x+o]; __syncthreads(); }
    float mean=red[0]/D; __syncthreads();
    s=0; for(int i=threadIdx.x;i<D;i+=256){float d=b2f(x[row*D+i])-mean; s+=d*d;}
    red[threadIdx.x]=s; __syncthreads();
    for(int o=128;o>0;o>>=1){ if(threadIdx.x<o) red[threadIdx.x]+=red[threadIdx.x+o]; __syncthreads(); }
    float var=red[0]/D;
    float rstd=1.0f/sqrtf(var+1e-5f);
    for(int i=threadIdx.x;i<D;i+=256) xn[row*D+i]=__float2bfloat16((b2f(x[row*D+i])-mean)*rstd);
}
template<int MODE>
__global__ void k_gemm(const bf16* A,const bf16* B,const bf16* bias,void* Cout,int N,int K,const bf16* x,const float* g){
    int n=blockIdx.x*256+threadIdx.x; int m=blockIdx.y; if(n>=N) return;
    float acc=0; for(int k=0;k<K;++k) acc+=b2f(A[(size_t)m*K+k])*b2f(B[(size_t)k*N+n]);
    if(MODE==0) ((bf16*)Cout)[(size_t)m*N+n]=__float2bfloat16(acc);
    if(MODE==1){ float h=acc+b2f(bias[n]); h=h>0?h:0; ((bf16*)Cout)[(size_t)m*N+n]=__float2bfloat16(h*h);}
    if(MODE==2){ float v=acc+b2f(bias[n]); ((float*)Cout)[(size_t)m*N+n]=v/(1.f+__expf(-v)); }
    if(MODE==3){ ((bf16*)Cout)[(size_t)m*N+n]=__float2bfloat16(b2f(x[(size_t)m*N+n])+acc*g[(size_t)m*N+n]); }
}
__global__ void k_attn(const bf16* qkv, bf16* o){
    int h=blockIdx.y; int q=blockIdx.x*64+threadIdx.x;
    float qv[DH]; for(int d=0;d<DH;++d) qv[d]=b2f(qkv[(size_t)q*3*D+h*DH+d])*0.23570226f;
    float m=-1e30f,l=0,acc[DH]; for(int d=0;d<DH;++d) acc[d]=0;
    for(int j=0;j<S;++j){
        float s=0; for(int d=0;d<DH;++d) s+=qv[d]*b2f(qkv[(size_t)j*3*D+D+h*DH+d]);
        float mn=fmaxf(m,s); float a=__expf(m-mn), p=__expf(s-mn);
        l=l*a+p; for(int d=0;d<DH;++d) acc[d]=acc[d]*a+p*b2f(qkv[(size_t)j*3*D+2*D+h*DH+d]);
        m=mn;
    }
    for(int d=0;d<DH;++d) o[(size_t)q*D+h*DH+d]=__float2bfloat16(acc[d]/l);
}
extern "C" void kernel_launch(void* const* d_in, const int* in_sizes, int n_in, void* d_out, int out_size, void* d_ws, size_t ws_size, hipStream_t stream){
    const bf16* x=(const bf16*)d_in[0]; const bf16* wqkv=(const bf16*)d_in[1]; const bf16* wo=(const bf16*)d_in[2];
    const bf16* w1=(const bf16*)d_in[4]; const bf16* b1=(const bf16*)d_in[5]; const bf16* w2=(const bf16*)d_in[6]; const bf16* b2=(const bf16*)d_in[7];
    char* ws=(char*)d_ws;
    bf16* xn=(bf16*)ws; bf16* qkv=(bf16*)(ws+(4<<20)); bf16* o=(bf16*)(ws+(12<<20)); bf16* h=(bf16*)(ws+(16<<20)); float* g=(float*)(ws+(40<<20));
    k_ln<<<S,256,0,stream>>>(x,xn);
    k_gemm<0><<<dim3((3*D+255)/256,S),256,0,stream>>>(xn,wqkv,nullptr,qkv,3*D,D,nullptr,nullptr);
    k_attn<<<dim3(S/64,H),64,0,stream>>>(qkv,o);
    k_gemm<1><<<dim3(F/256,S),256,0,stream>>>(xn,w1,b1,h,F,D,nullptr,nullptr);
    k_gemm<2><<<dim3((D+255)/256,S),256,0,stream>>>(h,w2,b2,g,D,F,nullptr,nullptr);
    k_gemm<3><<<dim3((D+255)/256,S),256,0,stream>>>(o,wo,nullptr,d_out,D,D,x,g);
}
```

```cpp
#include <hip/hip_runtime.h>
#include <hip/hip_bf16.h>
#include <stdint.h>
constexpr size_t WS_G = 0;
constexpr size_t WS_FLAG = 1024;
constexpr size_t WS_RS2 = 4096;
constexpr size_t WS_KV = 16384;
constexpr size_t WS_K2 = 4734976;
constexpr size_t WS_Q1 = 4997120;
constexpr size_t WS_Q2 = 7094272;
constexpr size_t WS_O = 7356416;
constexpr size_t WS_PART = 9715712;
constexpr size_t WS_MSUM = 47464448;
constexpr size_t WS_END = 52183040;
struct K2Params { const void* x; const void* wo; const void* b2; const void* ws; void* out; };
__device__ __forceinline__ float bf_lo(unsigned u) { return __uint_as_float(u << 16); }
__device__ __forceinline__ float bf_hi(unsigned u) { return __uint_as_float(u & 0xffff0000u); }
__device__ __forceinline__ unsigned pack_bf16(float a, float b) {
    typedef float f32x2 __attribute__((ext_vector_type(2))); typedef __bf16 bf16x2 __attribute__((ext_vector_type(2)));
    f32x2 v = {a, b}; return __builtin_bit_cast(unsigned, __builtin_convertvector(v, bf16x2));
}
extern "C" __global__ __launch_bounds__(256, 1) void k2_final(K2Params prm) {
    asm volatile(
".p2align 3\n"
"LK2_CODE_START:\n"
"s_load_dwordx8 s[16:23], %0, 0\n"
"s_load_dwordx2 s[24:25], %0, 32\n"
"v_mov_b32 v151, %2\n"
"v_and_b32 v144, 63, v151\n"
"v_lshrrev_b32 v112, 6, v151\n"
"s_nop 0\n"
"v_readfirstlane_b32 s52, v112\n"
"s_getpc_b64 s[56:57]\n"
"LK2PC:\n"
"s_sub_u32 s56, s56, LK2PC-LK2_CODE_START\n"
"s_subb_u32 s57, s57, 0\n"
"s_mov_b32 s40, s56\n"
"s_and_b32 s41, s57, 0xffff\n"
"s_mov_b32 s42, LK2_CODE_END-LK2_CODE_START\n"
"s_mov_b32 s43, 0x20000\n"
"v_lshlrev_b32_e64 v121, 7, v144\n"
"buffer_load_dword v122, v121, s[40:43], 0 offen\n"
"s_mov_b32 s56, %1\n"
"s_and_b32 s57, s56, 7\n"
"s_lshl_b32 s57, s57, 1\n"
"s_bfe_u32 s58, s56, 0x10003\n"
"s_add_u32 s57, s57, s58\n"
"s_lshr_b32 s54, s56, 4\n"
"s_lshl_b32 s53, s57, 2\n"
"s_add_u32 s53, s53, s52\n"
"v_lshlrev_b32 v145, 4, v144\n"
"s_waitcnt lgkmcnt(0)\n"
"s_mov_b32 s28, s22\n"
"s_and_b32 s29, s23, 0xffff\n"
"s_mov_b32 s30, 52183040\n"
"s_mov_b32 s31, 0x20000\n"
"s_mov_b32 s36, s18\n"
"s_and_b32 s37, s19, 0xffff\n"
"s_mov_b32 s38, 663552\n"
"s_mov_b32 s39, 0x20000\n"
"s_mul_i32 s56, s53, 36864\n"
"s_add_u32 s56, s56, 7356416\n"
"v_lshrrev_b32 v112, 3, v144\n"
"v_lshrrev_b32_e64 v113, 1, v112\n"
"v_and_b32 v113, 1, v113\n"
"v_lshlrev_b32 v113, 2, v113\n"
"v_and_b32 v114, 7, v144\n"
"v_xor_b32 v114, v114, v113\n"
"v_mul_u32_u24 v112, 1152, v112\n"
"v_lshl_add_u32 v112, v114, 4, v112\n"
"s_lshl_b32 s57, s54, 7\n"
"s_mul_i32 s58, s52, 165888\n"
"s_add_u32 s57, s57, s58\n"
"s_mul_i32 s58, s52, 18432\n"
"s_add_u32 m0, s58, 0\n"
"s_add_u32 s60, s57, 0\n"
"buffer_load_dwordx4 v112, s[36:39], s60 offen lds\n"
"s_add_u32 m0, s58, 1024\n"
"s_add_u32 s60, s57, 9216\n"
"buffer_load_dwordx4 v112, s[36:39], s60 offen lds\n"
"s_add_u32 m0, s58, 2048\n"
"s_add_u32 s60, s57, 18432\n"
"buffer_load_dwordx4 v112, s[36:39], s60 offen lds\n"
"s_add_u32 m0, s58, 3072\n"
"s_add_u32 s60, s57, 27648\n"
"buffer_load_dwordx4 v112, s[36:39], s60 offen lds\n"
"s_add_u32 m0, s58, 4096\n"
"s_add_u32 s60, s57, 36864\n"
"buffer_load_dwordx4 v112, s[36:39], s60 offen lds\n"
"s_add_u32 m0, s58, 5120\n"
"s_add_u32 s60, s57, 46080\n"
"buffer_load_dwordx4 v112, s[36:39], s60 offen lds\n"
"s_add_u32 m0, s58, 6144\n"
"s_add_u32 s60, s57, 55296\n"
"buffer_load_dwordx4 v112, s[36:39], s60 offen lds\n"
"s_add_u32 m0, s58, 7168\n"
"s_add_u32 s60, s57, 64512\n"
"buffer_load_dwordx4 v112, s[36:39], s60 offen lds\n"
"s_add_u32 m0, s58, 8192\n"
"s_add_u32 s60, s57, 73728\n"
"buffer_load_dwordx4 v112, s[36:39], s60 offen lds\n"
"s_add_u32 m0, s58, 9216\n"
"s_add_u32 s60, s57, 82944\n"
"buffer_load_dwordx4 v112, s[36:39], s60 offen lds\n"
"s_add_u32 m0, s58, 10240\n"
"s_add_u32 s60, s57, 92160\n"
"buffer_load_dwordx4 v112, s[36:39], s60 offen lds\n"
"s_add_u32 m0, s58, 11264\n"
"s_add_u32 s60, s57, 101376\n"
"buffer_load_dwordx4 v112, s[36:39], s60 offen lds\n"
"s_add_u32 m0, s58, 12288\n"
"s_add_u32 s60, s57, 110592\n"
"buffer_load_dwordx4 v112, s[36:39], s60 offen lds\n"
"s_add_u32 m0, s58, 13312\n"
"s_add_u32 s60, s57, 119808\n"
"buffer_load_dwordx4 v112, s[36:39], s60 offen lds\n"
"s_add_u32 m0, s58, 14336\n"
"s_add_u32 s60, s57, 129024\n"
"buffer_load_dwordx4 v112, s[36:39], s60 offen lds\n"
"s_add_u32 m0, s58, 15360\n"
"s_add_u32 s60, s57, 138240\n"
"buffer_load_dwordx4 v112, s[36:39], s60 offen lds\n"
"s_add_u32 m0, s58, 16384\n"
"s_add_u32 s60, s57, 147456\n"
"buffer_load_dwordx4 v112, s[36:39], s60 offen lds\n"
"s_add_u32 m0, s58, 17408\n"
"s_add_u32 s60, s57, 156672\n"
"buffer_load_dwordx4 v112, s[36:39], s60 offen lds\n"
"s_add_u32 s59, s56, 0\n"
"buffer_load_dwordx4 a[0:3], v145, s[28:31], s59 offen\n"
"buffer_load_dwordx4 a[4:7], v145, s[28:31], s59 offen offset:1024\n"
"buffer_load_dwordx4 a[8:11], v145, s[28:31], s59 offen offset:2048\n"
"buffer_load_dwordx4 a[12:15], v145, s[28:31], s59 offen offset:3072\n"
"s_add_u32 s59, s56, 4096\n"
"buffer_load_dwordx4 a[16:19], v145, s[28:31], s59 offen\n"
"buffer_load_dwordx4 a[20:23], v145, s[28:31], s59 offen offset:1024\n"
"buffer_load_dwordx4 a[24:27], v145, s[28:31], s59 offen offset:2048\n"
"buffer_load_dwordx4 a[28:31], v145, s[28:31], s59 offen offset:3072\n"
"s_add_u32 s59, s56, 8192\n"
"buffer_load_dwordx4 a[32:35], v145, s[28:31], s59 offen\n"
"buffer_load_dwordx4 a[36:39], v145, s[28:31], s59 offen offset:1024\n"
"buffer_load_dwordx4 a[40:43], v145, s[28:31], s59 offen offset:2048\n"
"buffer_load_dwordx4 a[44:47], v145, s[28:31], s59 offen offset:3072\n"
"s_add_u32 s59, s56, 12288\n"
"buffer_load_dwordx4 a[48:51], v145, s[28:31], s59 offen\n"
"buffer_load_dwordx4 a[52:55], v145, s[28:31], s59 offen offset:1024\n"
"s_mov_b32 s62, s56\n"
"s_mov_b32 s40, s16\n"
"s_and_b32 s41, s17, 0xffff\n"
"s_mov_b32 s42, 2359296\n"
"s_mov_b32 s43, 0x20000\n"
"s_mov_b32 s44, s24\n"
"s_and_b32 s45, s25, 0xffff\n"
"s_mov_b32 s46, 2359296\n"
"s_mov_b32 s47, 0x20000\n"
"s_mov_b32 s48, s20\n"
"s_and_b32 s49, s21, 0xffff\n"
"s_mov_b32 s50, 1152\n"
"s_mov_b32 s51, 0x20000\n"
"v_lshrrev_b32 v115, 5, v144\n"
"v_lshrrev_b32_e64 v116, 2, v144\n"
"v_and_b32 v116, 3, v116\n"
"v_lshrrev_b32 v117, 4, v144\n"
"v_and_b32 v117, 1, v117\n"
"v_and_b32 v118, 3, v144\n"
"v_lshl_add_u32 v119, v115, 3, v116\n"
"v_lshlrev_b32_e64 v119, 7, v119\n"
"v_lshl_add_u32 v119, v117, 5, v119\n"
"v_lshl_add_u32 v119, v118, 3, v119\n"
"v_lshrrev_b32_e64 v120, 1, v116\n"
"v_lshlrev_b32 v120, 6, v120\n"
"v_add_u32 v146, v119, v120\n"
"v_xor_b32 v120, 64, v120\n"
"v_add_u32 v147, v119, v120\n"
"v_and_b32 v150, 31, v144\n"
"s_lshl_b32 s56, s53, 5\n"
"v_add_u32 v150, s56, v150\n"
"v_lshlrev_b32 v149, 3, v115\n"
"s_lshl_b32 s56, s54, 7\n"
"v_add_u32 v149, s56, v149\n"
"v_mul_u32_u24 v148, 1152, v150\n"
"v_add_u32_e64 v148, v148, v149\n"
"v_lshlrev_b32 v150, 2, v150\n"
"s_lshr_b32 s56, s53, 2\n"
"s_mul_i32 s56, s56, 72\n"
"s_and_b32 s57, s53, 3\n"
"s_mul_i32 s57, s57, 18\n"
"s_add_u32 s56, s56, s57\n"
"s_lshl_b32 s57, s54, 1\n"
"s_add_u32 s56, s56, s57\n"
"s_lshl_b32 s56, s56, 12\n"
"s_add_u32 s61, s56, 47464448\n"
"s_waitcnt vmcnt(14)\n"
"s_barrier\n"
"ds_read_b64_tr_b16 v[64:65], v146\n"
"ds_read_b64_tr_b16 v[66:67], v146 offset:512\n"
"ds_read_b64_tr_b16 v[68:69], v146 offset:2048\n"
"ds_read_b64_tr_b16 v[70:71], v146 offset:2560\n"
"ds_read_b64_tr_b16 v[72:73], v146 offset:4096\n"
"ds_read_b64_tr_b16 v[74:75], v146 offset:4608\n"
"s_waitcnt lgkmcnt(4)\n"
"s_waitcnt vmcnt(13)\n"
"v_mfma_f32_32x32x16_bf16 v[32:47], v[64:67], a[0:3], 0\n"
"ds_read_b64_tr_b16 v[76:77], v146 offset:6144\n"
"ds_read_b64_tr_b16 v[78:79], v146 offset:6656\n"
"s_add_u32 s59, s62, 12288\n"
"buffer_load_dwordx4 a[56:59], v145, s[28:31], s59 offen offset:2048\n"
"s_waitcnt lgkmcnt(4)\n"
"s_waitcnt vmcnt(13)\n"
"v_mfma_f32_32x32x16_bf16 v[32:47], v[68:71], a[4:7], v[32:47]\n"
"ds_read_b64_tr_b16 v[64:65], v146 offset:8192\n"
"ds_read_b64_tr_b16 v[66:67], v146 offset:8704\n"
"s_add_u32 s59, s62, 12288\n"
"buffer_load_dwordx4 a[60:63], v145, s[28:31], s59 offen offset:3072\n"
"s_waitcnt lgkmcnt(4)\n"
"s_waitcnt vmcnt(13)\n"
"v_mfma_f32_32x32x16_bf16 v[32:47], v[72:75], a[8:11], v[32:47]\n"
"ds_read_b64_tr_b16 v[68:69], v146 offset:10240\n"
"ds_read_b64_tr_b16 v[70:71], v146 offset:10752\n"
"s_add_u32 s59, s62, 16384\n"
"buffer_load_dwordx4 a[64:67], v145, s[28:31], s59 offen\n"
"s_waitcnt lgkmcnt(4)\n"
"s_waitcnt vmcnt(13)\n"
"v_mfma_f32_32x32x16_bf16 v[32:47], v[76:79], a[12:15], v[32:47]\n"
"ds_read_b64_tr_b16 v[72:73], v146 offset:12288\n"
"ds_read_b64_tr_b16 v[74:75], v146 offset:12800\n"
"s_add_u32 s59, s62, 16384\n"
"buffer_load_dwordx4 a[68:71], v145, s[28:31], s59 offen offset:1024\n"
"s_waitcnt lgkmcnt(4)\n"
"s_waitcnt vmcnt(13)\n"
"v_mfma_f32_32x32x16_bf16 v[32:47], v[64:67], a[16:19], v[32:47]\n"
"ds_read_b64_tr_b16 v[76:77], v146 offset:14336\n"
"ds_read_b64_tr_b16 v[78:79], v146 offset:14848\n"
"s_add_u32 s59, s62, 16384\n"
"buffer_load_dwordx4 a[72:75], v145, s[28:31], s59 offen offset:2048\n"
"s_waitcnt lgkmcnt(4)\n"
"s_waitcnt vmcnt(13)\n"
"v_mfma_f32_32x32x16_bf16 v[32:47], v[68:71], a[20:23], v[32:47]\n"
"ds_read_b64_tr_b16 v[64:65], v146 offset:16384\n"
"ds_read_b64_tr_b16 v[66:67], v146 offset:16896\n"
"s_add_u32 s59, s62, 16384\n"
"buffer_load_dwordx4 a[76:79], v145, s[28:31], s59 offen offset:3072\n"
"s_waitcnt lgkmcnt(4)\n"
"s_waitcnt vmcnt(13)\n"
"v_mfma_f32_32x32x16_bf16 v[32:47], v[72:75], a[24:27], v[32:47]\n"
"ds_read_b64_tr_b16 v[68:69], v146 offset:18432\n"
"ds_read_b64_tr_b16 v[70:71], v146 offset:18944\n"
"s_add_u32 s59, s62, 20480\n"
"buffer_load_dwordx4 a[80:83], v145, s[28:31], s59 offen\n"
"s_waitcnt lgkmcnt(4)\n"
"s_waitcnt vmcnt(13)\n"
"v_mfma_f32_32x32x16_bf16 v[32:47], v[76:79], a[28:31], v[32:47]\n"
"ds_read_b64_tr_b16 v[72:73], v146 offset:20480\n"
"ds_read_b64_tr_b16 v[74:75], v146 offset:20992\n"
"s_add_u32 s59, s62, 20480\n"
"buffer_load_dwordx4 a[84:87], v145, s[28:31], s59 offen offset:1024\n"
"s_waitcnt lgkmcnt(4)\n"
"s_waitcnt vmcnt(13)\n"
"v_mfma_f32_32x32x16_bf16 v[32:47], v[64:67], a[32:35], v[32:47]\n"
"ds_read_b64_tr_b16 v[76:77], v146 offset:22528\n"
"ds_read_b64_tr_b16 v[78:79], v146 offset:23040\n"
"s_add_u32 s59, s62, 20480\n"
"buffer_load_dwordx4 a[88:91], v145, s[28:31], s59 offen offset:2048\n"
"s_waitcnt lgkmcnt(4)\n"
"s_waitcnt vmcnt(13)\n"
"v_mfma_f32_32x32x16_bf16 v[32:47], v[68:71], a[36:39], v[32:47]\n"
"ds_read_b64_tr_b16 v[64:65], v146 offset:24576\n"
"ds_read_b64_tr_b16 v[66:67], v146 offset:25088\n"
"s_add_u32 s59, s62, 20480\n"
"buffer_load_dwordx4 a[92:95], v145, s[28:31], s59 offen offset:3072\n"
"s_waitcnt lgkmcnt(4)\n"
"s_waitcnt vmcnt(13)\n"
"v_mfma_f32_32x32x16_bf16 v[32:47], v[72:75], a[40:43], v[32:47]\n"
"ds_read_b64_tr_b16 v[68:69], v146 offset:26624\n"
"ds_read_b64_tr_b16 v[70:71], v146 offset:27136\n"
"s_add_u32 s59, s62, 24576\n"
"buffer_load_dwordx4 a[96:99], v145, s[28:31], s59 offen\n"
"s_waitcnt lgkmcnt(4)\n"
"s_waitcnt vmcnt(13)\n"
"v_mfma_f32_32x32x16_bf16 v[32:47], v[76:79], a[44:47], v[32:47]\n"
"ds_read_b64_tr_b16 v[72:73], v146 offset:28672\n"
"ds_read_b64_tr_b16 v[74:75], v146 offset:29184\n"
"s_add_u32 s59, s62, 24576\n"
"buffer_load_dwordx4 a[100:103], v145, s[28:31], s59 offen offset:1024\n"
"s_waitcnt lgkmcnt(4)\n"
"s_waitcnt vmcnt(13)\n"
"v_mfma_f32_32x32x16_bf16 v[32:47], v[64:67], a[48:51], v[32:47]\n"
"ds_read_b64_tr_b16 v[76:77], v146 offset:30720\n"
"ds_read_b64_tr_b16 v[78:79], v146 offset:31232\n"
"s_add_u32 s59, s62, 24576\n"
"buffer_load_dwordx4 a[104:107], v145, s[28:31], s59 offen offset:2048\n"
"s_waitcnt lgkmcnt(4)\n"
"s_waitcnt vmcnt(13)\n"
"v_mfma_f32_32x32x16_bf16 v[32:47], v[68:71], a[52:55], v[32:47]\n"
"v_add_u32 v146, 32768, v146\n"
"ds_read_b64_tr_b16 v[64:65], v146\n"
"ds_read_b64_tr_b16 v[66:67], v146 offset:512\n"
"s_add_u32 s59, s62, 24576\n"
"buffer_load_dwordx4 a[108:111], v145, s[28:31], s59 offen offset:3072\n"
"s_waitcnt lgkmcnt(4)\n"
"s_waitcnt vmcnt(13)\n"
"v_mfma_f32_32x32x16_bf16 v[32:47], v[72:75], a[56:59], v[32:47]\n"
"ds_read_b64_tr_b16 v[68:69], v146 offset:2048\n"
"ds_read_b64_tr_b16 v[70:71], v146 offset:2560\n"
"s_add_u32 s59, s62, 28672\n"
"buffer_load_dwordx4 a[112:115], v145, s[28:31], s59 offen\n"
"s_waitcnt lgkmcnt(4)\n"
"s_waitcnt vmcnt(13)\n"
"v_mfma_f32_32x32x16_bf16 v[32:47], v[76:79], a[60:63], v[32:47]\n"
"ds_read_b64_tr_b16 v[72:73], v146 offset:4096\n"
"ds_read_b64_tr_b16 v[74:75], v146 offset:4608\n"
"s_add_u32 s59, s62, 28672\n"
"buffer_load_dwordx4 a[116:119], v145, s[28:31], s59 offen offset:1024\n"
"s_waitcnt lgkmcnt(4)\n"
"s_waitcnt vmcnt(13)\n"
"v_mfma_f32_32x32x16_bf16 v[32:47], v[64:67], a[64:67], v[32:47]\n"
"ds_read_b64_tr_b16 v[76:77], v146 offset:6144\n"
"ds_read_b64_tr_b16 v[78:79], v146 offset:6656\n"
"s_add_u32 s59, s62, 28672\n"
"buffer_load_dwordx4 a[120:123], v145, s[28:31], s59 offen offset:2048\n"
"s_waitcnt lgkmcnt(4)\n"
"s_waitcnt vmcnt(13)\n"
"v_mfma_f32_32x32x16_bf16 v[32:47], v[68:71], a[68:71], v[32:47]\n"
"ds_read_b64_tr_b16 v[64:65], v146 offset:8192\n"
"ds_read_b64_tr_b16 v[66:67], v146 offset:8704\n"
"s_add_u32 s59, s62, 28672\n"
"buffer_load_dwordx4 a[124:127], v145, s[28:31], s59 offen offset:3072\n"
"s_waitcnt lgkmcnt(4)\n"
"s_waitcnt vmcnt(13)\n"
"v_mfma_f32_32x32x16_bf16 v[32:47], v[72:75], a[72:75], v[32:47]\n"
"ds_read_b64_tr_b16 v[68:69], v146 offset:10240\n"
"ds_read_b64_tr_b16 v[70:71], v146 offset:10752\n"
"s_add_u32 s59, s62, 32768\n"
"buffer_load_dwordx4 a[128:131], v145, s[28:31], s59 offen\n"
"s_waitcnt lgkmcnt(4)\n"
"s_waitcnt vmcnt(13)\n"
"v_mfma_f32_32x32x16_bf16 v[32:47], v[76:79], a[76:79], v[32:47]\n"
"ds_read_b64_tr_b16 v[72:73], v146 offset:12288\n"
"ds_read_b64_tr_b16 v[74:75], v146 offset:12800\n"
"s_add_u32 s59, s62, 32768\n"
"buffer_load_dwordx4 a[132:135], v145, s[28:31], s59 offen offset:1024\n"
"s_waitcnt lgkmcnt(4)\n"
"s_waitcnt vmcnt(13)\n"
"v_mfma_f32_32x32x16_bf16 v[32:47], v[64:67], a[80:83], v[32:47]\n"
"ds_read_b64_tr_b16 v[76:77], v146 offset:14336\n"
"ds_read_b64_tr_b16 v[78:79], v146 offset:14848\n"
"s_add_u32 s59, s62, 32768\n"
"buffer_load_dwordx4 a[136:139], v145, s[28:31], s59 offen offset:2048\n"
"s_waitcnt lgkmcnt(4)\n"
"s_waitcnt vmcnt(13)\n"
"v_mfma_f32_32x32x16_bf16 v[32:47], v[68:71], a[84:87], v[32:47]\n"
"ds_read_b64_tr_b16 v[64:65], v146 offset:16384\n"
"ds_read_b64_tr_b16 v[66:67], v146 offset:16896\n"
"s_add_u32 s59, s62, 32768\n"
"buffer_load_dwordx4 a[140:143], v145, s[28:31], s59 offen offset:3072\n"
"s_add_u32 s60, s61, 0\n"
"buffer_load_dwordx4 v[0:3], v145, s[28:31], s60 offen\n"
"s_waitcnt lgkmcnt(4)\n"
"s_waitcnt vmcnt(14)\n"
"v_mfma_f32_32x32x16_bf16 v[32:47], v[72:75], a[88:91], v[32:47]\n"
"ds_read_b64_tr_b16 v[68:69], v146 offset:18432\n"
"ds_read_b64_tr_b16 v[70:71], v146 offset:18944\n"
"s_add_u32 s60, s61, 0\n"
"buffer_load_dwordx4 v[4:7], v145, s[28:31], s60 offen offset:1024\n"
"s_waitcnt lgkmcnt(4)\n"
"s_waitcnt vmcnt(14)\n"
"v_mfma_f32_32x32x16_bf16 v[32:47], v[76:79], a[92:95], v[32:47]\n"
"ds_read_b64_tr_b16 v[72:73], v146 offset:20480\n"
"ds_read_b64_tr_b16 v[74:75], v146 offset:20992\n"
"s_add_u32 s60, s61, 0\n"
"buffer_load_dwordx4 v[8:11], v145, s[28:31], s60 offen offset:2048\n"
"s_add_u32 s60, s61, 0\n"
"buffer_load_dwordx4 v[12:15], v145, s[28:31], s60 offen offset:3072\n"
"s_waitcnt lgkmcnt(4)\n"
"s_waitcnt vmcnt(15)\n"
"v_mfma_f32_32x32x16_bf16 v[32:47], v[64:67], a[96:99], v[32:47]\n"
"ds_read_b64_tr_b16 v[76:77], v146 offset:22528\n"
"ds_read_b64_tr_b16 v[78:79], v146 offset:23040\n"
"buffer_load_dwordx2 v[80:81], v148, s[40:43], 0 offen\n"
"s_waitcnt lgkmcnt(4)\n"
"s_waitcnt vmcnt(15)\n"
"v_mfma_f32_32x32x16_bf16 v[32:47], v[68:71], a[100:103], v[32:47]\n"
"ds_read_b64_tr_b16 v[64:65], v146 offset:24576\n"
"ds_read_b64_tr_b16 v[66:67], v146 offset:25088\n"
"buffer_load_dwordx2 v[96:97], v149, s[48:51], 0 offen\n"
"buffer_load_dwordx2 v[82:83], v148, s[40:43], 0 offen offset:16\n"
"s_waitcnt lgkmcnt(4)\n"
"s_waitcnt vmcnt(16)\n"
"v_mfma_f32_32x32x16_bf16 v[32:47], v[72:75], a[104:107], v[32:47]\n"
"ds_read_b64_tr_b16 v[68:69], v146 offset:26624\n"
"ds_read_b64_tr_b16 v[70:71], v146 offset:27136\n"
"buffer_load_dwordx2 v[98:99], v149, s[48:51], 0 offen offset:16\n"
"s_waitcnt lgkmcnt(4)\n"
"s_waitcnt vmcnt(16)\n"
"v_mfma_f32_32x32x16_bf16 v[32:47], v[76:79], a[108:111], v[32:47]\n"
"ds_read_b64_tr_b16 v[72:73], v146 offset:28672\n"
"ds_read_b64_tr_b16 v[74:75], v146 offset:29184\n"
"buffer_load_dwordx2 v[84:85], v148, s[40:43], 0 offen offset:32\n"
"buffer_load_dwordx2 v[100:101], v149, s[48:51], 0 offen offset:32\n"
"s_waitcnt lgkmcnt(4)\n"
"s_waitcnt vmcnt(17)\n"
"v_mfma_f32_32x32x16_bf16 v[32:47], v[64:67], a[112:115], v[32:47]\n"
"ds_read_b64_tr_b16 v[76:77], v146 offset:30720\n"
"ds_read_b64_tr_b16 v[78:79], v146 offset:31232\n"
"buffer_load_dwordx2 v[86:87], v148, s[40:43], 0 offen offset:48\n"
"s_waitcnt lgkmcnt(4)\n"
"s_waitcnt vmcnt(17)\n"
"v_mfma_f32_32x32x16_bf16 v[32:47], v[68:71], a[116:119], v[32:47]\n"
"v_add_u32 v146, 32768, v146\n"
"ds_read_b64_tr_b16 v[64:65], v146\n"
"ds_read_b64_tr_b16 v[66:67], v146 offset:512\n"
"buffer_load_dwordx2 v[102:103], v149, s[48:51], 0 offen offset:48\n"
"s_mov_b32 s60, 4096\n"
"buffer_load_dword v150, v150, s[28:31], s60 offen\n"
"s_waitcnt lgkmcnt(4)\n"
"s_waitcnt vmcnt(18)\n"
"v_mfma_f32_32x32x16_bf16 v[32:47], v[72:75], a[120:123], v[32:47]\n"
"ds_read_b64_tr_b16 v[68:69], v146 offset:2048\n"
"ds_read_b64_tr_b16 v[70:71], v146 offset:2560\n"
"s_add_u32 s60, s61, 4096\n"
"buffer_load_dwordx4 v[16:19], v145, s[28:31], s60 offen\n"
"s_waitcnt lgkmcnt(4)\n"
"s_waitcnt vmcnt(18)\n"
"v_mfma_f32_32x32x16_bf16 v[32:47], v[76:79], a[124:127], v[32:47]\n"
"ds_read_b64_tr_b16 v[72:73], v146 offset:4096\n"
"ds_read_b64_tr_b16 v[74:75], v146 offset:4608\n"
"s_add_u32 s60, s61, 4096\n"
"buffer_load_dwordx4 v[20:23], v145, s[28:31], s60 offen offset:1024\n"
"s_add_u32 s60, s61, 4096\n"
"buffer_load_dwordx4 v[24:27], v145, s[28:31], s60 offen offset:2048\n"
"s_waitcnt lgkmcnt(4)\n"
"s_waitcnt vmcnt(19)\n"
"v_mfma_f32_32x32x16_bf16 v[32:47], v[64:67], a[128:131], v[32:47]\n"
"ds_read_b64_tr_b16 v[76:77], v146 offset:6144\n"
"ds_read_b64_tr_b16 v[78:79], v146 offset:6656\n"
"s_add_u32 s60, s61, 4096\n"
"buffer_load_dwordx4 v[28:31], v145, s[28:31], s60 offen offset:3072\n"
"s_waitcnt lgkmcnt(4)\n"
"s_waitcnt vmcnt(19)\n"
"v_mfma_f32_32x32x16_bf16 v[32:47], v[68:71], a[132:135], v[32:47]\n"
"ds_read_b64_tr_b16 v[64:65], v147\n"
"ds_read_b64_tr_b16 v[66:67], v147 offset:512\n"
"buffer_load_dwordx2 v[88:89], v148, s[40:43], 0 offen offset:64\n"
"buffer_load_dwordx2 v[104:105], v149, s[48:51], 0 offen offset:64\n"
"s_waitcnt lgkmcnt(4)\n"
"s_waitcnt vmcnt(20)\n"
"v_mfma_f32_32x32x16_bf16 v[32:47], v[72:75], a[136:139], v[32:47]\n"
"ds_read_b64_tr_b16 v[68:69], v147 offset:2048\n"
"ds_read_b64_tr_b16 v[70:71], v147 offset:2560\n"
"buffer_load_dwordx2 v[90:91], v148, s[40:43], 0 offen offset:80\n"
"s_waitcnt lgkmcnt(4)\n"
"s_waitcnt vmcnt(20)\n"
"v_mfma_f32_32x32x16_bf16 v[32:47], v[76:79], a[140:143], v[32:47]\n"
"ds_read_b64_tr_b16 v[72:73], v147 offset:4096\n"
"ds_read_b64_tr_b16 v[74:75], v147 offset:4608\n"
"buffer_load_dwordx2 v[106:107], v149, s[48:51], 0 offen offset:80\n"
"buffer_load_dwordx2 v[92:93], v148, s[40:43], 0 offen offset:96\n"
"s_mov_b32 s56, 0xbfb8aa3b\n"
"s_waitcnt lgkmcnt(4)\n"
"v_mfma_f32_32x32x16_bf16 v[48:63], v[64:67], a[0:3], 0\n"
"ds_read_b64_tr_b16 v[76:77], v147 offset:6144\n"
"ds_read_b64_tr_b16 v[78:79], v147 offset:6656\n"
"buffer_load_dwordx2 v[108:109], v149, s[48:51], 0 offen offset:96\n"
"s_waitcnt lgkmcnt(4)\n"
"v_mfma_f32_32x32x16_bf16 v[48:63], v[68:71], a[4:7], v[48:63]\n"
"ds_read_b64_tr_b16 v[64:65], v147 offset:8192\n"
"ds_read_b64_tr_b16 v[66:67], v147 offset:8704\n"
"buffer_load_dwordx2 v[94:95], v148, s[40:43], 0 offen offset:112\n"
"buffer_load_dwordx2 v[110:111], v149, s[48:51], 0 offen offset:112\n"
"s_waitcnt lgkmcnt(4)\n"
"v_mfma_f32_32x32x16_bf16 v[48:63], v[72:75], a[8:11], v[48:63]\n"
"ds_read_b64_tr_b16 v[68:69], v147 offset:10240\n"
"ds_read_b64_tr_b16 v[70:71], v147 offset:10752\n"
"s_waitcnt lgkmcnt(4)\n"
"v_mfma_f32_32x32x16_bf16 v[48:63], v[76:79], a[12:15], v[48:63]\n"
"ds_read_b64_tr_b16 v[72:73], v147 offset:12288\n"
"ds_read_b64_tr_b16 v[74:75], v147 offset:12800\n"
"s_waitcnt vmcnt(19)\n"
"v_lshlrev_b32 v132, 16, v96\n"
"v_lshlrev_b32_e64 v128, 16, v80\n"
"v_and_b32 v133, 0xffff0000, v96\n"
"v_and_b32 v129, 0xffff0000, v80\n"
"v_lshlrev_b32 v134, 16, v97\n"
"s_waitcnt lgkmcnt(4)\n"
"v_mfma_f32_32x32x16_bf16 v[48:63], v[64:67], a[16:19], v[48:63]\n"
"ds_read_b64_tr_b16 v[76:77], v147 offset:14336\n"
"ds_read_b64_tr_b16 v[78:79], v147 offset:14848\n"
"v_lshlrev_b32_e64 v130, 16, v81\n"
"v_and_b32 v135, 0xffff0000, v97\n"
"v_and_b32 v131, 0xffff0000, v81\n"
"s_waitcnt vmcnt(12)\n"
"v_fma_f32 v112, v0, v150, v132\n"
"v_fma_f32 v113, v1, v150, v133\n"
"s_waitcnt lgkmcnt(4)\n"
"v_mfma_f32_32x32x16_bf16 v[48:63], v[68:71], a[20:23], v[48:63]\n"
"ds_read_b64_tr_b16 v[64:65], v147 offset:16384\n"
"ds_read_b64_tr_b16 v[66:67], v147 offset:16896\n"
"v_fma_f32 v114, v2, v150, v134\n"
"v_fma_f32 v115, v3, v150, v135\n"
"v_mul_f32 v116, s56, v112\n"
"v_mul_f32 v117, s56, v113\n"
"v_mul_f32 v118, s56, v114\n"
"s_waitcnt lgkmcnt(4)\n"
"v_mfma_f32_32x32x16_bf16 v[48:63], v[72:75], a[24:27], v[48:63]\n"
"ds_read_b64_tr_b16 v[68:69], v147 offset:18432\n"
"ds_read_b64_tr_b16 v[70:71], v147 offset:18944\n"
"v_mul_f32 v119, s56, v115\n"
"v_exp_f32 v116, v116\n"
"v_exp_f32 v117, v117\n"
"v_exp_f32 v118, v118\n"
"v_exp_f32 v119, v119\n"
"s_waitcnt lgkmcnt(4)\n"
"v_mfma_f32_32x32x16_bf16 v[48:63], v[76:79], a[28:31], v[48:63]\n"
"ds_read_b64_tr_b16 v[72:73], v147 offset:20480\n"
"ds_read_b64_tr_b16 v[74:75], v147 offset:20992\n"
"v_add_f32 v116, 1.0, v116\n"
"v_add_f32 v117, 1.0, v117\n"
"v_add_f32 v118, 1.0, v118\n"
"v_add_f32 v119, 1.0, v119\n"
"v_rcp_f32 v116, v116\n"
"s_waitcnt lgkmcnt(4)\n"
"v_mfma_f32_32x32x16_bf16 v[48:63], v[64:67], a[32:35], v[48:63]\n"
"ds_read_b64_tr_b16 v[76:77], v147 offset:22528\n"
"ds_read_b64_tr_b16 v[78:79], v147 offset:23040\n"
"v_rcp_f32 v117, v117\n"
"v_rcp_f32 v118, v118\n"
"v_rcp_f32 v119, v119\n"
"v_mul_f32 v112, v112, v116\n"
"v_mul_f32 v113, v113, v117\n"
"s_waitcnt lgkmcnt(4)\n"
"v_mfma_f32_32x32x16_bf16 v[48:63], v[68:71], a[36:39], v[48:63]\n"
"ds_read_b64_tr_b16 v[64:65], v147 offset:24576\n"
"ds_read_b64_tr_b16 v[66:67], v147 offset:25088\n"
"v_mul_f32 v114, v114, v118\n"
"s_waitcnt lgkmcnt(4)\n"
"v_mul_f32_e64 v115, v115, v119\n"
"v_fma_f32 v128, v32, v112, v128\n"
"v_fma_f32 v129, v33, v113, v129\n"
"v_fma_f32 v130, v34, v114, v130\n"
"v_mfma_f32_32x32x16_bf16 v[48:63], v[72:75], a[40:43], v[48:63]\n"
"ds_read_b64_tr_b16 v[68:69], v147 offset:26624\n"
"ds_read_b64_tr_b16 v[70:71], v147 offset:27136\n"
"v_fma_f32 v131, v35, v115, v131\n"
"v_cvt_pk_bf16_f32 v136, v128, v129\n"
"v_cvt_pk_bf16_f32 v137, v130, v131\n"
"buffer_store_dwordx2 v[136:137], v148, s[44:47], 0 offen\n"
"v_lshlrev_b32 v132, 16, v98\n"
"s_waitcnt lgkmcnt(4)\n"
"v_mfma_f32_32x32x16_bf16 v[48:63], v[76:79], a[44:47], v[48:63]\n"
"ds_read_b64_tr_b16 v[72:73], v147 offset:28672\n"
"ds_read_b64_tr_b16 v[74:75], v147 offset:29184\n"
"v_lshlrev_b32_e64 v128, 16, v82\n"
"v_and_b32 v133, 0xffff0000, v98\n"
"v_and_b32 v129, 0xffff0000, v82\n"
"v_lshlrev_b32_e64 v134, 16, v99\n"
"v_lshlrev_b32 v130, 16, v83\n"
"s_waitcnt lgkmcnt(4)\n"
"v_mfma_f32_32x32x16_bf16 v[48:63], v[64:67], a[48:51], v[48:63]\n"
"ds_read_b64_tr_b16 v[76:77], v147 offset:30720\n"
"ds_read_b64_tr_b16 v[78:79], v147 offset:31232\n"
"v_and_b32 v135, 0xffff0000, v99\n"
"v_and_b32 v131, 0xffff0000, v83\n"
"v_fma_f32 v112, v4, v150, v132\n"
"v_fma_f32 v113, v5, v150, v133\n"
"v_fma_f32 v114, v6, v150, v134\n"
"s_waitcnt lgkmcnt(4)\n"
"v_mfma_f32_32x32x16_bf16 v[48:63], v[68:71], a[52:55], v[48:63]\n"
"v_add_u32 v147, 32768, v147\n"
"ds_read_b64_tr_b16 v[64:65], v147\n"
"ds_read_b64_tr_b16 v[66:67], v147 offset:512\n"
"v_fma_f32 v115, v7, v150, v135\n"
"v_mul_f32 v116, s56, v112\n"
"v_mul_f32 v117, s56, v113\n"
"v_mul_f32 v118, s56, v114\n"
"v_mul_f32 v119, s56, v115\n"
"s_waitcnt lgkmcnt(4)\n"
"v_mfma_f32_32x32x16_bf16 v[48:63], v[72:75], a[56:59], v[48:63]\n"
"ds_read_b64_tr_b16 v[68:69], v147 offset:2048\n"
"ds_read_b64_tr_b16 v[70:71], v147 offset:2560\n"
"v_exp_f32 v116, v116\n"
"v_exp_f32 v117, v117\n"
"v_exp_f32 v118, v118\n"
"v_exp_f32 v119, v119\n"
"v_add_f32 v116, 1.0, v116\n"
"s_waitcnt lgkmcnt(4)\n"
"v_mfma_f32_32x32x16_bf16 v[48:63], v[76:79], a[60:63], v[48:63]\n"
"ds_read_b64_tr_b16 v[72:73], v147 offset:4096\n"
"ds_read_b64_tr_b16 v[74:75], v147 offset:4608\n"
"v_add_f32 v117, 1.0, v117\n"
"v_add_f32 v118, 1.0, v118\n"
"v_add_f32 v119, 1.0, v119\n"
"v_rcp_f32 v116, v116\n"
"v_rcp_f32 v117, v117\n"
"s_waitcnt lgkmcnt(4)\n"
"v_mfma_f32_32x32x16_bf16 v[48:63], v[64:67], a[64:67], v[48:63]\n"
"ds_read_b64_tr_b16 v[76:77], v147 offset:6144\n"
"ds_read_b64_tr_b16 v[78:79], v147 offset:6656\n"
"v_rcp_f32 v118, v118\n"
"v_rcp_f32 v119, v119\n"
"v_mul_f32 v112, v112, v116\n"
"v_mul_f32 v113, v113, v117\n"
"v_mul_f32 v114, v114, v118\n"
"s_waitcnt lgkmcnt(4)\n"
"v_mfma_f32_32x32x16_bf16 v[48:63], v[68:71], a[68:71], v[48:63]\n"
"ds_read_b64_tr_b16 v[64:65], v147 offset:8192\n"
"ds_read_b64_tr_b16 v[66:67], v147 offset:8704\n"
"v_mul_f32_e64 v115, v115, v119\n"
"v_fma_f32 v128, v36, v112, v128\n"
"v_fma_f32 v129, v37, v113, v129\n"
"v_fma_f32 v130, v38, v114, v130\n"
"v_fma_f32 v131, v39, v115, v131\n"
"s_waitcnt lgkmcnt(4)\n"
"v_mfma_f32_32x32x16_bf16 v[48:63], v[72:75], a[72:75], v[48:63]\n"
"ds_read_b64_tr_b16 v[68:69], v147 offset:10240\n"
"ds_read_b64_tr_b16 v[70:71], v147 offset:10752\n"
"v_cvt_pk_bf16_f32 v138, v128, v129\n"
"v_cvt_pk_bf16_f32 v139, v130, v131\n"
"buffer_store_dwordx2 v[138:139], v148, s[44:47], 0 offen offset:16\n"
"v_lshlrev_b32 v132, 16, v100\n"
"v_lshlrev_b32 v128, 16, v84\n"
"s_waitcnt lgkmcnt(4)\n"
"v_mfma_f32_32x32x16_bf16 v[48:63], v[76:79], a[76:79], v[48:63]\n"
"ds_read_b64_tr_b16 v[72:73], v147 offset:12288\n"
"ds_read_b64_tr_b16 v[74:75], v147 offset:12800\n"
"v_and_b32 v133, 0xffff0000, v100\n"
"v_and_b32 v129, 0xffff0000, v84\n"
"v_lshlrev_b32 v134, 16, v101\n"
"s_waitcnt lgkmcnt(4)\n"
"v_lshlrev_b32_e64 v130, 16, v85\n"
"v_and_b32 v135, 0xffff0000, v101\n"
"v_mfma_f32_32x32x16_bf16 v[48:63], v[64:67], a[80:83], v[48:63]\n"
"ds_read_b64_tr_b16 v[76:77], v147 offset:14336\n"
"ds_read_b64_tr_b16 v[78:79], v147 offset:14848\n"
"v_and_b32 v131, 0xffff0000, v85\n"
"v_fma_f32 v112, v8, v150, v132\n"
"v_fma_f32 v113, v9, v150, v133\n"
"v_fma_f32 v114, v10, v150, v134\n"
"v_fma_f32 v115, v11, v150, v135\n"
"s_waitcnt lgkmcnt(4)\n"
"v_mfma_f32_32x32x16_bf16 v[48:63], v[68:71], a[84:87], v[48:63]\n"
"ds_read_b64_tr_b16 v[64:65], v147 offset:16384\n"
"ds_read_b64_tr_b16 v[66:67], v147 offset:16896\n"
"v_mul_f32 v116, s56, v112\n"
"v_mul_f32_e64 v117, s56, v113\n"
"v_mul_f32 v118, s56, v114\n"
"v_mul_f32 v119, s56, v115\n"
"v_exp_f32 v116, v116\n"
"s_waitcnt lgkmcnt(4)\n"
"v_mfma_f32_32x32x16_bf16 v[48:63], v[72:75], a[88:91], v[48:63]\n"
"ds_read_b64_tr_b16 v[68:69], v147 offset:18432\n"
"ds_read_b64_tr_b16 v[70:71], v147 offset:18944\n"
"v_exp_f32 v117, v117\n"
"v_exp_f32 v118, v118\n"
"v_exp_f32 v119, v119\n"
"v_add_f32 v116, 1.0, v116\n"
"v_add_f32 v117, 1.0, v117\n"
"s_waitcnt lgkmcnt(4)\n"
"v_mfma_f32_32x32x16_bf16 v[48:63], v[76:79], a[92:95], v[48:63]\n"
"ds_read_b64_tr_b16 v[72:73], v147 offset:20480\n"
"ds_read_b64_tr_b16 v[74:75], v147 offset:20992\n"
"v_add_f32 v118, 1.0, v118\n"
"v_add_f32 v119, 1.0, v119\n"
"v_rcp_f32 v116, v116\n"
"v_rcp_f32 v117, v117\n"
"v_rcp_f32 v118, v118\n"
"s_waitcnt lgkmcnt(4)\n"
"v_mfma_f32_32x32x16_bf16 v[48:63], v[64:67], a[96:99], v[48:63]\n"
"ds_read_b64_tr_b16 v[76:77], v147 offset:22528\n"
"ds_read_b64_tr_b16 v[78:79], v147 offset:23040\n"
"v_rcp_f32 v119, v119\n"
"v_mul_f32 v112, v112, v116\n"
"v_mul_f32 v113, v113, v117\n"
"v_mul_f32 v114, v114, v118\n"
"v_mul_f32 v115, v115, v119\n"
"s_waitcnt lgkmcnt(4)\n"
"v_mfma_f32_32x32x16_bf16 v[48:63], v[68:71], a[100:103], v[48:63]\n"
"ds_read_b64_tr_b16 v[64:65], v147 offset:24576\n"
"ds_read_b64_tr_b16 v[66:67], v147 offset:25088\n"
"v_fma_f32 v128, v40, v112, v128\n"
"v_fma_f32 v129, v41, v113, v129\n"
"v_fma_f32 v130, v42, v114, v130\n"
"v_fma_f32 v131, v43, v115, v131\n"
"v_cvt_pk_bf16_f32 v140, v128, v129\n"
"s_waitcnt lgkmcnt(4)\n"
"v_mfma_f32_32x32x16_bf16 v[48:63], v[72:75], a[104:107], v[48:63]\n"
"ds_read_b64_tr_b16 v[68:69], v147 offset:26624\n"
"ds_read_b64_tr_b16 v[70:71], v147 offset:27136\n"
"v_cvt_pk_bf16_f32 v141, v130, v131\n"
"buffer_store_dwordx2 v[140:141], v148, s[44:47], 0 offen offset:32\n"
"s_waitcnt lgkmcnt(4)\n"
"v_lshlrev_b32_e64 v132, 16, v102\n"
"v_lshlrev_b32_e64 v128, 16, v86\n"
"v_and_b32 v133, 0xffff0000, v102\n"
"v_mfma_f32_32x32x16_bf16 v[48:63], v[76:79], a[108:111], v[48:63]\n"
"ds_read_b64_tr_b16 v[72:73], v147 offset:28672\n"
"ds_read_b64_tr_b16 v[74:75], v147 offset:29184\n"
"v_and_b32 v129, 0xffff0000, v86\n"
"v_lshlrev_b32 v134, 16, v103\n"
"s_waitcnt lgkmcnt(4)\n"
"v_lshlrev_b32_e64 v130, 16, v87\n"
"v_and_b32 v135, 0xffff0000, v103\n"
"v_and_b32 v131, 0xffff0000, v87\n"
"v_mfma_f32_32x32x16_bf16 v[48:63], v[64:67], a[112:115], v[48:63]\n"
"ds_read_b64_tr_b16 v[76:77], v147 offset:30720\n"
"ds_read_b64_tr_b16 v[78:79], v147 offset:31232\n"
"v_fma_f32 v112, v12, v150, v132\n"
"v_fma_f32 v113, v13, v150, v133\n"
"v_fma_f32 v114, v14, v150, v134\n"
"v_fma_f32 v115, v15, v150, v135\n"
"v_mul_f32 v116, s56, v112\n"
"s_waitcnt lgkmcnt(4)\n"
"v_mfma_f32_32x32x16_bf16 v[48:63], v[68:71], a[116:119], v[48:63]\n"
"v_add_u32 v147, 32768, v147\n"
"ds_read_b64_tr_b16 v[64:65], v147\n"
"ds_read_b64_tr_b16 v[66:67], v147 offset:512\n"
"v_mul_f32 v117, s56, v113\n"
"v_mul_f32 v118, s56, v114\n"
"v_mul_f32 v119, s56, v115\n"
"v_exp_f32 v116, v116\n"
"v_exp_f32 v117, v117\n"
"s_waitcnt lgkmcnt(4)\n"
"v_mfma_f32_32x32x16_bf16 v[48:63], v[72:75], a[120:123], v[48:63]\n"
"ds_read_b64_tr_b16 v[68:69], v147 offset:2048\n"
"ds_read_b64_tr_b16 v[70:71], v147 offset:2560\n"
"v_exp_f32 v118, v118\n"
"v_exp_f32 v119, v119\n"
"v_add_f32 v116, 1.0, v116\n"
"v_add_f32 v117, 1.0, v117\n"
"v_add_f32 v118, 1.0, v118\n"
"s_waitcnt lgkmcnt(4)\n"
"v_mfma_f32_32x32x16_bf16 v[48:63], v[76:79], a[124:127], v[48:63]\n"
"ds_read_b64_tr_b16 v[72:73], v147 offset:4096\n"
"ds_read_b64_tr_b16 v[74:75], v147 offset:4608\n"
"v_add_f32 v119, 1.0, v119\n"
"v_rcp_f32 v116, v116\n"
"v_rcp_f32 v117, v117\n"
"v_rcp_f32 v118, v118\n"
"v_rcp_f32 v119, v119\n"
"s_waitcnt lgkmcnt(4)\n"
"v_mfma_f32_32x32x16_bf16 v[48:63], v[64:67], a[128:131], v[48:63]\n"
"ds_read_b64_tr_b16 v[76:77], v147 offset:6144\n"
"ds_read_b64_tr_b16 v[78:79], v147 offset:6656\n"
"v_mul_f32 v112, v112, v116\n"
"v_mul_f32 v113, v113, v117\n"
"v_mul_f32 v114, v114, v118\n"
"s_waitcnt lgkmcnt(4)\n"
"v_mul_f32_e64 v115, v115, v119\n"
"v_fma_f32 v128, v44, v112, v128\n"
"v_mfma_f32_32x32x16_bf16 v[48:63], v[68:71], a[132:135], v[48:63]\n"
"v_fma_f32 v129, v45, v113, v129\n"
"v_fma_f32 v130, v46, v114, v130\n"
"v_fma_f32 v131, v47, v115, v131\n"
"v_cvt_pk_bf16_f32 v142, v128, v129\n"
"v_cvt_pk_bf16_f32 v143, v130, v131\n"
"s_waitcnt lgkmcnt(2)\n"
"v_mfma_f32_32x32x16_bf16 v[48:63], v[72:75], a[136:139], v[48:63]\n"
"buffer_store_dwordx2 v[142:143], v148, s[44:47], 0 offen offset:48\n"
"s_waitcnt lgkmcnt(0)\n"
"v_mfma_f32_32x32x16_bf16 v[48:63], v[76:79], a[140:143], v[48:63]\n"
"s_nop 11\n"
"s_waitcnt vmcnt(10)\n"
"v_lshlrev_b32 v132, 16, v104\n"
"v_lshlrev_b32 v128, 16, v88\n"
"v_and_b32 v133, 0xffff0000, v104\n"
"v_and_b32 v129, 0xffff0000, v88\n"
"v_lshlrev_b32 v134, 16, v105\n"
"v_lshlrev_b32 v130, 16, v89\n"
"v_and_b32 v135, 0xffff0000, v105\n"
"v_and_b32 v131, 0xffff0000, v89\n"
"v_fma_f32 v112, v16, v150, v132\n"
"v_fma_f32 v113, v17, v150, v133\n"
"v_fma_f32 v114, v18, v150, v134\n"
"v_fma_f32 v115, v19, v150, v135\n"
"v_mul_f32 v116, s56, v112\n"
"v_mul_f32 v117, s56, v113\n"
"v_mul_f32 v118, s56, v114\n"
"v_mul_f32 v119, s56, v115\n"
"v_exp_f32 v116, v116\n"
"v_exp_f32 v117, v117\n"
"v_exp_f32 v118, v118\n"
"v_exp_f32 v119, v119\n"
"v_add_f32 v116, 1.0, v116\n"
"v_add_f32 v117, 1.0, v117\n"
"v_add_f32 v118, 1.0, v118\n"
"v_add_f32 v119, 1.0, v119\n"
"v_rcp_f32 v116, v116\n"
"v_rcp_f32 v117, v117\n"
"v_rcp_f32 v118, v118\n"
"v_rcp_f32 v119, v119\n"
"v_mul_f32 v112, v112, v116\n"
"v_mul_f32 v113, v113, v117\n"
"v_mul_f32 v114, v114, v118\n"
"v_mul_f32 v115, v115, v119\n"
"v_fma_f32 v128, v48, v112, v128\n"
"v_fma_f32 v129, v49, v113, v129\n"
"v_fma_f32 v130, v50, v114, v130\n"
"v_fma_f32 v131, v51, v115, v131\n"
"v_cvt_pk_bf16_f32 v136, v128, v129\n"
"v_cvt_pk_bf16_f32 v137, v130, v131\n"
"buffer_store_dwordx2 v[136:137], v148, s[44:47], 0 offen offset:64\n"
"s_waitcnt vmcnt(9)\n"
"v_lshlrev_b32 v132, 16, v106\n"
"v_lshlrev_b32_e64 v128, 16, v90\n"
"v_and_b32 v133, 0xffff0000, v106\n"
"v_and_b32 v129, 0xffff0000, v90\n"
"v_lshlrev_b32 v134, 16, v107\n"
"v_lshlrev_b32 v130, 16, v91\n"
"v_and_b32 v135, 0xffff0000, v107\n"
"v_and_b32 v131, 0xffff0000, v91\n"
"v_fma_f32 v112, v20, v150, v132\n"
"v_fma_f32 v113, v21, v150, v133\n"
"v_fma_f32 v114, v22, v150, v134\n"
"v_fma_f32 v115, v23, v150, v135\n"
"v_mul_f32 v116, s56, v112\n"
"v_mul_f32 v117, s56, v113\n"
"v_mul_f32 v118, s56, v114\n"
"v_mul_f32 v119, s56, v115\n"
"v_exp_f32 v116, v116\n"
"v_exp_f32 v117, v117\n"
"v_exp_f32 v118, v118\n"
"v_exp_f32 v119, v119\n"
"v_add_f32 v116, 1.0, v116\n"
"v_add_f32 v117, 1.0, v117\n"
"v_add_f32 v118, 1.0, v118\n"
"v_add_f32 v119, 1.0, v119\n"
"v_rcp_f32 v116, v116\n"
"v_rcp_f32 v117, v117\n"
"v_rcp_f32 v118, v118\n"
"v_rcp_f32 v119, v119\n"
"v_mul_f32 v112, v112, v116\n"
"v_mul_f32 v113, v113, v117\n"
"v_mul_f32 v114, v114, v118\n"
"v_mul_f32 v115, v115, v119\n"
"v_fma_f32 v128, v52, v112, v128\n"
"v_fma_f32 v129, v53, v113, v129\n"
"v_fma_f32 v130, v54, v114, v130\n"
"v_fma_f32 v131, v55, v115, v131\n"
"v_cvt_pk_bf16_f32 v138, v128, v129\n"
"v_cvt_pk_bf16_f32 v139, v130, v131\n"
"buffer_store_dwordx2 v[138:139], v148, s[44:47], 0 offen offset:80\n"
"s_waitcnt vmcnt(8)\n"
"v_lshlrev_b32 v132, 16, v108\n"
"v_lshlrev_b32_e64 v128, 16, v92\n"
"v_and_b32 v133, 0xffff0000, v108\n"
"v_and_b32 v129, 0xffff0000, v92\n"
"v_lshlrev_b32 v134, 16, v109\n"
"v_lshlrev_b32 v130, 16, v93\n"
"v_and_b32 v135, 0xffff0000, v109\n"
"v_and_b32 v131, 0xffff0000, v93\n"
"v_fma_f32 v112, v24, v150, v132\n"
"v_fma_f32 v113, v25, v150, v133\n"
"v_fma_f32 v114, v26, v150, v134\n"
"v_fma_f32 v115, v27, v150, v135\n"
"v_mul_f32 v116, s56, v112\n"
"v_mul_f32 v117, s56, v113\n"
"v_mul_f32 v118, s56, v114\n"
"v_mul_f32 v119, s56, v115\n"
"v_exp_f32 v116, v116\n"
"v_exp_f32 v117, v117\n"
"v_exp_f32 v118, v118\n"
"v_exp_f32 v119, v119\n"
"v_add_f32 v116, 1.0, v116\n"
"v_add_f32 v117, 1.0, v117\n"
"v_add_f32 v118, 1.0, v118\n"
"v_add_f32 v119, 1.0, v119\n"
"v_rcp_f32 v116, v116\n"
"v_rcp_f32 v117, v117\n"
"v_rcp_f32 v118, v118\n"
"v_rcp_f32 v119, v119\n"
"v_mul_f32 v112, v112, v116\n"
"v_mul_f32 v113, v113, v117\n"
"v_mul_f32 v114, v114, v118\n"
"v_mul_f32 v115, v115, v119\n"
"v_fma_f32 v128, v56, v112, v128\n"
"v_fma_f32 v129, v57, v113, v129\n"
"v_fma_f32 v130, v58, v114, v130\n"
"v_fma_f32 v131, v59, v115, v131\n"
"v_cvt_pk_bf16_f32 v140, v128, v129\n"
"v_cvt_pk_bf16_f32 v141, v130, v131\n"
"buffer_store_dwordx2 v[140:141], v148, s[44:47], 0 offen offset:96\n"
"s_waitcnt vmcnt(7)\n"
"v_lshlrev_b32 v132, 16, v110\n"
"v_lshlrev_b32_e64 v128, 16, v94\n"
"v_and_b32 v133, 0xffff0000, v110\n"
"v_and_b32 v129, 0xffff0000, v94\n"
"v_lshlrev_b32 v134, 16, v111\n"
"v_lshlrev_b32 v130, 16, v95\n"
"v_and_b32 v135, 0xffff0000, v111\n"
"v_and_b32 v131, 0xffff0000, v95\n"
"v_fma_f32 v112, v28, v150, v132\n"
"v_fma_f32 v113, v29, v150, v133\n"
"v_fma_f32 v114, v30, v150, v134\n"
"v_fma_f32 v115, v31, v150, v135\n"
"v_mul_f32 v116, s56, v112\n"
"v_mul_f32 v117, s56, v113\n"
"v_mul_f32 v118, s56, v114\n"
"v_mul_f32 v119, s56, v115\n"
"v_exp_f32 v116, v116\n"
"v_exp_f32 v117, v117\n"
"v_exp_f32 v118, v118\n"
"v_exp_f32 v119, v119\n"
"v_add_f32 v116, 1.0, v116\n"
"v_add_f32 v117, 1.0, v117\n"
"v_add_f32 v118, 1.0, v118\n"
"v_add_f32 v119, 1.0, v119\n"
"v_rcp_f32 v116, v116\n"
"v_rcp_f32 v117, v117\n"
"v_rcp_f32 v118, v118\n"
"v_rcp_f32 v119, v119\n"
"v_mul_f32 v112, v112, v116\n"
"v_mul_f32 v113, v113, v117\n"
"v_mul_f32 v114, v114, v118\n"
"v_mul_f32 v115, v115, v119\n"
"v_fma_f32 v128, v60, v112, v128\n"
"v_fma_f32 v129, v61, v113, v129\n"
"v_fma_f32 v130, v62, v114, v130\n"
"v_fma_f32 v131, v63, v115, v131\n"
"v_cvt_pk_bf16_f32 v142, v128, v129\n"
"v_cvt_pk_bf16_f32 v143, v130, v131\n"
"buffer_store_dwordx2 v[142:143], v148, s[44:47], 0 offen offset:112\n"
"LK2_CODE_END:\n"
    :: "s"(__builtin_amdgcn_kernarg_segment_ptr()), "s"(blockIdx.x), "v"(threadIdx.x)
    : "v0", "v1", "v2", "v3", "v4", "v5", "v6", "v7", "v8", "v9", "v10", "v11", "v12", "v13", "v14", "v15", "v16", "v17", "v18", "v19", "v20", "v21", "v22", "v23", "v24", "v25", "v26", "v27", "v28", "v29", "v30", "v31", "v32", "v33", "v34", "v35", "v36", "v37", "v38", "v39", "v40", "v41", "v42", "v43", "v44", "v45", "v46", "v47", "v48", "v49", "v50", "v51", "v52", "v53", "v54", "v55", "v56", "v57", "v58", "v59", "v60", "v61", "v62", "v63", "v64", "v65", "v66", "v67", "v68", "v69", "v70", "v71", "v72", "v73", "v74", "v75", "v76", "v77", "v78", "v79", "v80", "v81", "v82", "v83", "v84", "v85", "v86", "v87", "v88", "v89", "v90", "v91", "v92", "v93", "v94", "v95", "v96", "v97", "v98", "v99", "v100", "v101", "v102", "v103", "v104", "v105", "v106", "v107", "v108", "v109", "v110", "v111", "v112", "v113", "v114", "v115", "v116", "v117", "v118", "v119", "v120", "v121", "v122", "v123", "v124", "v125", "v126", "v127", "v128", "v129", "v130", "v131", "v132", "v133", "v134", "v135", "v136", "v137", "v138", "v139", "v140", "v141", "v142", "v143", "v144", "v145", "v146", "v147", "v148", "v149", "v150", "v151", "a0", "a1", "a2", "a3", "a4", "a5", "a6", "a7", "a8", "a9", "a10", "a11", "a12", "a13", "a14", "a15", "a16", "a17", "a18", "a19", "a20", "a21", "a22", "a23", "a24", "a25", "a26", "a27", "a28", "a29", "a30", "a31", "a32", "a33", "a34", "a35", "a36", "a37", "a38", "a39", "a40", "a41", "a42", "a43", "a44", "a45", "a46", "a47", "a48", "a49", "a50", "a51", "a52", "a53", "a54", "a55", "a56", "a57", "a58", "a59", "a60", "a61", "a62", "a63", "a64", "a65", "a66", "a67", "a68", "a69", "a70", "a71", "a72", "a73", "a74", "a75", "a76", "a77", "a78", "a79", "a80", "a81", "a82", "a83", "a84", "a85", "a86", "a87", "a88", "a89", "a90", "a91", "a92", "a93", "a94", "a95", "a96", "a97", "a98", "a99", "a100", "a101", "a102", "a103", "a104", "a105", "a106", "a107", "a108", "a109", "a110", "a111", "a112", "a113", "a114", "a115", "a116", "a117", "a118", "a119", "a120", "a121", "a122", "a123", "a124", "a125", "a126", "a127", "a128", "a129", "a130", "a131", "a132", "a133", "a134", "a135", "a136", "a137", "a138", "a139", "a140", "a141", "a142", "a143", "s16", "s17", "s18", "s19", "s20", "s21", "s22", "s23", "s24", "s25", "s26", "s27", "s28", "s29", "s30", "s31", "s33", "s34", "s35", "s36", "s37", "s38", "s39", "s40", "s41", "s42", "s43", "s44", "s45", "s46", "s47", "s48", "s49", "s50", "s51", "s52", "s53", "s54", "s55", "s56", "s57", "s58", "s59", "s60", "s61", "s62", "s63", "s64", "s65", "s66", "s67", "s68", "s69", "s70", "s71", "s72", "s73", "s74", "s75", "s76", "s77", "s78", "s79", "vcc", "memory", "scc");
}

__device__ __forceinline__ float fb_bf(unsigned short u) { return __uint_as_float((unsigned)u << 16); }
__device__ __forceinline__ void attention_true_max(const char* lds, char* ws, int head, int qtile0, int lane) {
    const int qt = qtile0 + (lane >> 5), r = lane & 31;
    unsigned* q1 = (unsigned*)(ws + WS_Q1 + ((size_t)head * 64 + qt) * 1024);
    unsigned* q2 = (unsigned*)(ws + WS_Q2 + ((size_t)head * 64 + qt) * 128);
    float q[18], acc[18];
#pragma unroll
    for (int d = 0; d < 18; d += 2) {
        const unsigned u = __hip_atomic_load(d < 16 ? q1 + (d >> 3) * 128 + r * 4 + ((d & 7) >> 1) : q2 + r, __ATOMIC_RELAXED, __HIP_MEMORY_SCOPE_AGENT);
        q[d] = bf_lo(u); q[d + 1] = bf_hi(u);
    }
#pragma unroll
    for (int d = 0; d < 18; ++d) acc[d] = 0.f;
    float m = -INFINITY, l = 0.f;
    for (int key = 0; key < 2048; ++key) {
        const int blk = key >> 5, kr = key & 31;
        const unsigned short* k1 = (const unsigned short*)(lds + blk * 2304);
        const unsigned short* k2 = (const unsigned short*)(lds + 147456 + (blk >> 2) * 512 + kr * 16 + (blk & 3) * 4);
        float s = 0.f;
#pragma unroll
        for (int d = 0; d < 16; ++d) s += q[d] * fb_bf(k1[(d >> 3) * 256 + kr * 8 + (d & 7)]);
        s += q[16] * fb_bf(k2[0]) + q[17] * fb_bf(k2[1]);
        const float mn = fmaxf(m, s);
        const float a = (m == mn) ? 1.f : exp2f(m - mn), pr = exp2f(s - mn);
        const int ss = kr >> 4, jh = (kr >> 3) & 1, hh = (kr >> 2) & 1, jl = kr & 3;
        const unsigned short* vv = (const unsigned short*)(lds + blk * 2304 + 1024 + (ss * 2 + hh) * 320) + jh * 4 + jl;
        l = l * a + pr;
#pragma unroll
        for (int d = 0; d < 18; ++d) acc[d] = acc[d] * a + pr * fb_bf(vv[d * 8]);
        m = mn;
    }
    const float rl = 1.f / l;
    char* o = ws + WS_O + (size_t)qt * 36864 + r * 16;
#pragma unroll
    for (int d = 0; d < 18; d += 2) {
        const int k = head * 18 + d;
        *(unsigned*)(o + (k >> 3) * 512 + (k & 7) * 2) = pack_bf16(acc[d] * rl, acc[d + 1] * rl);
    }
}

struct K1Params { const void* x; const void* wqkv; const void* w1; const void* b1; const void* w2; void* ws; void* dbg; const void* wo; };

extern "C" __global__ __launch_bounds__(256, 1) void k1_main(K1Params prm) {
    extern __shared__ __attribute__((aligned(16))) char lds[];
    unsigned bad;
    asm volatile(
".p2align 3\n"
"LSTART_%=:\n"
"s_load_dwordx16 s[16:31], %1, 0\n"
"v_mov_b32 v250, %3\n"
"v_and_b32 v228, 63, v250\n"
"v_lshrrev_b32 v0, 6, v250\n"
"s_nop 0\n"
"v_readfirstlane_b32 s52, v0\n"
"s_getpc_b64 s[56:57]\n"
"LPC_%=:\n"
"s_sub_u32 s56, s56, LPC_%=-LSTART_%=\n"
"s_subb_u32 s57, s57, 0\n"
"s_mov_b32 s92, s56\n"
"s_and_b32 s93, s57, 0xffff\n"
"s_mov_b32 s94, LEND_%=-LSTART_%=\n"
"s_mov_b32 s95, 0x20000\n"
"v_lshlrev_b32_e64 v30, 7, v250\n"
"s_mov_b32 s58, 32768\n"
"buffer_load_dword v28, v30, s[92:95], 0 offen\n"
"buffer_load_dword v29, v30, s[92:95], s58 offen\n"
"s_mov_b32 s56, %2\n"
"s_mov_b32 s35, s56\n"
"s_and_b32 s57, s56, 1\n"
"s_lshl_b32 s57, s57, 3\n"
"s_bfe_u32 s53, s56, 0x30003\n"
"s_add_u32 s53, s53, s57\n"
"s_bfe_u32 s57, s56, 0x20001\n"
"s_lshl_b32 s57, s57, 2\n"
"s_lshr_b32 s54, s56, 6\n"
"s_add_u32 s54, s54, s57\n"
"s_mul_i32 s55, s52, 3072\n"
"s_mov_b32 s96, 0x3f80\n"
"s_mov_b32 s97, 0x3f800000\n"
"s_mov_b32 s75, 0\n"
"s_waitcnt lgkmcnt(0)\n"
"s_mov_b32 s36, s16\n"
"s_and_b32 s37, s17, 0xffff\n"
"s_mov_b32 s38, 2359296\n"
"s_mov_b32 s39, 0x20000\n"
"s_mov_b32 s48, s26\n"
"s_and_b32 s49, s27, 0xffff\n"
"s_mov_b32 s50, 52183040\n"
"s_mov_b32 s51, 0x20000\n"
"s_lshl_b32 s56, s53, 7\n"
"s_lshl_b32 s57, s52, 5\n"
"s_add_u32 s56, s56, s57\n"
"s_mul_i32 s67, s56, 1152\n"
"s_mul_i32 s68, s52, 36864\n"
"v_lshlrev_b32 v1, 4, v228\n"
"s_add_u32 m0, s68, 0\n"
"s_add_u32 s60, s67, 0\n"
"v_mov_b32_e64 v2, v1\n"
"buffer_load_dwordx4 v2, s[36:39], s60 offen lds\n"
"s_add_u32 m0, s68, 1024\n"
"s_add_u32 s60, s67, 1152\n"
"v_xor_b32_e64 v3, 16, v1\n"
"buffer_load_dwordx4 v3, s[36:39], s60 offen lds\n"
"s_add_u32 m0, s68, 2048\n"
"s_add_u32 s60, s67, 2304\n"
"v_xor_b32_e64 v2, 32, v1\n"
"buffer_load_dwordx4 v2, s[36:39], s60 offen lds\n"
"s_add_u32 m0, s68, 3072\n"
"s_add_u32 s60, s67, 3456\n"
"v_xor_b32_e64 v3, 48, v1\n"
"buffer_load_dwordx4 v3, s[36:39], s60 offen lds\n"
"s_add_u32 m0, s68, 4096\n"
"s_add_u32 s60, s67, 4608\n"
"v_xor_b32_e64 v2, 64, v1\n"
"buffer_load_dwordx4 v2, s[36:39], s60 offen lds\n"
"s_add_u32 m0, s68, 5120\n"
"s_add_u32 s60, s67, 5760\n"
"v_xor_b32 v3, 80, v1\n"
"buffer_load_dwordx4 v3, s[36:39], s60 offen lds\n"
"s_add_u32 m0, s68, 6144\n"
"s_add_u32 s60, s67, 6912\n"
"v_xor_b32 v2, 96, v1\n"
"buffer_load_dwordx4 v2, s[36:39], s60 offen lds\n"
"s_add_u32 m0, s68, 7168\n"
"s_add_u32 s60, s67, 8064\n"
"v_xor_b32 v3, 112, v1\n"
"buffer_load_dwordx4 v3, s[36:39], s60 offen lds\n"
"s_add_u32 m0, s68, 8192\n"
"s_add_u32 s60, s67, 9216\n"
"v_xor_b32 v2, 128, v1\n"
"buffer_load_dwordx4 v2, s[36:39], s60 offen lds\n"
"s_add_u32 m0, s68, 9216\n"
"s_add_u32 s60, s67, 10368\n"
"v_xor_b32 v3, 144, v1\n"
"buffer_load_dwordx4 v3, s[36:39], s60 offen lds\n"
"s_add_u32 m0, s68, 10240\n"
"s_add_u32 s60, s67, 11520\n"
"v_xor_b32 v2, 160, v1\n"
"buffer_load_dwordx4 v2, s[36:39], s60 offen lds\n"
"s_add_u32 m0, s68, 11264\n"
"s_add_u32 s60, s67, 12672\n"
"v_xor_b32 v3, 176, v1\n"
"buffer_load_dwordx4 v3, s[36:39], s60 offen lds\n"
"s_add_u32 m0, s68, 12288\n"
"s_add_u32 s60, s67, 13824\n"
"v_xor_b32 v2, 192, v1\n"
"buffer_load_dwordx4 v2, s[36:39], s60 offen lds\n"
"s_add_u32 m0, s68, 13312\n"
"s_add_u32 s60, s67, 14976\n"
"v_xor_b32 v3, 208, v1\n"
"buffer_load_dwordx4 v3, s[36:39], s60 offen lds\n"
"s_add_u32 m0, s68, 14336\n"
"s_add_u32 s60, s67, 16128\n"
"v_xor_b32 v2, 224, v1\n"
"buffer_load_dwordx4 v2, s[36:39], s60 offen lds\n"
"s_add_u32 m0, s68, 15360\n"
"s_add_u32 s60, s67, 17280\n"
"v_xor_b32 v3, 240, v1\n"
"buffer_load_dwordx4 v3, s[36:39], s60 offen lds\n"
"s_add_u32 m0, s68, 16384\n"
"s_add_u32 s60, s67, 18432\n"
"v_mov_b32_e64 v2, v1\n"
"buffer_load_dwordx4 v2, s[36:39], s60 offen lds\n"
"s_add_u32 m0, s68, 17408\n"
"s_add_u32 s60, s67, 19584\n"
"v_xor_b32_e64 v3, 16, v1\n"
"buffer_load_dwordx4 v3, s[36:39], s60 offen lds\n"
"s_add_u32 m0, s68, 18432\n"
"s_add_u32 s60, s67, 20736\n"
"v_xor_b32_e64 v2, 32, v1\n"
"buffer_load_dwordx4 v2, s[36:39], s60 offen lds\n"
"s_add_u32 m0, s68, 19456\n"
"s_add_u32 s60, s67, 21888\n"
"v_xor_b32_e64 v3, 48, v1\n"
"buffer_load_dwordx4 v3, s[36:39], s60 offen lds\n"
"s_add_u32 m0, s68, 20480\n"
"s_add_u32 s60, s67, 23040\n"
"v_xor_b32_e64 v2, 64, v1\n"
"buffer_load_dwordx4 v2, s[36:39], s60 offen lds\n"
"s_add_u32 m0, s68, 21504\n"
"s_add_u32 s60, s67, 24192\n"
"v_xor_b32 v3, 80, v1\n"
"buffer_load_dwordx4 v3, s[36:39], s60 offen lds\n"
"s_add_u32 m0, s68, 22528\n"
"s_add_u32 s60, s67, 25344\n"
"v_xor_b32 v2, 96, v1\n"
"buffer_load_dwordx4 v2, s[36:39], s60 offen lds\n"
"s_add_u32 m0, s68, 23552\n"
"s_add_u32 s60, s67, 26496\n"
"v_xor_b32 v3, 112, v1\n"
"buffer_load_dwordx4 v3, s[36:39], s60 offen lds\n"
"s_add_u32 m0, s68, 24576\n"
"s_add_u32 s60, s67, 27648\n"
"v_xor_b32 v2, 128, v1\n"
"buffer_load_dwordx4 v2, s[36:39], s60 offen lds\n"
"s_add_u32 m0, s68, 25600\n"
"s_add_u32 s60, s67, 28800\n"
"v_xor_b32 v3, 144, v1\n"
"buffer_load_dwordx4 v3, s[36:39], s60 offen lds\n"
"s_add_u32 m0, s68, 26624\n"
"s_add_u32 s60, s67, 29952\n"
"v_xor_b32 v2, 160, v1\n"
"buffer_load_dwordx4 v2, s[36:39], s60 offen lds\n"
"s_add_u32 m0, s68, 27648\n"
"s_add_u32 s60, s67, 31104\n"
"v_xor_b32 v3, 176, v1\n"
"buffer_load_dwordx4 v3, s[36:39], s60 offen lds\n"
"s_add_u32 m0, s68, 28672\n"
"s_add_u32 s60, s67, 32256\n"
"v_xor_b32 v2, 192, v1\n"
"buffer_load_dwordx4 v2, s[36:39], s60 offen lds\n"
"s_add_u32 m0, s68, 29696\n"
"s_add_u32 s60, s67, 33408\n"
"v_xor_b32 v3, 208, v1\n"
"buffer_load_dwordx4 v3, s[36:39], s60 offen lds\n"
"s_add_u32 m0, s68, 30720\n"
"s_add_u32 s60, s67, 34560\n"
"v_xor_b32 v2, 224, v1\n"
"buffer_load_dwordx4 v2, s[36:39], s60 offen lds\n"
"s_add_u32 m0, s68, 31744\n"
"s_add_u32 s60, s67, 35712\n"
"v_xor_b32 v3, 240, v1\n"
"buffer_load_dwordx4 v3, s[36:39], s60 offen lds\n"
"v_lshrrev_b32_e64 v4, 3, v228\n"
"v_mul_u32_u24 v4, 1152, v4\n"
"v_and_b32_e64 v5, 7, v228\n"
"v_lshrrev_b32 v6, 4, v228\n"
"v_xor_b32 v5, v5, v6\n"
"v_lshl_add_u32 v4, v5, 4, v4\n"
"v_add_u32 v4, 1024, v4\n"
"v_xor_b32_e64 v5, 64, v4\n"
"s_add_u32 m0, s68, 32768\n"
"s_add_u32 s60, s67, 0\n"
"buffer_load_dwordx4 v4, s[36:39], s60 offen lds\n"
"s_add_u32 m0, s68, 33792\n"
"s_add_u32 s60, s67, 9216\n"
"buffer_load_dwordx4 v5, s[36:39], s60 offen lds\n"
"s_add_u32 m0, s68, 34816\n"
"s_add_u32 s60, s67, 18432\n"
"buffer_load_dwordx4 v4, s[36:39], s60 offen lds\n"
"s_add_u32 m0, s68, 35840\n"
"s_add_u32 s60, s67, 27648\n"
"buffer_load_dwordx4 v5, s[36:39], s60 offen lds\n"
"v_mov_b32 v6, 0\n"
"buffer_load_dwordx2 v[8:9], v6, s[48:51], 0 offen sc1\n"
"s_mov_b32 s36, s18\n"
"s_and_b32 s37, s19, 0xffff\n"
"s_mov_b32 s38, 1990656\n"
"s_mov_b32 s39, 0x20000\n"
"s_mov_b32 s40, s20\n"
"s_and_b32 s41, s21, 0xffff\n"
"s_mov_b32 s42, 5308416\n"
"s_mov_b32 s43, 0x20000\n"
"s_mov_b32 s44, s24\n"
"s_and_b32 s45, s25, 0xffff\n"
"s_mov_b32 s46, 5308416\n"
"s_mov_b32 s47, 0x20000\n"
"s_mul_i32 s56, s54, 576\n"
"s_add_u32 s22, s22, s56\n"
"s_addc_u32 s23, s23, 0\n"
"v_lshlrev_b32 v10, 1, v250\n"
"global_load_ushort v11, v10, s[22:23]\n"
"v_min_u32 v12, 31, v250\n"
"v_lshlrev_b32 v13, 1, v12\n"
"global_load_ushort v14, v13, s[22:23] offset:512\n"
"v_lshrrev_b32_e64 v16, 5, v228\n"
"v_lshrrev_b32 v17, 4, v228\n"
"v_and_b32 v17, 1, v17\n"
"v_lshrrev_b32 v18, 2, v228\n"
"v_and_b32 v18, 3, v18\n"
"v_and_b32 v19, 3, v228\n"
"v_lshlrev_b32 v19, 3, v19\n"
"v_lshl_add_u32 v20, v17, 5, v19\n"
"v_lshl_add_u32 v21, v16, 2, v18\n"
"v_lshl_add_u32 v24, v21, 6, v20\n"
"v_add_u32 v238, 73728, v24\n"
"v_add_u32 v239, 110592, v24\n"
"v_lshl_add_u32 v22, v16, 2, v18\n"
"v_mul_u32_u24 v22, 384, v22\n"
"v_add_u32_e64 v22, v22, v20\n"
"v_lshrrev_b32 v23, 1, v18\n"
"v_lshlrev_b32 v23, 6, v23\n"
"v_add_u32 v24, v22, v23\n"
"v_sub_u32 v25, v22, v23\n"
"v_add_u32 v240, 73728, v24\n"
"v_add_u32 v241, 73728, v25\n"
"v_add_u32 v242, 110592, v24\n"
"v_add_u32 v243, 110592, v25\n"
"v_xor_b32_e64 v24, 0, v18\n"
"v_lshl_add_u32 v24, v24, 6, v20\n"
"v_lshl_add_u32 v244, v21, 8, v24\n"
"v_add_u32 v244, 110592, v244\n"
"v_xor_b32_e64 v24, 1, v18\n"
"v_lshl_add_u32 v24, v24, 6, v20\n"
"v_lshl_add_u32 v245, v21, 8, v24\n"
"v_add_u32 v245, 110592, v245\n"
"v_xor_b32_e64 v24, 2, v18\n"
"v_lshl_add_u32 v24, v24, 6, v20\n"
"v_lshl_add_u32 v246, v21, 8, v24\n"
"v_add_u32 v246, 110592, v246\n"
"v_xor_b32_e64 v24, 3, v18\n"
"v_lshl_add_u32 v24, v24, 6, v20\n"
"v_lshl_add_u32 v247, v21, 8, v24\n"
"v_add_u32 v247, 110592, v247\n"
"v_lshlrev_b32_e64 v248, 4, v16\n"
"v_add_u32 v248, 155648, v248\n"
"s_cmp_ge_u32 s53, 9\n"
"s_cselect_b32 s56, 9, 0\n"
"s_sub_u32 s16, s53, s56\n"
"s_mov_b32 s17, s16\n"
"s_mov_b32 s18, s16\n"
"s_lshl_b32 s56, s16, 7\n"
"v_add_u32 v248, s56, v248\n"
"v_lshrrev_b32 v229, 2, v228\n"
"v_mul_u32_u24 v229, 9216, v229\n"
"v_and_b32_e64 v24, 3, v228\n"
"v_lshl_add_u32 v229, v24, 4, v229\n"
"s_mul_i32 s56, s52, 442368\n"
"s_mul_i32 s57, s54, 576\n"
"s_add_u32 s56, s56, s57\n"
"v_add_u32 v229, s56, v229\n"
"v_add_u32 v230, 146432, v229\n"
"v_add_u32 v231, 292864, v229\n"
"s_mul_i32 s56, s52, 9216\n"
"s_mul_i32 s57, s54, 331776\n"
"s_add_u32 s56, s56, s57\n"
"v_lshlrev_b32 v24, 4, v228\n"
"v_mul_u32_u24 v25, 171, v24\n"
"v_lshrrev_b32_e64 v25, 16, v25\n"
"v_mul_u32_u24 v26, 384, v25\n"
"v_sub_u32_e64 v26, v24, v26\n"
"v_lshrrev_b32 v27, 1, v25\n"
"v_and_b32 v27, 1, v27\n"
"v_lshlrev_b32 v27, 6, v27\n"
"v_xor_b32 v26, v26, v27\n"
"v_mul_u32_u24 v25, 1152, v25\n"
"v_add_u32_e64 v232, v25, v26\n"
"v_add_u32 v232, s56, v232\n"
"v_lshlrev_b32 v24, 4, v228\n"
"v_add_u32 v24, 1024, v24\n"
"v_mul_u32_u24 v25, 171, v24\n"
"v_lshrrev_b32_e64 v25, 16, v25\n"
"v_mul_u32_u24 v26, 384, v25\n"
"v_sub_u32_e64 v26, v24, v26\n"
"v_lshrrev_b32 v27, 1, v25\n"
"v_and_b32 v27, 1, v27\n"
"v_lshlrev_b32 v27, 6, v27\n"
"v_xor_b32 v26, v26, v27\n"
"v_mul_u32_u24 v25, 1152, v25\n"
"v_add_u32 v233, v25, v26\n"
"v_add_u32 v233, s56, v233\n"
"v_subrev_u32 v233, 1024, v233\n"
"v_lshlrev_b32_e64 v24, 4, v228\n"
"v_add_u32 v24, 2048, v24\n"
"v_mul_u32_u24 v25, 171, v24\n"
"v_lshrrev_b32_e64 v25, 16, v25\n"
"v_mul_u32_u24 v26, 384, v25\n"
"v_sub_u32_e64 v26, v24, v26\n"
"v_lshrrev_b32 v27, 1, v25\n"
"v_and_b32 v27, 1, v27\n"
"v_lshlrev_b32 v27, 6, v27\n"
"v_xor_b32 v26, v26, v27\n"
"v_mul_u32_u24 v25, 1152, v25\n"
"v_add_u32 v234, v25, v26\n"
"v_add_u32 v234, s56, v234\n"
"v_subrev_u32 v234, 2048, v234\n"
"s_mul_i32 s64, s54, 52\n"
"s_lshr_b32 s64, s64, 8\n"
"s_min_u32 s64, s64, 2\n"
"s_mul_i32 s56, s64, 5\n"
"s_sub_u32 s56, s54, s56\n"
"s_min_u32 s56, s56, 4\n"
"s_add_u32 s57, s56, 1\n"
"s_lshr_b32 s57, s57, 1\n"
"s_min_u32 s57, s57, 2\n"
"s_mul_i32 s65, s56, 6\n"
"s_add_u32 s65, s65, s57\n"
"s_and_b32 s57, s56, 5\n"
"s_cmp_eq_u32 s57, 0\n"
"s_cselect_b32 s66, 7, 6\n"
"s_cmp_eq_u32 s54, 15\n"
"s_cselect_b32 s66, 0, s66\n"
"s_mul_i32 s57, s65, 36\n"
"s_and_b32 s33, s57, 15\n"
"s_sub_u32 s57, s57, s33\n"
"s_mul_i32 s56, s64, 1152\n"
"s_add_u32 s56, s56, s57\n"
"s_mul_i32 s57, s52, 41472\n"
"s_add_u32 s56, s56, s57\n"
"v_lshrrev_b32 v24, 4, v228\n"
"v_mul_u32_u24 v25, 3456, v24\n"
"v_lshrrev_b32_e64 v26, 2, v228\n"
"v_and_b32 v26, 3, v26\n"
"v_xor_b32 v26, v26, v24\n"
"v_lshl_add_u32 v25, v26, 6, v25\n"
"v_and_b32_e64 v26, 3, v228\n"
"v_lshl_add_u32 v25, v26, 4, v25\n"
"v_add_u32_e64 v235, s56, v25\n"
"v_add_u32 v236, 12800, v235\n"
"v_add_u32 v237, 25600, v235\n"
"v_lshlrev_b32_e64 v15, 2, v250\n"
"v_add_u32 v15, 155648, v15\n"
"s_waitcnt vmcnt(1)\n"
"v_lshlrev_b32 v11, 16, v11\n"
"ds_write_b32 v15, v11\n"
"v_lshlrev_b32_e64 v12, 2, v12\n"
"v_add_u32 v12, 156672, v12\n"
"s_waitcnt vmcnt(0)\n"
"v_lshlrev_b32 v14, 16, v14\n"
"ds_write_b32 v12, v14\n"
"v_readfirstlane_b32 s98, v8\n"
"v_readfirstlane_b32 s99, v9\n"
"s_nop 1\n"
"s_mul_i32 s62, s98, 0x9E3779B1\n"
"s_add_u32 s62, s62, 0x7F4A7C15\n"
"s_mul_i32 s63, s99, 0x85EBCA6B\n"
"s_add_u32 s63, s63, s62\n"
"s_xor_b32 s63, s63, 0xC2B2AE35\n"
"v_and_b32 v16, 31, v228\n"
"v_lshrrev_b32 v17, 5, v228\n"
"s_mul_i32 s56, s52, 36864\n"
"v_and_b32_e64 v18, 15, v16\n"
"v_xor_b32 v18, v18, v17\n"
"v_lshlrev_b32 v18, 4, v18\n"
"v_lshl_add_u32 v18, v16, 10, v18\n"
"v_add_u32_e64 v18, s56, v18\n"
"v_lshrrev_b32 v19, 1, v16\n"
"v_and_b32 v19, 7, v19\n"
"v_xor_b32 v19, v19, v17\n"
"v_lshlrev_b32 v19, 4, v19\n"
"v_lshl_add_u32 v19, v16, 7, v19\n"
"v_add_u32_e64 v19, s56, v19\n"
"v_add_u32 v19, 32768, v19\n"
"v_xor_b32_e64 v176, 0, v18\n"
"v_xor_b32 v177, 32, v18\n"
"v_xor_b32 v178, 64, v18\n"
"v_xor_b32 v179, 96, v18\n"
"v_xor_b32 v180, 128, v18\n"
"v_xor_b32 v181, 160, v18\n"
"v_xor_b32 v182, 192, v18\n"
"v_xor_b32 v183, 224, v18\n"
"v_xor_b32_e64 v184, 0, v19\n"
"v_xor_b32 v185, 32, v19\n"
"v_xor_b32 v186, 64, v19\n"
"v_xor_b32 v187, 96, v19\n"
"v_mov_b32 v188, 0\n"
"v_mov_b32 v189, 0\n"
"v_mov_b32 v190, 0\n"
"v_mov_b32 v191, 0\n"
"v_mov_b32 v251, 0x3f803f80\n"
"ds_read_b128 v[32:35], v176\n"
"ds_read_b128 v[36:39], v177\n"
"ds_read_b128 v[40:43], v178\n"
"ds_read_b128 v[44:47], v179\n"
"ds_read_b128 v[48:51], v180\n"
"ds_read_b128 v[52:55], v181\n"
"ds_read_b128 v[56:59], v182\n"
"ds_read_b128 v[60:63], v183\n"
"ds_read_b128 v[64:67], v176 offset:256\n"
"ds_read_b128 v[68:71], v177 offset:256\n"
"ds_read_b128 v[72:75], v178 offset:256\n"
"ds_read_b128 v[76:79], v179 offset:256\n"
"ds_read_b128 v[80:83], v180 offset:256\n"
"s_waitcnt lgkmcnt(12)\n"
"v_dot2c_f32_bf16 v188, v32, v251\n"
"v_dot2c_f32_bf16 v189, v33, v251\n"
"v_dot2c_f32_bf16 v190, v34, v251\n"
"v_dot2c_f32_bf16 v191, v35, v251\n"
"ds_read_b128 v[84:87], v181 offset:256\n"
"s_waitcnt lgkmcnt(12)\n"
"v_dot2c_f32_bf16 v188, v36, v251\n"
"v_dot2c_f32_bf16 v189, v37, v251\n"
"v_dot2c_f32_bf16 v190, v38, v251\n"
"v_dot2c_f32_bf16 v191, v39, v251\n"
"ds_read_b128 v[88:91], v182 offset:256\n"
"s_waitcnt lgkmcnt(12)\n"
"v_dot2c_f32_bf16 v188, v40, v251\n"
"v_dot2c_f32_bf16 v189, v41, v251\n"
"v_dot2c_f32_bf16 v190, v42, v251\n"
"v_dot2c_f32_bf16 v191, v43, v251\n"
"ds_read_b128 v[92:95], v183 offset:256\n"
"s_waitcnt lgkmcnt(12)\n"
"v_dot2c_f32_bf16 v188, v44, v251\n"
"v_dot2c_f32_bf16 v189, v45, v251\n"
"v_dot2c_f32_bf16 v190, v46, v251\n"
"v_dot2c_f32_bf16 v191, v47, v251\n"
"ds_read_b128 v[96:99], v176 offset:512\n"
"s_waitcnt lgkmcnt(12)\n"
"v_dot2c_f32_bf16 v188, v48, v251\n"
"v_dot2c_f32_bf16 v189, v49, v251\n"
"v_dot2c_f32_bf16 v190, v50, v251\n"
"v_dot2c_f32_bf16 v191, v51, v251\n"
"ds_read_b128 v[100:103], v177 offset:512\n"
"s_waitcnt lgkmcnt(12)\n"
"v_dot2c_f32_bf16 v188, v52, v251\n"
"v_dot2c_f32_bf16 v189, v53, v251\n"
"v_dot2c_f32_bf16 v190, v54, v251\n"
"v_dot2c_f32_bf16 v191, v55, v251\n"
"ds_read_b128 v[104:107], v178 offset:512\n"
"s_waitcnt lgkmcnt(12)\n"
"v_dot2c_f32_bf16 v188, v56, v251\n"
"v_dot2c_f32_bf16 v189, v57, v251\n"
"v_dot2c_f32_bf16 v190, v58, v251\n"
"v_dot2c_f32_bf16 v191, v59, v251\n"
"ds_read_b128 v[108:111], v179 offset:512\n"
"s_waitcnt lgkmcnt(12)\n"
"v_dot2c_f32_bf16 v188, v60, v251\n"
"v_dot2c_f32_bf16 v189, v61, v251\n"
"v_dot2c_f32_bf16 v190, v62, v251\n"
"v_dot2c_f32_bf16 v191, v63, v251\n"
"ds_read_b128 v[112:115], v180 offset:512\n"
"s_waitcnt lgkmcnt(12)\n"
"v_dot2c_f32_bf16 v188, v64, v251\n"
"v_dot2c_f32_bf16 v189, v65, v251\n"
"v_dot2c_f32_bf16 v190, v66, v251\n"
"v_dot2c_f32_bf16 v191, v67, v251\n"
"ds_read_b128 v[116:119], v181 offset:512\n"
"s_waitcnt lgkmcnt(12)\n"
"v_dot2c_f32_bf16 v188, v68, v251\n"
"v_dot2c_f32_bf16 v189, v69, v251\n"
"v_dot2c_f32_bf16 v190, v70, v251\n"
"v_dot2c_f32_bf16 v191, v71, v251\n"
"ds_read_b128 v[120:123], v182 offset:512\n"
"s_waitcnt lgkmcnt(12)\n"
"v_dot2c_f32_bf16 v188, v72, v251\n"
"v_dot2c_f32_bf16 v189, v73, v251\n"
"v_dot2c_f32_bf16 v190, v74, v251\n"
"v_dot2c_f32_bf16 v191, v75, v251\n"
"ds_read_b128 v[124:127], v183 offset:512\n"
"s_waitcnt lgkmcnt(12)\n"
"v_dot2c_f32_bf16 v188, v76, v251\n"
"v_dot2c_f32_bf16 v189, v77, v251\n"
"v_dot2c_f32_bf16 v190, v78, v251\n"
"v_dot2c_f32_bf16 v191, v79, v251\n"
"ds_read_b128 v[128:131], v176 offset:768\n"
"s_waitcnt lgkmcnt(12)\n"
"v_dot2c_f32_bf16 v188, v80, v251\n"
"v_dot2c_f32_bf16 v189, v81, v251\n"
"v_dot2c_f32_bf16 v190, v82, v251\n"
"v_dot2c_f32_bf16 v191, v83, v251\n"
"ds_read_b128 v[132:135], v177 offset:768\n"
"s_waitcnt lgkmcnt(12)\n"
"v_dot2c_f32_bf16 v188, v84, v251\n"
"v_dot2c_f32_bf16 v189, v85, v251\n"
"v_dot2c_f32_bf16 v190, v86, v251\n"
"v_dot2c_f32_bf16 v191, v87, v251\n"
"ds_read_b128 v[136:139], v178 offset:768\n"
"s_waitcnt lgkmcnt(12)\n"
"v_dot2c_f32_bf16 v188, v88, v251\n"
"v_dot2c_f32_bf16 v189, v89, v251\n"
"v_dot2c_f32_bf16 v190, v90, v251\n"
"v_dot2c_f32_bf16 v191, v91, v251\n"
"ds_read_b128 v[140:143], v179 offset:768\n"
"s_waitcnt lgkmcnt(12)\n"
"v_dot2c_f32_bf16 v188, v92, v251\n"
"v_dot2c_f32_bf16 v189, v93, v251\n"
"v_dot2c_f32_bf16 v190, v94, v251\n"
"v_dot2c_f32_bf16 v191, v95, v251\n"
"ds_read_b128 v[144:147], v180 offset:768\n"
"s_waitcnt lgkmcnt(12)\n"
"v_dot2c_f32_bf16 v188, v96, v251\n"
"v_dot2c_f32_bf16 v189, v97, v251\n"
"v_dot2c_f32_bf16 v190, v98, v251\n"
"v_dot2c_f32_bf16 v191, v99, v251\n"
"ds_read_b128 v[148:151], v181 offset:768\n"
"s_waitcnt lgkmcnt(12)\n"
"v_dot2c_f32_bf16 v188, v100, v251\n"
"v_dot2c_f32_bf16 v189, v101, v251\n"
"v_dot2c_f32_bf16 v190, v102, v251\n"
"v_dot2c_f32_bf16 v191, v103, v251\n"
"ds_read_b128 v[152:155], v182 offset:768\n"
"s_waitcnt lgkmcnt(12)\n"
"v_dot2c_f32_bf16 v188, v104, v251\n"
"v_dot2c_f32_bf16 v189, v105, v251\n"
"v_dot2c_f32_bf16 v190, v106, v251\n"
"v_dot2c_f32_bf16 v191, v107, v251\n"
"ds_read_b128 v[156:159], v183 offset:768\n"
"s_waitcnt lgkmcnt(12)\n"
"v_dot2c_f32_bf16 v188, v108, v251\n"
"v_dot2c_f32_bf16 v189, v109, v251\n"
"v_dot2c_f32_bf16 v190, v110, v251\n"
"v_dot2c_f32_bf16 v191, v111, v251\n"
"ds_read_b128 v[160:163], v184\n"
"s_waitcnt lgkmcnt(12)\n"
"v_dot2c_f32_bf16 v188, v112, v251\n"
"v_dot2c_f32_bf16 v189, v113, v251\n"
"v_dot2c_f32_bf16 v190, v114, v251\n"
"v_dot2c_f32_bf16 v191, v115, v251\n"
"ds_read_b128 v[164:167], v185\n"
"s_waitcnt lgkmcnt(12)\n"
"v_dot2c_f32_bf16 v188, v116, v251\n"
"v_dot2c_f32_bf16 v189, v117, v251\n"
"v_dot2c_f32_bf16 v190, v118, v251\n"
"v_dot2c_f32_bf16 v191, v119, v251\n"
"ds_read_b128 v[168:171], v186\n"
"s_waitcnt lgkmcnt(12)\n"
"v_dot2c_f32_bf16 v188, v120, v251\n"
"v_dot2c_f32_bf16 v189, v121, v251\n"
"v_dot2c_f32_bf16 v190, v122, v251\n"
"v_dot2c_f32_bf16 v191, v123, v251\n"
"ds_read_b128 v[172:175], v187\n"
"s_waitcnt lgkmcnt(12)\n"
"v_dot2c_f32_bf16 v188, v124, v251\n"
"v_dot2c_f32_bf16 v189, v125, v251\n"
"v_dot2c_f32_bf16 v190, v126, v251\n"
"v_dot2c_f32_bf16 v191, v127, v251\n"
"s_waitcnt lgkmcnt(11)\n"
"v_dot2c_f32_bf16 v188, v128, v251\n"
"v_dot2c_f32_bf16 v189, v129, v251\n"
"v_dot2c_f32_bf16 v190, v130, v251\n"
"v_dot2c_f32_bf16 v191, v131, v251\n"
"s_waitcnt lgkmcnt(10)\n"
"v_dot2c_f32_bf16 v188, v132, v251\n"
"v_dot2c_f32_bf16 v189, v133, v251\n"
"v_dot2c_f32_bf16 v190, v134, v251\n"
"v_dot2c_f32_bf16 v191, v135, v251\n"
"s_waitcnt lgkmcnt(9)\n"
"v_dot2c_f32_bf16 v188, v136, v251\n"
"v_dot2c_f32_bf16 v189, v137, v251\n"
"v_dot2c_f32_bf16 v190, v138, v251\n"
"v_dot2c_f32_bf16 v191, v139, v251\n"
"s_waitcnt lgkmcnt(8)\n"
"v_dot2c_f32_bf16 v188, v140, v251\n"
"v_dot2c_f32_bf16 v189, v141, v251\n"
"v_dot2c_f32_bf16 v190, v142, v251\n"
"v_dot2c_f32_bf16 v191, v143, v251\n"
"s_waitcnt lgkmcnt(7)\n"
"v_dot2c_f32_bf16 v188, v144, v251\n"
"v_dot2c_f32_bf16 v189, v145, v251\n"
"v_dot2c_f32_bf16 v190, v146, v251\n"
"v_dot2c_f32_bf16 v191, v147, v251\n"
"s_waitcnt lgkmcnt(6)\n"
"v_dot2c_f32_bf16 v188, v148, v251\n"
"v_dot2c_f32_bf16 v189, v149, v251\n"
"v_dot2c_f32_bf16 v190, v150, v251\n"
"v_dot2c_f32_bf16 v191, v151, v251\n"
"s_waitcnt lgkmcnt(5)\n"
"v_dot2c_f32_bf16 v188, v152, v251\n"
"v_dot2c_f32_bf16 v189, v153, v251\n"
"v_dot2c_f32_bf16 v190, v154, v251\n"
"v_dot2c_f32_bf16 v191, v155, v251\n"
"s_waitcnt lgkmcnt(4)\n"
"v_dot2c_f32_bf16 v188, v156, v251\n"
"v_dot2c_f32_bf16 v189, v157, v251\n"
"v_dot2c_f32_bf16 v190, v158, v251\n"
"v_dot2c_f32_bf16 v191, v159, v251\n"
"s_waitcnt lgkmcnt(3)\n"
"v_dot2c_f32_bf16 v188, v160, v251\n"
"v_dot2c_f32_bf16 v189, v161, v251\n"
"v_dot2c_f32_bf16 v190, v162, v251\n"
"v_dot2c_f32_bf16 v191, v163, v251\n"
"s_waitcnt lgkmcnt(2)\n"
"v_dot2c_f32_bf16 v188, v164, v251\n"
"v_dot2c_f32_bf16 v189, v165, v251\n"
"v_dot2c_f32_bf16 v190, v166, v251\n"
"v_dot2c_f32_bf16 v191, v167, v251\n"
"s_waitcnt lgkmcnt(1)\n"
"v_dot2c_f32_bf16 v188, v168, v251\n"
"v_dot2c_f32_bf16 v189, v169, v251\n"
"v_dot2c_f32_bf16 v190, v170, v251\n"
"v_dot2c_f32_bf16 v191, v171, v251\n"
"s_waitcnt lgkmcnt(0)\n"
"v_dot2c_f32_bf16 v188, v172, v251\n"
"v_dot2c_f32_bf16 v189, v173, v251\n"
"v_dot2c_f32_bf16 v190, v174, v251\n"
"v_dot2c_f32_bf16 v191, v175, v251\n"
"s_barrier\n"
"s_add_u32 m0, s55, 110592\n"
"s_mov_b32 s60, 0\n"
"buffer_load_dwordx4 v235, s[36:39], s60 offen lds\n"
"buffer_load_dwordx4 v236, s[36:39], s60 offen offset:1024 lds\n"
"buffer_load_dwordx4 v237, s[36:39], s60 offen offset:2048 lds\n"
"s_nop 2\n"
"v_add_f32 v188, v188, v189\n"
"v_add_f32 v190, v190, v191\n"
"v_add_f32 v188, v188, v190\n"
"v_mov_b32 v189, v188\n"
"s_nop 1\n"
"v_permlane32_swap_b32 v188, v189\n"
"v_add_f32 v188, v188, v189\n"
"v_mul_f32 v188, 0x3ae38e39, v188\n"
"v_cvt_pk_bf16_f32 v189, v188, v188\n"
"v_lshlrev_b32 v189, 16, v189\n"
"v_sub_f32 v249, v188, v189\n"
"v_xor_b32 v189, 0x80000000, v189\n"
"v_and_b32_e64 v184, 31, v228\n"
"v_lshrrev_b32 v185, 5, v228\n"
"v_lshrrev_b32 v186, 3, v184\n"
"v_cmp_eq_u32 vcc, v186, v185\n"
"v_and_b32 v186, 7, v184\n"
"v_mov_b32 v184, 99\n"
"v_mov_b32 v185, 0x3f80\n"
"v_cndmask_b32 v186, v184, v186\n"
"v_mov_b32 v184, 0x3f800000\n"
"v_cmp_eq_u32 vcc, 0, v186\n"
"s_nop 1\n"
"v_cndmask_b32 v187, 0, v185\n"
"v_cmp_eq_u32 vcc, 1, v186\n"
"s_nop 1\n"
"v_cndmask_b32 v224, v187, v184\n"
"v_cmp_eq_u32 vcc, 2, v186\n"
"s_nop 1\n"
"v_cndmask_b32 v187, 0, v185\n"
"v_cmp_eq_u32 vcc, 3, v186\n"
"s_nop 1\n"
"v_cndmask_b32 v225, v187, v184\n"
"v_cmp_eq_u32 vcc, 4, v186\n"
"s_nop 1\n"
"v_cndmask_b32 v187, 0, v185\n"
"v_cmp_eq_u32 vcc, 5, v186\n"
"s_nop 1\n"
"v_cndmask_b32 v226, v187, v184\n"
"v_cmp_eq_u32 vcc, 6, v186\n"
"s_nop 1\n"
"v_cndmask_b32 v187, 0, v185\n"
"v_cmp_eq_u32 vcc, 7, v186\n"
"s_nop 1\n"
"v_cndmask_b32 v227, v187, v184\n"
"v_mov_b32 v208, v189\n"
"v_mov_b32_e64 v209, v189\n"
"v_mov_b32 v210, v189\n"
"v_mov_b32 v211, v189\n"
"v_mov_b32 v212, v189\n"
"v_mov_b32 v213, v189\n"
"v_mov_b32 v214, v189\n"
"v_mov_b32 v215, v189\n"
"v_mov_b32 v216, v189\n"
"v_mov_b32 v217, v189\n"
"v_mov_b32 v218, v189\n"
"v_mov_b32 v219, v189\n"
"v_mov_b32 v220, v189\n"
"v_mov_b32 v221, v189\n"
"v_mov_b32 v222, v189\n"
"v_mov_b32 v223, v189\n"
"v_mov_b32 v176, 0\n"
"v_mov_b32 v177, 0\n"
"v_mov_b32 v178, 0\n"
"v_mov_b32 v179, 0\n"
"v_mov_b32 v180, 0\n"
"v_mov_b32 v181, 0\n"
"v_mov_b32 v182, 0\n"
"v_mov_b32 v183, 0\n"
"v_mfma_f32_32x32x16_bf16 v[0:15], v[224:227], v[32:35], v[208:223]\n"
"s_add_u32 m0, s55, 122880\n"
"v_mfma_f32_32x32x16_bf16 v[16:31], v[224:227], v[36:39], v[208:223]\n"
"s_nop 9\n"
"v_mfma_f32_32x32x16_bf16 v[192:207], v[224:227], v[40:43], v[208:223]\n"
"s_mov_b32 s60, 165888\n"
"v_cvt_pk_bf16_f32 v32, v0, v1\n"
"v_fmac_f32 v176, v0, v0\n"
"v_fmac_f32 v177, v1, v1\n"
"v_cvt_pk_bf16_f32 v33, v2, v3\n"
"v_fmac_f32 v178, v2, v2\n"
"v_fmac_f32 v179, v3, v3\n"
"v_cvt_pk_bf16_f32 v34, v4, v5\n"
"v_fmac_f32 v180, v4, v4\n"
"v_fmac_f32 v181, v5, v5\n"
"v_cvt_pk_bf16_f32 v35, v6, v7\n"
"v_fmac_f32 v182, v6, v6\n"
"v_fmac_f32 v183, v7, v7\n"
"v_mfma_f32_32x32x16_bf16 v[0:15], v[224:227], v[44:47], v[208:223]\n"
"v_cvt_pk_bf16_f32 v36, v16, v17\n"
"v_fmac_f32 v176, v16, v16\n"
"v_fmac_f32 v177, v17, v17\n"
"v_cvt_pk_bf16_f32 v37, v18, v19\n"
"v_fmac_f32 v178, v18, v18\n"
"v_fmac_f32 v179, v19, v19\n"
"v_cvt_pk_bf16_f32 v38, v20, v21\n"
"v_fmac_f32 v180, v20, v20\n"
"v_fmac_f32 v181, v21, v21\n"
"v_cvt_pk_bf16_f32 v39, v22, v23\n"
"v_fmac_f32 v182, v22, v22\n"
"v_fmac_f32 v183, v23, v23\n"
"v_mfma_f32_32x32x16_bf16 v[16:31], v[224:227], v[48:51], v[208:223]\n"
"buffer_load_dwordx4 v235, s[36:39], s60 offen lds\n"
"v_cvt_pk_bf16_f32 v40, v192, v193\n"
"v_fmac_f32 v176, v192, v192\n"
"v_fmac_f32 v177, v193, v193\n"
"v_cvt_pk_bf16_f32 v41, v194, v195\n"
"v_fmac_f32 v178, v194, v194\n"
"v_fmac_f32 v179, v195, v195\n"
"v_cvt_pk_bf16_f32 v42, v196, v197\n"
"v_fmac_f32 v180, v196, v196\n"
"v_fmac_f32 v181, v197, v197\n"
"v_cvt_pk_bf16_f32 v43, v198, v199\n"
"v_fmac_f32 v182, v198, v198\n"
"v_fmac_f32 v183, v199, v199\n"
"v_mfma_f32_32x32x16_bf16 v[192:207], v[224:227], v[52:55], v[208:223]\n"
"v_cvt_pk_bf16_f32 v44, v0, v1\n"
"v_fmac_f32 v176, v0, v0\n"
"v_fmac_f32 v177, v1, v1\n"
"v_cvt_pk_bf16_f32 v45, v2, v3\n"
"v_fmac_f32 v178, v2, v2\n"
"v_fmac_f32 v179, v3, v3\n"
"v_cvt_pk_bf16_f32 v46, v4, v5\n"
"v_fmac_f32 v180, v4, v4\n"
"v_fmac_f32 v181, v5, v5\n"
"v_cvt_pk_bf16_f32 v47, v6, v7\n"
"v_fmac_f32 v182, v6, v6\n"
"v_fmac_f32 v183, v7, v7\n"
"v_mfma_f32_32x32x16_bf16 v[0:15], v[224:227], v[56:59], v[208:223]\n"
"buffer_load_dwordx4 v236, s[36:39], s60 offen offset:1024 lds\n"
"v_cvt_pk_bf16_f32 v48, v16, v17\n"
"v_fmac_f32 v176, v16, v16\n"
"v_fmac_f32 v177, v17, v17\n"
"v_cvt_pk_bf16_f32 v49, v18, v19\n"
"v_fmac_f32 v178, v18, v18\n"
"v_fmac_f32 v179, v19, v19\n"
"v_cvt_pk_bf16_f32 v50, v20, v21\n"
"v_fmac_f32 v180, v20, v20\n"
"v_fmac_f32 v181, v21, v21\n"
"v_cvt_pk_bf16_f32 v51, v22, v23\n"
"v_fmac_f32 v182, v22, v22\n"
"v_fmac_f32 v183, v23, v23\n"
"v_mfma_f32_32x32x16_bf16 v[16:31], v[224:227], v[60:63], v[208:223]\n"
"v_cvt_pk_bf16_f32 v52, v192, v193\n"
"v_fmac_f32 v176, v192, v192\n"
"v_fmac_f32 v177, v193, v193\n"
"v_cvt_pk_bf16_f32 v53, v194, v195\n"
"v_fmac_f32 v178, v194, v194\n"
"v_fmac_f32 v179, v195, v195\n"
"v_cvt_pk_bf16_f32 v54, v196, v197\n"
"v_fmac_f32 v180, v196, v196\n"
"v_fmac_f32 v181, v197, v197\n"
"v_cvt_pk_bf16_f32 v55, v198, v199\n"
"v_fmac_f32 v182, v198, v198\n"
"v_fmac_f32 v183, v199, v199\n"
"v_mfma_f32_32x32x16_bf16 v[192:207], v[224:227], v[64:67], v[208:223]\n"
"buffer_load_dwordx4 v237, s[36:39], s60 offen offset:2048 lds\n"
"v_cvt_pk_bf16_f32 v56, v0, v1\n"
"v_fmac_f32 v176, v0, v0\n"
"v_fmac_f32 v177, v1, v1\n"
"v_cvt_pk_bf16_f32 v57, v2, v3\n"
"v_fmac_f32 v178, v2, v2\n"
"v_fmac_f32 v179, v3, v3\n"
"v_cvt_pk_bf16_f32 v58, v4, v5\n"
"v_fmac_f32 v180, v4, v4\n"
"v_fmac_f32 v181, v5, v5\n"
"v_cvt_pk_bf16_f32 v59, v6, v7\n"
"v_fmac_f32 v182, v6, v6\n"
"v_fmac_f32 v183, v7, v7\n"
"v_mfma_f32_32x32x16_bf16 v[0:15], v[224:227], v[68:71], v[208:223]\n"
"v_cvt_pk_bf16_f32 v60, v16, v17\n"
"v_fmac_f32 v176, v16, v16\n"
"v_fmac_f32 v177, v17, v17\n"
"v_cvt_pk_bf16_f32 v61, v18, v19\n"
"v_fmac_f32 v178, v18, v18\n"
"v_fmac_f32 v179, v19, v19\n"
"v_cvt_pk_bf16_f32 v62, v20, v21\n"
"v_fmac_f32 v180, v20, v20\n"
"v_fmac_f32 v181, v21, v21\n"
"v_cvt_pk_bf16_f32 v63, v22, v23\n"
"v_fmac_f32 v182, v22, v22\n"
"v_fmac_f32 v183, v23, v23\n"
"v_mfma_f32_32x32x16_bf16 v[16:31], v[224:227], v[72:75], v[208:223]\n"
"s_add_u32 m0, s55, 135168\n"
"v_cvt_pk_bf16_f32 v64, v192, v193\n"
"v_fmac_f32 v176, v192, v192\n"
"v_fmac_f32 v177, v193, v193\n"
"v_cvt_pk_bf16_f32 v65, v194, v195\n"
"v_fmac_f32 v178, v194, v194\n"
"v_fmac_f32 v179, v195, v195\n"
"v_cvt_pk_bf16_f32 v66, v196, v197\n"
"v_fmac_f32 v180, v196, v196\n"
"v_fmac_f32 v181, v197, v197\n"
"v_cvt_pk_bf16_f32 v67, v198, v199\n"
"v_fmac_f32 v182, v198, v198\n"
"v_fmac_f32 v183, v199, v199\n"
"v_mfma_f32_32x32x16_bf16 v[192:207], v[224:227], v[76:79], v[208:223]\n"
"v_cvt_pk_bf16_f32 v68, v0, v1\n"
"v_fmac_f32 v176, v0, v0\n"
"v_fmac_f32 v177, v1, v1\n"
"v_cvt_pk_bf16_f32 v69, v2, v3\n"
"v_fmac_f32 v178, v2, v2\n"
"v_fmac_f32 v179, v3, v3\n"
"v_cvt_pk_bf16_f32 v70, v4, v5\n"
"v_fmac_f32 v180, v4, v4\n"
"v_fmac_f32 v181, v5, v5\n"
"v_cvt_pk_bf16_f32 v71, v6, v7\n"
"v_fmac_f32 v182, v6, v6\n"
"v_fmac_f32 v183, v7, v7\n"
"v_mfma_f32_32x32x16_bf16 v[0:15], v[224:227], v[80:83], v[208:223]\n"
"s_mov_b32 s60, 331776\n"
"v_cvt_pk_bf16_f32 v72, v16, v17\n"
"v_fmac_f32 v176, v16, v16\n"
"v_fmac_f32 v177, v17, v17\n"
"v_cvt_pk_bf16_f32 v73, v18, v19\n"
"v_fmac_f32 v178, v18, v18\n"
"v_fmac_f32 v179, v19, v19\n"
"v_cvt_pk_bf16_f32 v74, v20, v21\n"
"v_fmac_f32 v180, v20, v20\n"
"v_fmac_f32 v181, v21, v21\n"
"v_cvt_pk_bf16_f32 v75, v22, v23\n"
"v_fmac_f32 v182, v22, v22\n"
"v_fmac_f32 v183, v23, v23\n"
"v_mfma_f32_32x32x16_bf16 v[16:31], v[224:227], v[84:87], v[208:223]\n"
"v_cvt_pk_bf16_f32 v76, v192, v193\n"
"v_fmac_f32 v176, v192, v192\n"
"v_fmac_f32 v177, v193, v193\n"
"v_cvt_pk_bf16_f32 v77, v194, v195\n"
"v_fmac_f32 v178, v194, v194\n"
"v_fmac_f32 v179, v195, v195\n"
"v_cvt_pk_bf16_f32 v78, v196, v197\n"
"v_fmac_f32 v180, v196, v196\n"
"v_fmac_f32 v181, v197, v197\n"
"v_cvt_pk_bf16_f32 v79, v198, v199\n"
"v_fmac_f32 v182, v198, v198\n"
"v_fmac_f32 v183, v199, v199\n"
"v_mfma_f32_32x32x16_bf16 v[192:207], v[224:227], v[88:91], v[208:223]\n"
"buffer_load_dwordx4 v235, s[36:39], s60 offen lds\n"
"v_cvt_pk_bf16_f32 v80, v0, v1\n"
"v_fmac_f32 v176, v0, v0\n"
"v_fmac_f32 v177, v1, v1\n"
"v_cvt_pk_bf16_f32 v81, v2, v3\n"
"v_fmac_f32 v178, v2, v2\n"
"v_fmac_f32 v179, v3, v3\n"
"v_cvt_pk_bf16_f32 v82, v4, v5\n"
"v_fmac_f32 v180, v4, v4\n"
"v_fmac_f32 v181, v5, v5\n"
"v_cvt_pk_bf16_f32 v83, v6, v7\n"
"v_fmac_f32 v182, v6, v6\n"
"v_fmac_f32 v183, v7, v7\n"
"v_mfma_f32_32x32x16_bf16 v[0:15], v[224:227], v[92:95], v[208:223]\n"
"v_cvt_pk_bf16_f32 v84, v16, v17\n"
"v_fmac_f32 v176, v16, v16\n"
"v_fmac_f32 v177, v17, v17\n"
"v_cvt_pk_bf16_f32 v85, v18, v19\n"
"v_fmac_f32 v178, v18, v18\n"
"v_fmac_f32 v179, v19, v19\n"
"v_cvt_pk_bf16_f32 v86, v20, v21\n"
"v_fmac_f32 v180, v20, v20\n"
"v_fmac_f32 v181, v21, v21\n"
"v_cvt_pk_bf16_f32 v87, v22, v23\n"
"v_fmac_f32 v182, v22, v22\n"
"v_fmac_f32 v183, v23, v23\n"
"v_mfma_f32_32x32x16_bf16 v[16:31], v[224:227], v[96:99], v[208:223]\n"
"buffer_load_dwordx4 v236, s[36:39], s60 offen offset:1024 lds\n"
"v_cvt_pk_bf16_f32 v88, v192, v193\n"
"v_fmac_f32 v176, v192, v192\n"
"v_fmac_f32 v177, v193, v193\n"
"v_cvt_pk_bf16_f32 v89, v194, v195\n"
"v_fmac_f32 v178, v194, v194\n"
"v_fmac_f32 v179, v195, v195\n"
"v_cvt_pk_bf16_f32 v90, v196, v197\n"
"v_fmac_f32 v180, v196, v196\n"
"v_fmac_f32 v181, v197, v197\n"
"v_cvt_pk_bf16_f32 v91, v198, v199\n"
"v_fmac_f32 v182, v198, v198\n"
"v_fmac_f32 v183, v199, v199\n"
"v_mfma_f32_32x32x16_bf16 v[192:207], v[224:227], v[100:103], v[208:223]\n"
"v_cvt_pk_bf16_f32 v92, v0, v1\n"
"v_fmac_f32 v176, v0, v0\n"
"v_fmac_f32 v177, v1, v1\n"
"v_cvt_pk_bf16_f32 v93, v2, v3\n"
"v_fmac_f32 v178, v2, v2\n"
"v_fmac_f32 v179, v3, v3\n"
"v_cvt_pk_bf16_f32 v94, v4, v5\n"
"v_fmac_f32 v180, v4, v4\n"
"v_fmac_f32 v181, v5, v5\n"
"v_cvt_pk_bf16_f32 v95, v6, v7\n"
"v_fmac_f32 v182, v6, v6\n"
"v_fmac_f32 v183, v7, v7\n"
"v_mfma_f32_32x32x16_bf16 v[0:15], v[224:227], v[104:107], v[208:223]\n"
"buffer_load_dwordx4 v237, s[36:39], s60 offen offset:2048 lds\n"
"v_cvt_pk_bf16_f32 v96, v16, v17\n"
"v_fmac_f32 v176, v16, v16\n"
"v_fmac_f32 v177, v17, v17\n"
"v_cvt_pk_bf16_f32 v97, v18, v19\n"
"v_fmac_f32 v178, v18, v18\n"
"v_fmac_f32 v179, v19, v19\n"
"v_cvt_pk_bf16_f32 v98, v20, v21\n"
"v_fmac_f32 v180, v20, v20\n"
"v_fmac_f32 v181, v21, v21\n"
"v_cvt_pk_bf16_f32 v99, v22, v23\n"
"v_fmac_f32 v182, v22, v22\n"
"v_fmac_f32 v183, v23, v23\n"
"v_mfma_f32_32x32x16_bf16 v[16:31], v[224:227], v[108:111], v[208:223]\n"
"v_cvt_pk_bf16_f32 v100, v192, v193\n"
"v_fmac_f32 v176, v192, v192\n"
"v_fmac_f32 v177, v193, v193\n"
"v_cvt_pk_bf16_f32 v101, v194, v195\n"
"v_fmac_f32 v178, v194, v194\n"
"v_fmac_f32 v179, v195, v195\n"
"v_cvt_pk_bf16_f32 v102, v196, v197\n"
"v_fmac_f32 v180, v196, v196\n"
"v_fmac_f32 v181, v197, v197\n"
"v_cvt_pk_bf16_f32 v103, v198, v199\n"
"v_fmac_f32 v182, v198, v198\n"
"v_fmac_f32 v183, v199, v199\n"
"v_mfma_f32_32x32x16_bf16 v[192:207], v[224:227], v[112:115], v[208:223]\n"
"s_add_u32 m0, s55, 73728\n"
"v_cvt_pk_bf16_f32 v104, v0, v1\n"
"v_fmac_f32 v176, v0, v0\n"
"v_fmac_f32 v177, v1, v1\n"
"v_cvt_pk_bf16_f32 v105, v2, v3\n"
"v_fmac_f32 v178, v2, v2\n"
"v_fmac_f32 v179, v3, v3\n"
"v_cvt_pk_bf16_f32 v106, v4, v5\n"
"v_fmac_f32 v180, v4, v4\n"
"v_fmac_f32 v181, v5, v5\n"
"v_cvt_pk_bf16_f32 v107, v6, v7\n"
"v_fmac_f32 v182, v6, v6\n"
"v_fmac_f32 v183, v7, v7\n"
"v_mfma_f32_32x32x16_bf16 v[0:15], v[224:227], v[116:119], v[208:223]\n"
"v_cvt_pk_bf16_f32 v108, v16, v17\n"
"v_fmac_f32 v176, v16, v16\n"
"v_fmac_f32 v177, v17, v17\n"
"v_cvt_pk_bf16_f32 v109, v18, v19\n"
"v_fmac_f32 v178, v18, v18\n"
"v_fmac_f32 v179, v19, v19\n"
"v_cvt_pk_bf16_f32 v110, v20, v21\n"
"v_fmac_f32 v180, v20, v20\n"
"v_fmac_f32 v181, v21, v21\n"
"v_cvt_pk_bf16_f32 v111, v22, v23\n"
"v_fmac_f32 v182, v22, v22\n"
"v_fmac_f32 v183, v23, v23\n"
"v_mfma_f32_32x32x16_bf16 v[16:31], v[224:227], v[120:123], v[208:223]\n"
"s_mov_b32 s60, 497664\n"
"v_cvt_pk_bf16_f32 v112, v192, v193\n"
"v_fmac_f32 v176, v192, v192\n"
"v_fmac_f32 v177, v193, v193\n"
"v_cvt_pk_bf16_f32 v113, v194, v195\n"
"v_fmac_f32 v178, v194, v194\n"
"v_fmac_f32 v179, v195, v195\n"
"v_cvt_pk_bf16_f32 v114, v196, v197\n"
"v_fmac_f32 v180, v196, v196\n"
"v_fmac_f32 v181, v197, v197\n"
"v_cvt_pk_bf16_f32 v115, v198, v199\n"
"v_fmac_f32 v182, v198, v198\n"
"v_fmac_f32 v183, v199, v199\n"
"v_mfma_f32_32x32x16_bf16 v[192:207], v[224:227], v[124:127], v[208:223]\n"
"v_cvt_pk_bf16_f32 v116, v0, v1\n"
"v_fmac_f32 v176, v0, v0\n"
"v_fmac_f32 v177, v1, v1\n"
"v_cvt_pk_bf16_f32 v117, v2, v3\n"
"v_fmac_f32 v178, v2, v2\n"
"v_fmac_f32 v179, v3, v3\n"
"v_cvt_pk_bf16_f32 v118, v4, v5\n"
"v_fmac_f32 v180, v4, v4\n"
"v_fmac_f32 v181, v5, v5\n"
"v_cvt_pk_bf16_f32 v119, v6, v7\n"
"v_fmac_f32 v182, v6, v6\n"
"v_fmac_f32 v183, v7, v7\n"
"v_mfma_f32_32x32x16_bf16 v[0:15], v[224:227], v[128:131], v[208:223]\n"
"buffer_load_dwordx4 v235, s[36:39], s60 offen lds\n"
"v_cvt_pk_bf16_f32 v120, v16, v17\n"
"v_fmac_f32 v176, v16, v16\n"
"v_fmac_f32 v177, v17, v17\n"
"v_cvt_pk_bf16_f32 v121, v18, v19\n"
"v_fmac_f32 v178, v18, v18\n"
"v_fmac_f32 v179, v19, v19\n"
"v_cvt_pk_bf16_f32 v122, v20, v21\n"
"v_fmac_f32 v180, v20, v20\n"
"v_fmac_f32 v181, v21, v21\n"
"v_cvt_pk_bf16_f32 v123, v22, v23\n"
"v_fmac_f32 v182, v22, v22\n"
"v_fmac_f32 v183, v23, v23\n"
"v_mfma_f32_32x32x16_bf16 v[16:31], v[224:227], v[132:135], v[208:223]\n"
"v_cvt_pk_bf16_f32 v124, v192, v193\n"
"v_fmac_f32 v176, v192, v192\n"
"v_fmac_f32 v177, v193, v193\n"
"v_cvt_pk_bf16_f32 v125, v194, v195\n"
"v_fmac_f32 v178, v194, v194\n"
"v_fmac_f32 v179, v195, v195\n"
"v_cvt_pk_bf16_f32 v126, v196, v197\n"
"v_fmac_f32 v180, v196, v196\n"
"v_fmac_f32 v181, v197, v197\n"
"v_cvt_pk_bf16_f32 v127, v198, v199\n"
"v_fmac_f32 v182, v198, v198\n"
"v_fmac_f32 v183, v199, v199\n"
"v_mfma_f32_32x32x16_bf16 v[192:207], v[224:227], v[136:139], v[208:223]\n"
"buffer_load_dwordx4 v236, s[36:39], s60 offen offset:1024 lds\n"
"v_cvt_pk_bf16_f32 v128, v0, v1\n"
"v_fmac_f32 v176, v0, v0\n"
"v_fmac_f32 v177, v1, v1\n"
"v_cvt_pk_bf16_f32 v129, v2, v3\n"
"v_fmac_f32 v178, v2, v2\n"
"v_fmac_f32 v179, v3, v3\n"
"v_cvt_pk_bf16_f32 v130, v4, v5\n"
"v_fmac_f32 v180, v4, v4\n"
"v_fmac_f32 v181, v5, v5\n"
"v_cvt_pk_bf16_f32 v131, v6, v7\n"
"v_fmac_f32 v182, v6, v6\n"
"v_fmac_f32 v183, v7, v7\n"
"v_mfma_f32_32x32x16_bf16 v[0:15], v[224:227], v[140:143], v[208:223]\n"
"v_cvt_pk_bf16_f32 v132, v16, v17\n"
"v_fmac_f32 v176, v16, v16\n"
"v_fmac_f32 v177, v17, v17\n"
"v_cvt_pk_bf16_f32 v133, v18, v19\n"
"v_fmac_f32 v178, v18, v18\n"
"v_fmac_f32 v179, v19, v19\n"
"v_cvt_pk_bf16_f32 v134, v20, v21\n"
"v_fmac_f32 v180, v20, v20\n"
"v_fmac_f32 v181, v21, v21\n"
"v_cvt_pk_bf16_f32 v135, v22, v23\n"
"v_fmac_f32 v182, v22, v22\n"
"v_fmac_f32 v183, v23, v23\n"
"v_mfma_f32_32x32x16_bf16 v[16:31], v[224:227], v[144:147], v[208:223]\n"
"buffer_load_dwordx4 v237, s[36:39], s60 offen offset:2048 lds\n"
"v_cvt_pk_bf16_f32 v136, v192, v193\n"
"v_fmac_f32 v176, v192, v192\n"
"v_fmac_f32 v177, v193, v193\n"
"v_cvt_pk_bf16_f32 v137, v194, v195\n"
"v_fmac_f32 v178, v194, v194\n"
"v_fmac_f32 v179, v195, v195\n"
"v_cvt_pk_bf16_f32 v138, v196, v197\n"
"v_fmac_f32 v180, v196, v196\n"
"v_fmac_f32 v181, v197, v197\n"
"v_cvt_pk_bf16_f32 v139, v198, v199\n"
"v_fmac_f32 v182, v198, v198\n"
"v_fmac_f32 v183, v199, v199\n"
"v_mfma_f32_32x32x16_bf16 v[192:207], v[224:227], v[148:151], v[208:223]\n"
"v_cvt_pk_bf16_f32 v140, v0, v1\n"
"v_fmac_f32 v176, v0, v0\n"
"v_fmac_f32 v177, v1, v1\n"
"v_cvt_pk_bf16_f32 v141, v2, v3\n"
"v_fmac_f32 v178, v2, v2\n"
"v_fmac_f32 v179, v3, v3\n"
"v_cvt_pk_bf16_f32 v142, v4, v5\n"
"v_fmac_f32 v180, v4, v4\n"
"v_fmac_f32 v181, v5, v5\n"
"v_cvt_pk_bf16_f32 v143, v6, v7\n"
"v_fmac_f32 v182, v6, v6\n"
"v_fmac_f32 v183, v7, v7\n"
"v_mfma_f32_32x32x16_bf16 v[0:15], v[224:227], v[152:155], v[208:223]\n"
"v_cvt_pk_bf16_f32 v144, v16, v17\n"
"v_fmac_f32 v176, v16, v16\n"
"v_fmac_f32 v177, v17, v17\n"
"v_cvt_pk_bf16_f32 v145, v18, v19\n"
"v_fmac_f32 v178, v18, v18\n"
"v_fmac_f32 v179, v19, v19\n"
"v_cvt_pk_bf16_f32 v146, v20, v21\n"
"v_fmac_f32 v180, v20, v20\n"
"v_fmac_f32 v181, v21, v21\n"
"v_cvt_pk_bf16_f32 v147, v22, v23\n"
"v_fmac_f32 v182, v22, v22\n"
"v_fmac_f32 v183, v23, v23\n"
"v_mfma_f32_32x32x16_bf16 v[16:31], v[224:227], v[156:159], v[208:223]\n"
"v_cvt_pk_bf16_f32 v148, v192, v193\n"
"v_fmac_f32 v176, v192, v192\n"
"v_fmac_f32 v177, v193, v193\n"
"v_cvt_pk_bf16_f32 v149, v194, v195\n"
"v_fmac_f32 v178, v194, v194\n"
"v_fmac_f32 v179, v195, v195\n"
"v_cvt_pk_bf16_f32 v150, v196, v197\n"
"v_fmac_f32 v180, v196, v196\n"
"v_fmac_f32 v181, v197, v197\n"
"v_cvt_pk_bf16_f32 v151, v198, v199\n"
"v_fmac_f32 v182, v198, v198\n"
"v_fmac_f32 v183, v199, v199\n"
"v_mfma_f32_32x32x16_bf16 v[192:207], v[224:227], v[160:163], v[208:223]\n"
"v_cvt_pk_bf16_f32 v152, v0, v1\n"
"v_fmac_f32 v176, v0, v0\n"
"v_fmac_f32 v177, v1, v1\n"
"v_cvt_pk_bf16_f32 v153, v2, v3\n"
"v_fmac_f32 v178, v2, v2\n"
"v_fmac_f32 v179, v3, v3\n"
"v_cvt_pk_bf16_f32 v154, v4, v5\n"
"v_fmac_f32 v180, v4, v4\n"
"v_fmac_f32 v181, v5, v5\n"
"v_cvt_pk_bf16_f32 v155, v6, v7\n"
"v_fmac_f32 v182, v6, v6\n"
"v_fmac_f32 v183, v7, v7\n"
"v_mfma_f32_32x32x16_bf16 v[0:15], v[224:227], v[164:167], v[208:223]\n"
"v_cvt_pk_bf16_f32 v156, v16, v17\n"
"v_fmac_f32 v176, v16, v16\n"
"v_fmac_f32 v177, v17, v17\n"
"v_cvt_pk_bf16_f32 v157, v18, v19\n"
"v_fmac_f32 v178, v18, v18\n"
"v_fmac_f32 v179, v19, v19\n"
"v_cvt_pk_bf16_f32 v158, v20, v21\n"
"v_fmac_f32 v180, v20, v20\n"
"v_fmac_f32 v181, v21, v21\n"
"v_cvt_pk_bf16_f32 v159, v22, v23\n"
"v_fmac_f32 v182, v22, v22\n"
"v_fmac_f32 v183, v23, v23\n"
"v_mfma_f32_32x32x16_bf16 v[16:31], v[224:227], v[168:171], v[208:223]\n"
"v_cvt_pk_bf16_f32 v160, v192, v193\n"
"v_fmac_f32 v176, v192, v192\n"
"v_fmac_f32 v177, v193, v193\n"
"v_cvt_pk_bf16_f32 v161, v194, v195\n"
"v_fmac_f32 v178, v194, v194\n"
"v_fmac_f32 v179, v195, v195\n"
"v_cvt_pk_bf16_f32 v162, v196, v197\n"
"v_fmac_f32 v180, v196, v196\n"
"v_fmac_f32 v181, v197, v197\n"
"v_cvt_pk_bf16_f32 v163, v198, v199\n"
"v_fmac_f32 v182, v198, v198\n"
"v_fmac_f32 v183, v199, v199\n"
"v_mfma_f32_32x32x16_bf16 v[192:207], v[224:227], v[172:175], v[208:223]\n"
"v_cvt_pk_bf16_f32 v164, v0, v1\n"
"v_fmac_f32 v176, v0, v0\n"
"v_fmac_f32 v177, v1, v1\n"
"v_cvt_pk_bf16_f32 v165, v2, v3\n"
"v_fmac_f32 v178, v2, v2\n"
"v_fmac_f32 v179, v3, v3\n"
"v_cvt_pk_bf16_f32 v166, v4, v5\n"
"v_fmac_f32 v180, v4, v4\n"
"v_fmac_f32 v181, v5, v5\n"
"v_cvt_pk_bf16_f32 v167, v6, v7\n"
"v_fmac_f32 v182, v6, v6\n"
"v_fmac_f32 v183, v7, v7\n"
"v_cvt_pk_bf16_f32 v168, v16, v17\n"
"v_fmac_f32 v176, v16, v16\n"
"v_fmac_f32 v177, v17, v17\n"
"v_cvt_pk_bf16_f32 v169, v18, v19\n"
"v_fmac_f32 v178, v18, v18\n"
"v_fmac_f32 v179, v19, v19\n"
"v_cvt_pk_bf16_f32 v170, v20, v21\n"
"v_fmac_f32 v180, v20, v20\n"
"v_fmac_f32 v181, v21, v21\n"
"v_cvt_pk_bf16_f32 v171, v22, v23\n"
"v_fmac_f32 v182, v22, v22\n"
"v_fmac_f32 v183, v23, v23\n"
"v_cvt_pk_bf16_f32 v172, v192, v193\n"
"v_fmac_f32 v176, v192, v192\n"
"v_fmac_f32 v177, v193, v193\n"
"v_cvt_pk_bf16_f32 v173, v194, v195\n"
"v_fmac_f32 v178, v194, v194\n"
"v_fmac_f32 v179, v195, v195\n"
"v_cvt_pk_bf16_f32 v174, v196, v197\n"
"v_fmac_f32 v180, v196, v196\n"
"v_fmac_f32 v181, v197, v197\n"
"v_cvt_pk_bf16_f32 v175, v198, v199\n"
"v_fmac_f32 v182, v198, v198\n"
"v_fmac_f32 v183, v199, v199\n"
"v_add_f32 v176, v176, v177\n"
"v_add_f32 v178, v178, v179\n"
"v_add_f32 v180, v180, v181\n"
"v_add_f32 v182, v182, v183\n"
"v_add_f32 v176, v176, v178\n"
"v_add_f32 v180, v180, v182\n"
"v_add_f32 v253, v176, v180\n"
"s_waitcnt vmcnt(9)\n"
"s_barrier\n"
"ds_read_b64_tr_b16 v[208:209], v244\n"
"ds_read_b64_tr_b16 v[210:211], v244 offset:2048\n"
"ds_read_b64_tr_b16 v[212:213], v245\n"
"ds_read_b64_tr_b16 v[214:215], v245 offset:2048\n"
"ds_read_b64_tr_b16 v[216:217], v246\n"
"ds_read_b64_tr_b16 v[218:219], v246 offset:2048\n"
"s_add_u32 m0, s55, 86016\n"
"ds_read_b64_tr_b16 v[220:221], v247\n"
"ds_read_b64_tr_b16 v[222:223], v247 offset:2048\n"
"s_waitcnt lgkmcnt(4)\n"
"v_mfma_f32_32x32x16_bf16 v[0:15], v[208:211], v[32:35], 0\n"
"ds_read_b64_tr_b16 v[224:225], v244 offset:4096\n"
"ds_read_b64_tr_b16 v[226:227], v244 offset:6144\n"
"s_mov_b32 s60, 663552\n"
"v_mfma_f32_32x32x16_bf16 v[16:31], v[212:215], v[32:35], 0\n"
"ds_read_b64_tr_b16 v[208:209], v245 offset:4096\n"
"ds_read_b64_tr_b16 v[210:211], v245 offset:6144\n"
"s_waitcnt lgkmcnt(4)\n"
"v_mfma_f32_32x32x16_bf16 v[176:191], v[216:219], v[32:35], 0\n"
"ds_read_b64_tr_b16 v[212:213], v246 offset:4096\n"
"ds_read_b64_tr_b16 v[214:215], v246 offset:6144\n"
"buffer_load_dwordx4 v235, s[36:39], s60 offen lds\n"
"v_mfma_f32_32x32x16_bf16 v[192:207], v[220:223], v[32:35], 0\n"
"ds_read_b64_tr_b16 v[216:217], v247 offset:4096\n"
"ds_read_b64_tr_b16 v[218:219], v247 offset:6144\n"
"s_waitcnt lgkmcnt(4)\n"
"v_mfma_f32_32x32x16_bf16 v[0:15], v[224:227], v[36:39], v[0:15]\n"
"ds_read_b64_tr_b16 v[220:221], v244 offset:8192\n"
"ds_read_b64_tr_b16 v[222:223], v244 offset:10240\n"
"buffer_load_dwordx4 v236, s[36:39], s60 offen offset:1024 lds\n"
"v_mov_b32 v250, v253\n"
"v_mov_b32_e64 v251, v253\n"
"v_mfma_f32_32x32x16_bf16 v[16:31], v[208:211], v[36:39], v[16:31]\n"
"ds_read_b64_tr_b16 v[224:225], v245 offset:8192\n"
"ds_read_b64_tr_b16 v[226:227], v245 offset:10240\n"
"s_waitcnt lgkmcnt(4)\n"
"v_mfma_f32_32x32x16_bf16 v[176:191], v[212:215], v[36:39], v[176:191]\n"
"ds_read_b64_tr_b16 v[208:209], v246 offset:8192\n"
"ds_read_b64_tr_b16 v[210:211], v246 offset:10240\n"
"buffer_load_dwordx4 v237, s[36:39], s60 offen offset:2048 lds\n"
"v_mfma_f32_32x32x16_bf16 v[192:207], v[216:219], v[36:39], v[192:207]\n"
"s_waitcnt vmcnt(9)\n"
"ds_read_b64_tr_b16 v[212:213], v247 offset:8192\n"
"ds_read_b64_tr_b16 v[214:215], v247 offset:10240\n"
"v_mul_f32 v252, v249, v249\n"
"s_waitcnt lgkmcnt(4)\n"
"v_mfma_f32_32x32x16_bf16 v[0:15], v[220:223], v[40:43], v[0:15]\n"
"s_barrier\n"
"ds_read_b64_tr_b16 v[216:217], v244 offset:12288\n"
"ds_read_b64_tr_b16 v[218:219], v244 offset:14336\n"
"v_mfma_f32_32x32x16_bf16 v[16:31], v[224:227], v[40:43], v[16:31]\n"
"ds_read_b64_tr_b16 v[220:221], v245 offset:12288\n"
"ds_read_b64_tr_b16 v[222:223], v245 offset:14336\n"
"s_waitcnt lgkmcnt(4)\n"
"v_mfma_f32_32x32x16_bf16 v[176:191], v[208:211], v[40:43], v[176:191]\n"
"ds_read_b64_tr_b16 v[224:225], v246 offset:12288\n"
"ds_read_b64_tr_b16 v[226:227], v246 offset:14336\n"
"s_add_u32 m0, s55, 98304\n"
"v_permlane32_swap_b32 v250, v251\n"
"v_mfma_f32_32x32x16_bf16 v[192:207], v[212:215], v[40:43], v[192:207]\n"
"ds_read_b64_tr_b16 v[208:209], v247 offset:12288\n"
"ds_read_b64_tr_b16 v[210:211], v247 offset:14336\n"
"s_waitcnt lgkmcnt(4)\n"
"v_mfma_f32_32x32x16_bf16 v[0:15], v[216:219], v[44:47], v[0:15]\n"
"ds_read_b64_tr_b16 v[212:213], v244 offset:16384\n"
"ds_read_b64_tr_b16 v[214:215], v244 offset:18432\n"
"s_mov_b32 s60, 829440\n"
"v_mfma_f32_32x32x16_bf16 v[16:31], v[220:223], v[44:47], v[16:31]\n"
"ds_read_b64_tr_b16 v[216:217], v245 offset:16384\n"
"ds_read_b64_tr_b16 v[218:219], v245 offset:18432\n"
"v_add_f32 v250, v250, v251\n"
"s_waitcnt lgkmcnt(4)\n"
"v_mfma_f32_32x32x16_bf16 v[176:191], v[224:227], v[44:47], v[176:191]\n"
"ds_read_b64_tr_b16 v[220:221], v246 offset:16384\n"
"ds_read_b64_tr_b16 v[222:223], v246 offset:18432\n"
"buffer_load_dwordx4 v235, s[36:39], s60 offen lds\n"
"v_mfma_f32_32x32x16_bf16 v[192:207], v[208:211], v[44:47], v[192:207]\n"
"ds_read_b64_tr_b16 v[224:225], v247 offset:16384\n"
"ds_read_b64_tr_b16 v[226:227], v247 offset:18432\n"
"s_waitcnt lgkmcnt(4)\n"
"v_mfma_f32_32x32x16_bf16 v[0:15], v[212:215], v[48:51], v[0:15]\n"
"ds_read_b64_tr_b16 v[208:209], v244 offset:20480\n"
"ds_read_b64_tr_b16 v[210:211], v244 offset:22528\n"
"buffer_load_dwordx4 v236, s[36:39], s60 offen offset:1024 lds\n"
"v_mul_f32 v250, 0x3ae38e39, v250\n"
"v_mfma_f32_32x32x16_bf16 v[16:31], v[216:219], v[48:51], v[16:31]\n"
"ds_read_b64_tr_b16 v[212:213], v245 offset:20480\n"
"ds_read_b64_tr_b16 v[214:215], v245 offset:22528\n"
"s_waitcnt lgkmcnt(4)\n"
"v_mfma_f32_32x32x16_bf16 v[176:191], v[220:223], v[48:51], v[176:191]\n"
"ds_read_b64_tr_b16 v[216:217], v246 offset:20480\n"
"ds_read_b64_tr_b16 v[218:219], v246 offset:22528\n"
"buffer_load_dwordx4 v237, s[36:39], s60 offen offset:2048 lds\n"
"v_mfma_f32_32x32x16_bf16 v[192:207], v[224:227], v[48:51], v[192:207]\n"
"s_waitcnt vmcnt(9)\n"
"ds_read_b64_tr_b16 v[220:221], v247 offset:20480\n"
"ds_read_b64_tr_b16 v[222:223], v247 offset:22528\n"
"v_sub_f32 v250, v250, v252\n"
"s_waitcnt lgkmcnt(4)\n"
"v_mfma_f32_32x32x16_bf16 v[0:15], v[208:211], v[52:55], v[0:15]\n"
"s_barrier\n"
"ds_read_b64_tr_b16 v[224:225], v244 offset:24576\n"
"ds_read_b64_tr_b16 v[226:227], v244 offset:26624\n"
"v_mfma_f32_32x32x16_bf16 v[16:31], v[212:215], v[52:55], v[16:31]\n"
"ds_read_b64_tr_b16 v[208:209], v245 offset:24576\n"
"ds_read_b64_tr_b16 v[210:211], v245 offset:26624\n"
"s_waitcnt lgkmcnt(4)\n"
"v_mfma_f32_32x32x16_bf16 v[176:191], v[216:219], v[52:55], v[176:191]\n"
"ds_read_b64_tr_b16 v[212:213], v246 offset:24576\n"
"ds_read_b64_tr_b16 v[214:215], v246 offset:26624\n"
"s_add_u32 m0, s55, 110592\n"
"v_max_f32 v250, 0, v250\n"
"v_mfma_f32_32x32x16_bf16 v[192:207], v[220:223], v[52:55], v[192:207]\n"
"ds_read_b64_tr_b16 v[216:217], v247 offset:24576\n"
"ds_read_b64_tr_b16 v[218:219], v247 offset:26624\n"
"s_waitcnt lgkmcnt(4)\n"
"v_mfma_f32_32x32x16_bf16 v[0:15], v[224:227], v[56:59], v[0:15]\n"
"ds_read_b64_tr_b16 v[220:221], v244 offset:28672\n"
"ds_read_b64_tr_b16 v[222:223], v244 offset:30720\n"
"s_mov_b32 s60, 995328\n"
"v_mfma_f32_32x32x16_bf16 v[16:31], v[208:211], v[56:59], v[16:31]\n"
"ds_read_b64_tr_b16 v[224:225], v245 offset:28672\n"
"ds_read_b64_tr_b16 v[226:227], v245 offset:30720\n"
"v_cvt_pk_bf16_f32 v250, v250, v250\n"
"s_waitcnt lgkmcnt(4)\n"
"v_mfma_f32_32x32x16_bf16 v[176:191], v[212:215], v[56:59], v[176:191]\n"
"ds_read_b64_tr_b16 v[208:209], v246 offset:28672\n"
"ds_read_b64_tr_b16 v[210:211], v246 offset:30720\n"
"buffer_load_dwordx4 v235, s[36:39], s60 offen lds\n"
"v_mfma_f32_32x32x16_bf16 v[192:207], v[216:219], v[56:59], v[192:207]\n"
"ds_read_b64_tr_b16 v[212:213], v247 offset:28672\n"
"ds_read_b64_tr_b16 v[214:215], v247 offset:30720\n"
"s_waitcnt lgkmcnt(4)\n"
"v_mfma_f32_32x32x16_bf16 v[0:15], v[220:223], v[60:63], v[0:15]\n"
"ds_read_b64_tr_b16 v[216:217], v244 offset:32768\n"
"ds_read_b64_tr_b16 v[218:219], v244 offset:34816\n"
"buffer_load_dwordx4 v236, s[36:39], s60 offen offset:1024 lds\n"
"v_lshlrev_b32 v250, 16, v250\n"
"v_mfma_f32_32x32x16_bf16 v[16:31], v[224:227], v[60:63], v[16:31]\n"
"ds_read_b64_tr_b16 v[220:221], v245 offset:32768\n"
"ds_read_b64_tr_b16 v[222:223], v245 offset:34816\n"
"s_waitcnt lgkmcnt(4)\n"
"v_mfma_f32_32x32x16_bf16 v[176:191], v[208:211], v[60:63], v[176:191]\n"
"ds_read_b64_tr_b16 v[224:225], v246 offset:32768\n"
"ds_read_b64_tr_b16 v[226:227], v246 offset:34816\n"
"buffer_load_dwordx4 v237, s[36:39], s60 offen offset:2048 lds\n"
"v_mfma_f32_32x32x16_bf16 v[192:207], v[212:215], v[60:63], v[192:207]\n"
"s_waitcnt vmcnt(9)\n"
"ds_read_b64_tr_b16 v[208:209], v247 offset:32768\n"
"ds_read_b64_tr_b16 v[210:211], v247 offset:34816\n"
"v_add_f32 v250, 0x3727c5ac, v250\n"
"s_waitcnt lgkmcnt(4)\n"
"v_mfma_f32_32x32x16_bf16 v[0:15], v[216:219], v[64:67], v[0:15]\n"
"s_barrier\n"
"v_subrev_u32 v244, 36864, v244\n"
"v_subrev_u32 v245, 36864, v245\n"
"v_subrev_u32 v246, 36864, v246\n"
"v_subrev_u32 v247, 36864, v247\n"
"ds_read_b64_tr_b16 v[212:213], v244\n"
"ds_read_b64_tr_b16 v[214:215], v244 offset:2048\n"
"v_mfma_f32_32x32x16_bf16 v[16:31], v[220:223], v[64:67], v[16:31]\n"
"ds_read_b64_tr_b16 v[216:217], v245\n"
"ds_read_b64_tr_b16 v[218:219], v245 offset:2048\n"
"s_waitcnt lgkmcnt(4)\n"
"v_mfma_f32_32x32x16_bf16 v[176:191], v[224:227], v[64:67], v[176:191]\n"
"ds_read_b64_tr_b16 v[220:221], v246\n"
"ds_read_b64_tr_b16 v[222:223], v246 offset:2048\n"
"s_add_u32 m0, s55, 122880\n"
"v_cvt_pk_bf16_f32 v250, v250, v250\n"
"v_mfma_f32_32x32x16_bf16 v[192:207], v[208:211], v[64:67], v[192:207]\n"
"ds_read_b64_tr_b16 v[224:225], v247\n"
"ds_read_b64_tr_b16 v[226:227], v247 offset:2048\n"
"s_waitcnt lgkmcnt(4)\n"
"v_mfma_f32_32x32x16_bf16 v[0:15], v[212:215], v[68:71], v[0:15]\n"
"ds_read_b64_tr_b16 v[208:209], v244 offset:4096\n"
"ds_read_b64_tr_b16 v[210:211], v244 offset:6144\n"
"s_mov_b32 s60, 1161216\n"
"v_mfma_f32_32x32x16_bf16 v[16:31], v[216:219], v[68:71], v[16:31]\n"
"ds_read_b64_tr_b16 v[212:213], v245 offset:4096\n"
"ds_read_b64_tr_b16 v[214:215], v245 offset:6144\n"
"v_lshlrev_b32 v250, 16, v250\n"
"s_waitcnt lgkmcnt(4)\n"
"v_mfma_f32_32x32x16_bf16 v[176:191], v[220:223], v[68:71], v[176:191]\n"
"ds_read_b64_tr_b16 v[216:217], v246 offset:4096\n"
"ds_read_b64_tr_b16 v[218:219], v246 offset:6144\n"
"buffer_load_dwordx4 v235, s[36:39], s60 offen lds\n"
"v_mfma_f32_32x32x16_bf16 v[192:207], v[224:227], v[68:71], v[192:207]\n"
"ds_read_b64_tr_b16 v[220:221], v247 offset:4096\n"
"ds_read_b64_tr_b16 v[222:223], v247 offset:6144\n"
"s_waitcnt lgkmcnt(4)\n"
"v_mfma_f32_32x32x16_bf16 v[0:15], v[208:211], v[72:75], v[0:15]\n"
"ds_read_b64_tr_b16 v[224:225], v244 offset:8192\n"
"ds_read_b64_tr_b16 v[226:227], v244 offset:10240\n"
"buffer_load_dwordx4 v236, s[36:39], s60 offen offset:1024 lds\n"
"v_sqrt_f32 v250, v250\n"
"v_mfma_f32_32x32x16_bf16 v[16:31], v[212:215], v[72:75], v[16:31]\n"
"ds_read_b64_tr_b16 v[208:209], v245 offset:8192\n"
"ds_read_b64_tr_b16 v[210:211], v245 offset:10240\n"
"s_waitcnt lgkmcnt(4)\n"
"v_mfma_f32_32x32x16_bf16 v[176:191], v[216:219], v[72:75], v[176:191]\n"
"ds_read_b64_tr_b16 v[212:213], v246 offset:8192\n"
"ds_read_b64_tr_b16 v[214:215], v246 offset:10240\n"
"buffer_load_dwordx4 v237, s[36:39], s60 offen offset:2048 lds\n"
"v_mfma_f32_32x32x16_bf16 v[192:207], v[220:223], v[72:75], v[192:207]\n"
"s_waitcnt vmcnt(9)\n"
"ds_read_b64_tr_b16 v[216:217], v247 offset:8192\n"
"ds_read_b64_tr_b16 v[218:219], v247 offset:10240\n"
"s_waitcnt lgkmcnt(4)\n"
"v_mfma_f32_32x32x16_bf16 v[0:15], v[224:227], v[76:79], v[0:15]\n"
"s_barrier\n"
"ds_read_b64_tr_b16 v[220:221], v244 offset:12288\n"
"ds_read_b64_tr_b16 v[222:223], v244 offset:14336\n"
"v_mfma_f32_32x32x16_bf16 v[16:31], v[208:211], v[76:79], v[16:31]\n"
"ds_read_b64_tr_b16 v[224:225], v245 offset:12288\n"
"ds_read_b64_tr_b16 v[226:227], v245 offset:14336\n"
"s_waitcnt lgkmcnt(4)\n"
"v_mfma_f32_32x32x16_bf16 v[176:191], v[212:215], v[76:79], v[176:191]\n"
"ds_read_b64_tr_b16 v[208:209], v246 offset:12288\n"
"ds_read_b64_tr_b16 v[210:211], v246 offset:14336\n"
"s_add_u32 m0, s55, 135168\n"
"v_cvt_pk_bf16_f32 v250, v250, v250\n"
"v_mfma_f32_32x32x16_bf16 v[192:207], v[216:219], v[76:79], v[192:207]\n"
"ds_read_b64_tr_b16 v[212:213], v247 offset:12288\n"
"ds_read_b64_tr_b16 v[214:215], v247 offset:14336\n"
"s_waitcnt lgkmcnt(4)\n"
"v_mfma_f32_32x32x16_bf16 v[0:15], v[220:223], v[80:83], v[0:15]\n"
"ds_read_b64_tr_b16 v[216:217], v244 offset:16384\n"
"ds_read_b64_tr_b16 v[218:219], v244 offset:18432\n"
"s_mov_b32 s60, 1327104\n"
"v_mfma_f32_32x32x16_bf16 v[16:31], v[224:227], v[80:83], v[16:31]\n"
"ds_read_b64_tr_b16 v[220:221], v245 offset:16384\n"
"ds_read_b64_tr_b16 v[222:223], v245 offset:18432\n"
"v_lshlrev_b32 v249, 16, v250\n"
"s_waitcnt lgkmcnt(4)\n"
"v_mfma_f32_32x32x16_bf16 v[176:191], v[208:211], v[80:83], v[176:191]\n"
"ds_read_b64_tr_b16 v[224:225], v246 offset:16384\n"
"ds_read_b64_tr_b16 v[226:227], v246 offset:18432\n"
"buffer_load_dwordx4 v235, s[36:39], s60 offen lds\n"
"v_mfma_f32_32x32x16_bf16 v[192:207], v[212:215], v[80:83], v[192:207]\n"
"ds_read_b64_tr_b16 v[208:209], v247 offset:16384\n"
"ds_read_b64_tr_b16 v[210:211], v247 offset:18432\n"
"s_waitcnt lgkmcnt(4)\n"
"v_mfma_f32_32x32x16_bf16 v[0:15], v[216:219], v[84:87], v[0:15]\n"
"ds_read_b64_tr_b16 v[212:213], v244 offset:20480\n"
"ds_read_b64_tr_b16 v[214:215], v244 offset:22528\n"
"buffer_load_dwordx4 v236, s[36:39], s60 offen offset:1024 lds\n"
"v_rcp_f32 v252, v249\n"
"v_mfma_f32_32x32x16_bf16 v[16:31], v[220:223], v[84:87], v[16:31]\n"
"ds_read_b64_tr_b16 v[216:217], v245 offset:20480\n"
"ds_read_b64_tr_b16 v[218:219], v245 offset:22528\n"
"s_waitcnt lgkmcnt(4)\n"
"v_mfma_f32_32x32x16_bf16 v[176:191], v[224:227], v[84:87], v[176:191]\n"
"ds_read_b64_tr_b16 v[220:221], v246 offset:20480\n"
"ds_read_b64_tr_b16 v[222:223], v246 offset:22528\n"
"buffer_load_dwordx4 v237, s[36:39], s60 offen offset:2048 lds\n"
"v_mfma_f32_32x32x16_bf16 v[192:207], v[208:211], v[84:87], v[192:207]\n"
"s_waitcnt vmcnt(9)\n"
"ds_read_b64_tr_b16 v[224:225], v247 offset:20480\n"
"ds_read_b64_tr_b16 v[226:227], v247 offset:22528\n"
"s_waitcnt lgkmcnt(4)\n"
"v_mfma_f32_32x32x16_bf16 v[0:15], v[212:215], v[88:91], v[0:15]\n"
"s_barrier\n"
"ds_read_b64_tr_b16 v[208:209], v244 offset:24576\n"
"ds_read_b64_tr_b16 v[210:211], v244 offset:26624\n"
"v_mfma_f32_32x32x16_bf16 v[16:31], v[216:219], v[88:91], v[16:31]\n"
"ds_read_b64_tr_b16 v[212:213], v245 offset:24576\n"
"ds_read_b64_tr_b16 v[214:215], v245 offset:26624\n"
"s_waitcnt lgkmcnt(4)\n"
"v_mfma_f32_32x32x16_bf16 v[176:191], v[220:223], v[88:91], v[176:191]\n"
"ds_read_b64_tr_b16 v[216:217], v246 offset:24576\n"
"ds_read_b64_tr_b16 v[218:219], v246 offset:26624\n"
"s_add_u32 m0, s55, 73728\n"
"v_mfma_f32_32x32x16_bf16 v[192:207], v[224:227], v[88:91], v[192:207]\n"
"ds_read_b64_tr_b16 v[220:221], v247 offset:24576\n"
"ds_read_b64_tr_b16 v[222:223], v247 offset:26624\n"
"s_waitcnt lgkmcnt(4)\n"
"v_mfma_f32_32x32x16_bf16 v[0:15], v[208:211], v[92:95], v[0:15]\n"
"ds_read_b64_tr_b16 v[224:225], v244 offset:28672\n"
"ds_read_b64_tr_b16 v[226:227], v244 offset:30720\n"
"s_mov_b32 s60, 1492992\n"
"v_mfma_f32_32x32x16_bf16 v[16:31], v[212:215], v[92:95], v[16:31]\n"
"ds_read_b64_tr_b16 v[208:209], v245 offset:28672\n"
"ds_read_b64_tr_b16 v[210:211], v245 offset:30720\n"
"s_waitcnt lgkmcnt(4)\n"
"v_mfma_f32_32x32x16_bf16 v[176:191], v[216:219], v[92:95], v[176:191]\n"
"ds_read_b64_tr_b16 v[212:213], v246 offset:28672\n"
"ds_read_b64_tr_b16 v[214:215], v246 offset:30720\n"
"buffer_load_dwordx4 v235, s[36:39], s60 offen lds\n"
"v_mfma_f32_32x32x16_bf16 v[192:207], v[220:223], v[92:95], v[192:207]\n"
"ds_read_b64_tr_b16 v[216:217], v247 offset:28672\n"
"ds_read_b64_tr_b16 v[218:219], v247 offset:30720\n"
"s_waitcnt lgkmcnt(4)\n"
"v_mfma_f32_32x32x16_bf16 v[0:15], v[224:227], v[96:99], v[0:15]\n"
"ds_read_b64_tr_b16 v[220:221], v244 offset:32768\n"
"ds_read_b64_tr_b16 v[222:223], v244 offset:34816\n"
"buffer_load_dwordx4 v236, s[36:39], s60 offen offset:1024 lds\n"
"v_mfma_f32_32x32x16_bf16 v[16:31], v[208:211], v[96:99], v[16:31]\n"
"ds_read_b64_tr_b16 v[224:225], v245 offset:32768\n"
"ds_read_b64_tr_b16 v[226:227], v245 offset:34816\n"
"s_waitcnt lgkmcnt(4)\n"
"v_mfma_f32_32x32x16_bf16 v[176:191], v[212:215], v[96:99], v[176:191]\n"
"ds_read_b64_tr_b16 v[208:209], v246 offset:32768\n"
"ds_read_b64_tr_b16 v[210:211], v246 offset:34816\n"
"buffer_load_dwordx4 v237, s[36:39], s60 offen offset:2048 lds\n"
"v_mfma_f32_32x32x16_bf16 v[192:207], v[216:219], v[96:99], v[192:207]\n"
"s_waitcnt vmcnt(9)\n"
"ds_read_b64_tr_b16 v[212:213], v247 offset:32768\n"
"ds_read_b64_tr_b16 v[214:215], v247 offset:34816\n"
"s_waitcnt lgkmcnt(4)\n"
"v_mfma_f32_32x32x16_bf16 v[0:15], v[220:223], v[100:103], v[0:15]\n"
"s_barrier\n"
"v_add_u32 v244, 36864, v244\n"
"v_add_u32 v245, 36864, v245\n"
"v_add_u32 v246, 36864, v246\n"
"v_add_u32 v247, 36864, v247\n"
"ds_read_b64_tr_b16 v[216:217], v244\n"
"ds_read_b64_tr_b16 v[218:219], v244 offset:2048\n"
"v_mfma_f32_32x32x16_bf16 v[16:31], v[224:227], v[100:103], v[16:31]\n"
"ds_read_b64_tr_b16 v[220:221], v245\n"
"ds_read_b64_tr_b16 v[222:223], v245 offset:2048\n"
"s_waitcnt lgkmcnt(4)\n"
"v_mfma_f32_32x32x16_bf16 v[176:191], v[208:211], v[100:103], v[176:191]\n"
"ds_read_b64_tr_b16 v[224:225], v246\n"
"ds_read_b64_tr_b16 v[226:227], v246 offset:2048\n"
"s_add_u32 m0, s55, 86016\n"
"v_mfma_f32_32x32x16_bf16 v[192:207], v[212:215], v[100:103], v[192:207]\n"
"ds_read_b64_tr_b16 v[208:209], v247\n"
"ds_read_b64_tr_b16 v[210:211], v247 offset:2048\n"
"s_waitcnt lgkmcnt(4)\n"
"v_mfma_f32_32x32x16_bf16 v[0:15], v[216:219], v[104:107], v[0:15]\n"
"ds_read_b64_tr_b16 v[212:213], v244 offset:4096\n"
"ds_read_b64_tr_b16 v[214:215], v244 offset:6144\n"
"s_mov_b32 s60, 1658880\n"
"v_mfma_f32_32x32x16_bf16 v[16:31], v[220:223], v[104:107], v[16:31]\n"
"ds_read_b64_tr_b16 v[216:217], v245 offset:4096\n"
"ds_read_b64_tr_b16 v[218:219], v245 offset:6144\n"
"s_waitcnt lgkmcnt(4)\n"
"v_mfma_f32_32x32x16_bf16 v[176:191], v[224:227], v[104:107], v[176:191]\n"
"ds_read_b64_tr_b16 v[220:221], v246 offset:4096\n"
"ds_read_b64_tr_b16 v[222:223], v246 offset:6144\n"
"buffer_load_dwordx4 v235, s[36:39], s60 offen lds\n"
"v_mfma_f32_32x32x16_bf16 v[192:207], v[208:211], v[104:107], v[192:207]\n"
"ds_read_b64_tr_b16 v[224:225], v247 offset:4096\n"
"ds_read_b64_tr_b16 v[226:227], v247 offset:6144\n"
"s_waitcnt lgkmcnt(4)\n"
"v_mfma_f32_32x32x16_bf16 v[0:15], v[212:215], v[108:111], v[0:15]\n"
"ds_read_b64_tr_b16 v[208:209], v244 offset:8192\n"
"ds_read_b64_tr_b16 v[210:211], v244 offset:10240\n"
"buffer_load_dwordx4 v236, s[36:39], s60 offen offset:1024 lds\n"
"v_mfma_f32_32x32x16_bf16 v[16:31], v[216:219], v[108:111], v[16:31]\n"
"ds_read_b64_tr_b16 v[212:213], v245 offset:8192\n"
"ds_read_b64_tr_b16 v[214:215], v245 offset:10240\n"
"s_waitcnt lgkmcnt(4)\n"
"v_mfma_f32_32x32x16_bf16 v[176:191], v[220:223], v[108:111], v[176:191]\n"
"ds_read_b64_tr_b16 v[216:217], v246 offset:8192\n"
"ds_read_b64_tr_b16 v[218:219], v246 offset:10240\n"
"buffer_load_dwordx4 v237, s[36:39], s60 offen offset:2048 lds\n"
"v_mfma_f32_32x32x16_bf16 v[192:207], v[224:227], v[108:111], v[192:207]\n"
"s_waitcnt vmcnt(9)\n"
"ds_read_b64_tr_b16 v[220:221], v247 offset:8192\n"
"ds_read_b64_tr_b16 v[222:223], v247 offset:10240\n"
"s_waitcnt lgkmcnt(4)\n"
"v_mfma_f32_32x32x16_bf16 v[0:15], v[208:211], v[112:115], v[0:15]\n"
"s_barrier\n"
"ds_read_b64_tr_b16 v[224:225], v244 offset:12288\n"
"ds_read_b64_tr_b16 v[226:227], v244 offset:14336\n"
"v_mfma_f32_32x32x16_bf16 v[16:31], v[212:215], v[112:115], v[16:31]\n"
"ds_read_b64_tr_b16 v[208:209], v245 offset:12288\n"
"ds_read_b64_tr_b16 v[210:211], v245 offset:14336\n"
"s_waitcnt lgkmcnt(4)\n"
"v_mfma_f32_32x32x16_bf16 v[176:191], v[216:219], v[112:115], v[176:191]\n"
"ds_read_b64_tr_b16 v[212:213], v246 offset:12288\n"
"ds_read_b64_tr_b16 v[214:215], v246 offset:14336\n"
"s_add_u32 m0, s55, 98304\n"
"v_mfma_f32_32x32x16_bf16 v[192:207], v[220:223], v[112:115], v[192:207]\n"
"ds_read_b64_tr_b16 v[216:217], v247 offset:12288\n"
"ds_read_b64_tr_b16 v[218:219], v247 offset:14336\n"
"s_waitcnt lgkmcnt(4)\n"
"v_mfma_f32_32x32x16_bf16 v[0:15], v[224:227], v[116:119], v[0:15]\n"
"ds_read_b64_tr_b16 v[220:221], v244 offset:16384\n"
"ds_read_b64_tr_b16 v[222:223], v244 offset:18432\n"
"s_mov_b32 s60, 1824768\n"
"v_mfma_f32_32x32x16_bf16 v[16:31], v[208:211], v[116:119], v[16:31]\n"
"ds_read_b64_tr_b16 v[224:225], v245 offset:16384\n"
"ds_read_b64_tr_b16 v[226:227], v245 offset:18432\n"
"s_waitcnt lgkmcnt(4)\n"
"v_mfma_f32_32x32x16_bf16 v[176:191], v[212:215], v[116:119], v[176:191]\n"
"ds_read_b64_tr_b16 v[208:209], v246 offset:16384\n"
"ds_read_b64_tr_b16 v[210:211], v246 offset:18432\n"
"buffer_load_dwordx4 v235, s[36:39], s60 offen lds\n"
"v_mfma_f32_32x32x16_bf16 v[192:207], v[216:219], v[116:119], v[192:207]\n"
"ds_read_b64_tr_b16 v[212:213], v247 offset:16384\n"
"ds_read_b64_tr_b16 v[214:215], v247 offset:18432\n"
"s_waitcnt lgkmcnt(4)\n"
"v_mfma_f32_32x32x16_bf16 v[0:15], v[220:223], v[120:123], v[0:15]\n"
"ds_read_b64_tr_b16 v[216:217], v244 offset:20480\n"
"ds_read_b64_tr_b16 v[218:219], v244 offset:22528\n"
"buffer_load_dwordx4 v236, s[36:39], s60 offen offset:1024 lds\n"
"v_mfma_f32_32x32x16_bf16 v[16:31], v[224:227], v[120:123], v[16:31]\n"
"ds_read_b64_tr_b16 v[220:221], v245 offset:20480\n"
"ds_read_b64_tr_b16 v[222:223], v245 offset:22528\n"
"s_waitcnt lgkmcnt(4)\n"
"v_mfma_f32_32x32x16_bf16 v[176:191], v[208:211], v[120:123], v[176:191]\n"
"ds_read_b64_tr_b16 v[224:225], v246 offset:20480\n"
"ds_read_b64_tr_b16 v[226:227], v246 offset:22528\n"
"buffer_load_dwordx4 v237, s[36:39], s60 offen offset:2048 lds\n"
"v_mfma_f32_32x32x16_bf16 v[192:207], v[212:215], v[120:123], v[192:207]\n"
"s_waitcnt vmcnt(9)\n"
"ds_read_b64_tr_b16 v[208:209], v247 offset:20480\n"
"ds_read_b64_tr_b16 v[210:211], v247 offset:22528\n"
"s_waitcnt lgkmcnt(4)\n"
"v_mfma_f32_32x32x16_bf16 v[0:15], v[216:219], v[124:127], v[0:15]\n"
"s_barrier\n"
"ds_read_b64_tr_b16 v[212:213], v244 offset:24576\n"
"ds_read_b64_tr_b16 v[214:215], v244 offset:26624\n"
"v_mfma_f32_32x32x16_bf16 v[16:31], v[220:223], v[124:127], v[16:31]\n"
"ds_read_b64_tr_b16 v[216:217], v245 offset:24576\n"
"ds_read_b64_tr_b16 v[218:219], v245 offset:26624\n"
"s_waitcnt lgkmcnt(4)\n"
"v_mfma_f32_32x32x16_bf16 v[176:191], v[224:227], v[124:127], v[176:191]\n"
"ds_read_b64_tr_b16 v[220:221], v246 offset:24576\n"
"ds_read_b64_tr_b16 v[222:223], v246 offset:26624\n"
"s_add_u32 m0, s55, 110592\n"
"v_mfma_f32_32x32x16_bf16 v[192:207], v[208:211], v[124:127], v[192:207]\n"
"ds_read_b64_tr_b16 v[224:225], v247 offset:24576\n"
"ds_read_b64_tr_b16 v[226:227], v247 offset:26624\n"
"s_waitcnt lgkmcnt(4)\n"
"v_mfma_f32_32x32x16_bf16 v[0:15], v[212:215], v[128:131], v[0:15]\n"
"ds_read_b64_tr_b16 v[208:209], v244 offset:28672\n"
"ds_read_b64_tr_b16 v[210:211], v244 offset:30720\n"
"s_mul_i32 s19, s16, 64\n"
"s_add_u32 s60, s19, 0\n"
"v_mfma_f32_32x32x16_bf16 v[16:31], v[216:219], v[128:131], v[16:31]\n"
"ds_read_b64_tr_b16 v[212:213], v245 offset:28672\n"
"ds_read_b64_tr_b16 v[214:215], v245 offset:30720\n"
"s_waitcnt lgkmcnt(4)\n"
"v_mfma_f32_32x32x16_bf16 v[176:191], v[220:223], v[128:131], v[176:191]\n"
"ds_read_b64_tr_b16 v[216:217], v246 offset:28672\n"
"ds_read_b64_tr_b16 v[218:219], v246 offset:30720\n"
"buffer_load_dwordx4 v229, s[40:43], s60 offen lds\n"
"v_mfma_f32_32x32x16_bf16 v[192:207], v[224:227], v[128:131], v[192:207]\n"
"ds_read_b64_tr_b16 v[220:221], v247 offset:28672\n"
"ds_read_b64_tr_b16 v[222:223], v247 offset:30720\n"
"s_waitcnt lgkmcnt(4)\n"
"v_mfma_f32_32x32x16_bf16 v[0:15], v[208:211], v[132:135], v[0:15]\n"
"ds_read_b64_tr_b16 v[224:225], v244 offset:32768\n"
"ds_read_b64_tr_b16 v[226:227], v244 offset:34816\n"
"buffer_load_dwordx4 v230, s[40:43], s60 offen offset:1024 lds\n"
"v_mfma_f32_32x32x16_bf16 v[16:31], v[212:215], v[132:135], v[16:31]\n"
"ds_read_b64_tr_b16 v[208:209], v245 offset:32768\n"
"ds_read_b64_tr_b16 v[210:211], v245 offset:34816\n"
"s_waitcnt lgkmcnt(4)\n"
"v_mfma_f32_32x32x16_bf16 v[176:191], v[216:219], v[132:135], v[176:191]\n"
"ds_read_b64_tr_b16 v[212:213], v246 offset:32768\n"
"ds_read_b64_tr_b16 v[214:215], v246 offset:34816\n"
"buffer_load_dwordx4 v231, s[40:43], s60 offen offset:2048 lds\n"
"v_mfma_f32_32x32x16_bf16 v[192:207], v[220:223], v[132:135], v[192:207]\n"
"s_waitcnt vmcnt(9)\n"
"ds_read_b64_tr_b16 v[216:217], v247 offset:32768\n"
"ds_read_b64_tr_b16 v[218:219], v247 offset:34816\n"
"s_waitcnt lgkmcnt(4)\n"
"v_mfma_f32_32x32x16_bf16 v[0:15], v[224:227], v[136:139], v[0:15]\n"
"s_barrier\n"
"v_subrev_u32 v244, 36864, v244\n"
"v_subrev_u32 v245, 36864, v245\n"
"v_subrev_u32 v246, 36864, v246\n"
"v_subrev_u32 v247, 36864, v247\n"
"ds_read_b64_tr_b16 v[220:221], v244\n"
"ds_read_b64_tr_b16 v[222:223], v244 offset:2048\n"
"v_mfma_f32_32x32x16_bf16 v[16:31], v[208:211], v[136:139], v[16:31]\n"
"ds_read_b64_tr_b16 v[224:225], v245\n"
"ds_read_b64_tr_b16 v[226:227], v245 offset:2048\n"
"s_waitcnt lgkmcnt(4)\n"
"v_mfma_f32_32x32x16_bf16 v[176:191], v[212:215], v[136:139], v[176:191]\n"
"ds_read_b64_tr_b16 v[208:209], v246\n"
"ds_read_b64_tr_b16 v[210:211], v246 offset:2048\n"
"s_add_u32 m0, s55, 122880\n"
"v_mfma_f32_32x32x16_bf16 v[192:207], v[216:219], v[136:139], v[192:207]\n"
"ds_read_b64_tr_b16 v[212:213], v247\n"
"ds_read_b64_tr_b16 v[214:215], v247 offset:2048\n"
"s_waitcnt lgkmcnt(4)\n"
"v_mfma_f32_32x32x16_bf16 v[0:15], v[220:223], v[140:143], v[0:15]\n"
"ds_read_b64_tr_b16 v[216:217], v244 offset:4096\n"
"ds_read_b64_tr_b16 v[218:219], v244 offset:6144\n"
"s_add_u32 s60, s19, 1769472\n"
"v_mfma_f32_32x32x16_bf16 v[16:31], v[224:227], v[140:143], v[16:31]\n"
"ds_read_b64_tr_b16 v[220:221], v245 offset:4096\n"
"ds_read_b64_tr_b16 v[222:223], v245 offset:6144\n"
"s_waitcnt lgkmcnt(4)\n"
"v_mfma_f32_32x32x16_bf16 v[176:191], v[208:211], v[140:143], v[176:191]\n"
"ds_read_b64_tr_b16 v[224:225], v246 offset:4096\n"
"ds_read_b64_tr_b16 v[226:227], v246 offset:6144\n"
"buffer_load_dwordx4 v229, s[40:43], s60 offen lds\n"
"v_mfma_f32_32x32x16_bf16 v[192:207], v[212:215], v[140:143], v[192:207]\n"
"ds_read_b64_tr_b16 v[208:209], v247 offset:4096\n"
"ds_read_b64_tr_b16 v[210:211], v247 offset:6144\n"
"s_waitcnt lgkmcnt(4)\n"
"v_mfma_f32_32x32x16_bf16 v[0:15], v[216:219], v[144:147], v[0:15]\n"
"ds_read_b64_tr_b16 v[212:213], v244 offset:8192\n"
"ds_read_b64_tr_b16 v[214:215], v244 offset:10240\n"
"buffer_load_dwordx4 v230, s[40:43], s60 offen offset:1024 lds\n"
"v_mfma_f32_32x32x16_bf16 v[16:31], v[220:223], v[144:147], v[16:31]\n"
"ds_read_b64_tr_b16 v[216:217], v245 offset:8192\n"
"ds_read_b64_tr_b16 v[218:219], v245 offset:10240\n"
"s_waitcnt lgkmcnt(4)\n"
"v_mfma_f32_32x32x16_bf16 v[176:191], v[224:227], v[144:147], v[176:191]\n"
"ds_read_b64_tr_b16 v[220:221], v246 offset:8192\n"
"ds_read_b64_tr_b16 v[222:223], v246 offset:10240\n"
"buffer_load_dwordx4 v231, s[40:43], s60 offen offset:2048 lds\n"
"v_mfma_f32_32x32x16_bf16 v[192:207], v[208:211], v[144:147], v[192:207]\n"
"s_waitcnt vmcnt(9)\n"
"ds_read_b64_tr_b16 v[224:225], v247 offset:8192\n"
"ds_read_b64_tr_b16 v[226:227], v247 offset:10240\n"
"s_waitcnt lgkmcnt(4)\n"
"v_mfma_f32_32x32x16_bf16 v[0:15], v[212:215], v[148:151], v[0:15]\n"
"s_barrier\n"
"ds_read_b64_tr_b16 v[208:209], v244 offset:12288\n"
"ds_read_b64_tr_b16 v[210:211], v244 offset:14336\n"
"v_mfma_f32_32x32x16_bf16 v[16:31], v[216:219], v[148:151], v[16:31]\n"
"ds_read_b64_tr_b16 v[212:213], v245 offset:12288\n"
"ds_read_b64_tr_b16 v[214:215], v245 offset:14336\n"
"s_waitcnt lgkmcnt(4)\n"
"v_mfma_f32_32x32x16_bf16 v[176:191], v[220:223], v[148:151], v[176:191]\n"
"ds_read_b64_tr_b16 v[216:217], v246 offset:12288\n"
"ds_read_b64_tr_b16 v[218:219], v246 offset:14336\n"
"s_add_u32 m0, s55, 135168\n"
"v_mfma_f32_32x32x16_bf16 v[192:207], v[224:227], v[148:151], v[192:207]\n"
"ds_read_b64_tr_b16 v[220:221], v247 offset:12288\n"
"ds_read_b64_tr_b16 v[222:223], v247 offset:14336\n"
"s_waitcnt lgkmcnt(4)\n"
"v_mfma_f32_32x32x16_bf16 v[0:15], v[208:211], v[152:155], v[0:15]\n"
"ds_read_b64_tr_b16 v[224:225], v244 offset:16384\n"
"ds_read_b64_tr_b16 v[226:227], v244 offset:18432\n"
"s_add_u32 s60, s19, 3538944\n"
"v_mfma_f32_32x32x16_bf16 v[16:31], v[212:215], v[152:155], v[16:31]\n"
"ds_read_b64_tr_b16 v[208:209], v245 offset:16384\n"
"ds_read_b64_tr_b16 v[210:211], v245 offset:18432\n"
"s_waitcnt lgkmcnt(4)\n"
"v_mfma_f32_32x32x16_bf16 v[176:191], v[216:219], v[152:155], v[176:191]\n"
"ds_read_b64_tr_b16 v[212:213], v246 offset:16384\n"
"ds_read_b64_tr_b16 v[214:215], v246 offset:18432\n"
"buffer_load_dwordx4 v229, s[40:43], s60 offen lds\n"
"v_mfma_f32_32x32x16_bf16 v[192:207], v[220:223], v[152:155], v[192:207]\n"
"ds_read_b64_tr_b16 v[216:217], v247 offset:16384\n"
"ds_read_b64_tr_b16 v[218:219], v247 offset:18432\n"
"s_waitcnt lgkmcnt(4)\n"
"v_mfma_f32_32x32x16_bf16 v[0:15], v[224:227], v[156:159], v[0:15]\n"
"ds_read_b64_tr_b16 v[220:221], v244 offset:20480\n"
"ds_read_b64_tr_b16 v[222:223], v244 offset:22528\n"
"buffer_load_dwordx4 v230, s[40:43], s60 offen offset:1024 lds\n"
"v_mfma_f32_32x32x16_bf16 v[16:31], v[208:211], v[156:159], v[16:31]\n"
"ds_read_b64_tr_b16 v[224:225], v245 offset:20480\n"
"ds_read_b64_tr_b16 v[226:227], v245 offset:22528\n"
"s_waitcnt lgkmcnt(4)\n"
"v_mfma_f32_32x32x16_bf16 v[176:191], v[212:215], v[156:159], v[176:191]\n"
"ds_read_b64_tr_b16 v[208:209], v246 offset:20480\n"
"ds_read_b64_tr_b16 v[210:211], v246 offset:22528\n"
"buffer_load_dwordx4 v231, s[40:43], s60 offen offset:2048 lds\n"
"v_mfma_f32_32x32x16_bf16 v[192:207], v[216:219], v[156:159], v[192:207]\n"
"s_waitcnt vmcnt(9)\n"
"ds_read_b64_tr_b16 v[212:213], v247 offset:20480\n"
"ds_read_b64_tr_b16 v[214:215], v247 offset:22528\n"
"s_waitcnt lgkmcnt(4)\n"
"v_mfma_f32_32x32x16_bf16 v[0:15], v[220:223], v[160:163], v[0:15]\n"
"s_barrier\n"
"ds_read_b64_tr_b16 v[216:217], v244 offset:24576\n"
"ds_read_b64_tr_b16 v[218:219], v244 offset:26624\n"
"v_mfma_f32_32x32x16_bf16 v[16:31], v[224:227], v[160:163], v[16:31]\n"
"ds_read_b64_tr_b16 v[220:221], v245 offset:24576\n"
"ds_read_b64_tr_b16 v[222:223], v245 offset:26624\n"
"s_waitcnt lgkmcnt(4)\n"
"v_mfma_f32_32x32x16_bf16 v[176:191], v[208:211], v[160:163], v[176:191]\n"
"ds_read_b64_tr_b16 v[224:225], v246 offset:24576\n"
"ds_read_b64_tr_b16 v[226:227], v246 offset:26624\n"
"s_add_u32 m0, s55, 73728\n"
"v_mfma_f32_32x32x16_bf16 v[192:207], v[212:215], v[160:163], v[192:207]\n"
"ds_read_b64_tr_b16 v[208:209], v247 offset:24576\n"
"ds_read_b64_tr_b16 v[210:211], v247 offset:26624\n"
"s_waitcnt lgkmcnt(4)\n"
"v_mfma_f32_32x32x16_bf16 v[0:15], v[216:219], v[164:167], v[0:15]\n"
"ds_read_b64_tr_b16 v[212:213], v244 offset:28672\n"
"ds_read_b64_tr_b16 v[214:215], v244 offset:30720\n"
"s_add_u32 s16, s16, 1\n"
"s_cmp_eq_u32 s16, 9\n"
"s_cselect_b32 s16, 0, s16\n"
"s_mul_i32 s19, s16, 64\n"
"s_add_u32 s60, s19, 0\n"
"v_mfma_f32_32x32x16_bf16 v[16:31], v[220:223], v[164:167], v[16:31]\n"
"ds_read_b64_tr_b16 v[216:217], v245 offset:28672\n"
"ds_read_b64_tr_b16 v[218:219], v245 offset:30720\n"
"s_waitcnt lgkmcnt(4)\n"
"v_mfma_f32_32x32x16_bf16 v[176:191], v[224:227], v[164:167], v[176:191]\n"
"ds_read_b64_tr_b16 v[220:221], v246 offset:28672\n"
"ds_read_b64_tr_b16 v[222:223], v246 offset:30720\n"
"buffer_load_dwordx4 v229, s[40:43], s60 offen lds\n"
"v_mfma_f32_32x32x16_bf16 v[192:207], v[208:211], v[164:167], v[192:207]\n"
"ds_read_b64_tr_b16 v[224:225], v247 offset:28672\n"
"ds_read_b64_tr_b16 v[226:227], v247 offset:30720\n"
"s_waitcnt lgkmcnt(4)\n"
"v_mfma_f32_32x32x16_bf16 v[0:15], v[212:215], v[168:171], v[0:15]\n"
"ds_read_b64_tr_b16 v[208:209], v244 offset:32768\n"
"ds_read_b64_tr_b16 v[210:211], v244 offset:34816\n"
"buffer_load_dwordx4 v230, s[40:43], s60 offen offset:1024 lds\n"
"v_mfma_f32_32x32x16_bf16 v[16:31], v[216:219], v[168:171], v[16:31]\n"
"ds_read_b64_tr_b16 v[212:213], v245 offset:32768\n"
"ds_read_b64_tr_b16 v[214:215], v245 offset:34816\n"
"s_waitcnt lgkmcnt(4)\n"
"v_mfma_f32_32x32x16_bf16 v[176:191], v[220:223], v[168:171], v[176:191]\n"
"ds_read_b64_tr_b16 v[216:217], v246 offset:32768\n"
"ds_read_b64_tr_b16 v[218:219], v246 offset:34816\n"
"buffer_load_dwordx4 v231, s[40:43], s60 offen offset:2048 lds\n"
"v_mfma_f32_32x32x16_bf16 v[192:207], v[224:227], v[168:171], v[192:207]\n"
"ds_read_b64_tr_b16 v[220:221], v247 offset:32768\n"
"ds_read_b64_tr_b16 v[222:223], v247 offset:34816\n"
"s_waitcnt lgkmcnt(4)\n"
"v_mfma_f32_32x32x16_bf16 v[0:15], v[208:211], v[172:175], v[0:15]\n"
"v_mfma_f32_32x32x16_bf16 v[16:31], v[212:215], v[172:175], v[16:31]\n"
"s_waitcnt lgkmcnt(0)\n"
"v_mfma_f32_32x32x16_bf16 v[176:191], v[216:219], v[172:175], v[176:191]\n"
"v_mfma_f32_32x32x16_bf16 v[192:207], v[220:223], v[172:175], v[192:207]\n"
"s_nop 11\n"
"v_and_b32 v208, 31, v228\n"
"v_lshrrev_b32_e64 v209, 5, v228\n"
"s_mul_i32 s56, s52, 8448\n"
"s_add_u32 s56, s56, 0\n"
"v_mul_u32_u24 v212, 264, v208\n"
"v_add_u32 v212, s56, v212\n"
"v_lshl_add_u32 v210, v209, 3, v212\n"
"v_add_u32_e64 v212, s33, v212\n"
"v_lshl_add_u32 v211, v209, 4, v212\n"
"s_cmp_eq_u32 s64, 0\n"
"s_cselect_b32 s57, 0x3eae1a93, 1.0\n"
"v_mul_f32 v213, s57, v252\n"
"v_mul_f32_e64 v214, v252, v252\n"
"s_lshl_b32 s57, s53, 7\n"
"s_lshl_b32 s58, s52, 5\n"
"s_add_u32 s57, s57, s58\n"
"s_lshl_b32 s57, s57, 2\n"
"s_add_u32 s57, s57, 4096\n"
"s_cmp_eq_u32 s54, 0\n"
"s_cselect_b32 s57, s57, 0x7f000000\n"
"v_lshlrev_b32 v215, 2, v208\n"
"buffer_store_dword v214, v215, s[48:51], s57 offen\n"
"v_mul_f32 v0, v213, v0\n"
"v_mul_f32 v1, v213, v1\n"
"v_mul_f32 v2, v213, v2\n"
"v_mul_f32 v3, v213, v3\n"
"v_cvt_pk_bf16_f32 v0, v0, v1\n"
"v_cvt_pk_bf16_f32 v1, v2, v3\n"
"v_mul_f32 v4, v213, v4\n"
"v_mul_f32 v5, v213, v5\n"
"v_mul_f32 v6, v213, v6\n"
"v_mul_f32 v7, v213, v7\n"
"ds_write_b64 v210, v[0:1]\n"
"v_cvt_pk_bf16_f32 v4, v4, v5\n"
"v_cvt_pk_bf16_f32 v5, v6, v7\n"
"v_mul_f32 v8, v213, v8\n"
"v_mul_f32 v9, v213, v9\n"
"v_mul_f32 v10, v213, v10\n"
"v_mul_f32 v11, v213, v11\n"
"ds_write_b64 v210, v[4:5] offset:16\n"
"v_cvt_pk_bf16_f32 v8, v8, v9\n"
"v_cvt_pk_bf16_f32 v9, v10, v11\n"
"v_mul_f32 v12, v213, v12\n"
"v_mul_f32 v13, v213, v13\n"
"v_mul_f32 v14, v213, v14\n"
"v_mul_f32 v15, v213, v15\n"
"ds_write_b64 v210, v[8:9] offset:32\n"
"v_cvt_pk_bf16_f32 v12, v12, v13\n"
"v_cvt_pk_bf16_f32 v13, v14, v15\n"
"v_mul_f32 v16, v213, v16\n"
"v_mul_f32 v17, v213, v17\n"
"v_mul_f32 v18, v213, v18\n"
"v_mul_f32 v19, v213, v19\n"
"ds_write_b64 v210, v[12:13] offset:48\n"
"v_cvt_pk_bf16_f32 v16, v16, v17\n"
"v_cvt_pk_bf16_f32 v17, v18, v19\n"
"v_mul_f32 v20, v213, v20\n"
"v_mul_f32 v21, v213, v21\n"
"v_mul_f32 v22, v213, v22\n"
"v_mul_f32 v23, v213, v23\n"
"ds_write_b64 v210, v[16:17] offset:64\n"
"v_cvt_pk_bf16_f32 v20, v20, v21\n"
"v_cvt_pk_bf16_f32 v21, v22, v23\n"
"v_mul_f32 v24, v213, v24\n"
"v_mul_f32 v25, v213, v25\n"
"v_mul_f32 v26, v213, v26\n"
"v_mul_f32 v27, v213, v27\n"
"ds_write_b64 v210, v[20:21] offset:80\n"
"v_cvt_pk_bf16_f32 v24, v24, v25\n"
"v_cvt_pk_bf16_f32 v25, v26, v27\n"
"v_mul_f32 v28, v213, v28\n"
"v_mul_f32 v29, v213, v29\n"
"v_mul_f32 v30, v213, v30\n"
"v_mul_f32 v31, v213, v31\n"
"ds_write_b64 v210, v[24:25] offset:96\n"
"v_cvt_pk_bf16_f32 v28, v28, v29\n"
"v_cvt_pk_bf16_f32 v29, v30, v31\n"
"v_mul_f32 v176, v213, v176\n"
"v_mul_f32 v177, v213, v177\n"
"v_mul_f32 v178, v213, v178\n"
"v_mul_f32 v179, v213, v179\n"
"ds_write_b64 v210, v[28:29] offset:112\n"
"v_cvt_pk_bf16_f32 v176, v176, v177\n"
"v_cvt_pk_bf16_f32 v177, v178, v179\n"
"v_mul_f32 v180, v213, v180\n"
"v_mul_f32 v181, v213, v181\n"
"v_mul_f32 v182, v213, v182\n"
"v_mul_f32 v183, v213, v183\n"
"ds_write_b64 v210, v[176:177] offset:128\n"
"v_cvt_pk_bf16_f32 v180, v180, v181\n"
"v_cvt_pk_bf16_f32 v181, v182, v183\n"
"v_mul_f32 v184, v213, v184\n"
"v_mul_f32 v185, v213, v185\n"
"v_mul_f32 v186, v213, v186\n"
"v_mul_f32 v187, v213, v187\n"
"ds_write_b64 v210, v[180:181] offset:144\n"
"v_cvt_pk_bf16_f32 v184, v184, v185\n"
"v_cvt_pk_bf16_f32 v185, v186, v187\n"
"v_mul_f32 v188, v213, v188\n"
"v_mul_f32 v189, v213, v189\n"
"v_mul_f32 v190, v213, v190\n"
"v_mul_f32 v191, v213, v191\n"
"ds_write_b64 v210, v[184:185] offset:160\n"
"v_cvt_pk_bf16_f32 v188, v188, v189\n"
"v_cvt_pk_bf16_f32 v189, v190, v191\n"
"v_mul_f32 v192, v213, v192\n"
"v_mul_f32 v193, v213, v193\n"
"v_mul_f32 v194, v213, v194\n"
"v_mul_f32 v195, v213, v195\n"
"ds_write_b64 v210, v[188:189] offset:176\n"
"v_cvt_pk_bf16_f32 v192, v192, v193\n"
"v_cvt_pk_bf16_f32 v193, v194, v195\n"
"v_mul_f32 v196, v213, v196\n"
"v_mul_f32 v197, v213, v197\n"
"v_mul_f32 v198, v213, v198\n"
"v_mul_f32 v199, v213, v199\n"
"ds_write_b64 v210, v[192:193] offset:192\n"
"v_cvt_pk_bf16_f32 v196, v196, v197\n"
"v_cvt_pk_bf16_f32 v197, v198, v199\n"
"v_mul_f32 v200, v213, v200\n"
"v_mul_f32 v201, v213, v201\n"
"v_mul_f32 v202, v213, v202\n"
"v_mul_f32 v203, v213, v203\n"
"ds_write_b64 v210, v[196:197] offset:208\n"
"v_cvt_pk_bf16_f32 v200, v200, v201\n"
"v_cvt_pk_bf16_f32 v201, v202, v203\n"
"v_mul_f32 v204, v213, v204\n"
"v_mul_f32 v205, v213, v205\n"
"v_mul_f32 v206, v213, v206\n"
"v_mul_f32 v207, v213, v207\n"
"ds_write_b64 v210, v[200:201] offset:224\n"
"v_cvt_pk_bf16_f32 v204, v204, v205\n"
"v_cvt_pk_bf16_f32 v205, v206, v207\n"
"ds_write_b64 v210, v[204:205] offset:240\n"
"s_waitcnt lgkmcnt(15)\n"
"s_lshl_b32 s73, s53, 2\n"
"s_add_u32 s73, s73, s52\n"
"s_waitcnt lgkmcnt(0)\n"
"s_cmp_eq_u32 s64, 2\n"
"s_cbranch_scc1 LQKV_V_%=\n"
"v_mul_u32_u24 v218, 36, v209\n"
"v_add_u32 v218, v218, v212\n"
"ds_read2_b32 v[0:1], v211 offset0:0 offset1:1\n"
"ds_read2_b32 v[2:3], v211 offset0:2 offset1:3\n"
"ds_read_b32 v4, v218 offset:32\n"
"ds_read2_b32 v[6:7], v211 offset0:9 offset1:10\n"
"ds_read2_b32 v[8:9], v211 offset0:11 offset1:12\n"
"ds_read2_b32 v[12:13], v211 offset0:18 offset1:19\n"
"ds_read2_b32 v[14:15], v211 offset0:20 offset1:21\n"
"ds_read_b32 v16, v218 offset:104\n"
"ds_read2_b32 v[18:19], v211 offset0:27 offset1:28\n"
"ds_read2_b32 v[20:21], v211 offset0:29 offset1:30\n"
"ds_read2_b32 v[24:25], v211 offset0:36 offset1:37\n"
"ds_read2_b32 v[26:27], v211 offset0:38 offset1:39\n"
"ds_read_b32 v28, v218 offset:176\n"
"ds_read2_b32 v[176:177], v211 offset0:45 offset1:46\n"
"ds_read2_b32 v[178:179], v211 offset0:47 offset1:48\n"
"ds_read2_b32 v[182:183], v211 offset0:54 offset1:55\n"
"s_waitcnt lgkmcnt(15)\n"
"ds_read2_b32 v[184:185], v211 offset0:56 offset1:57\n"
"s_waitcnt lgkmcnt(15)\n"
"ds_read_b32 v186, v218 offset:248\n"
"s_waitcnt lgkmcnt(15)\n"
"s_lshl_b32 s56, s65, 6\n"
"s_add_u32 s56, s56, s73\n"
"s_cmp_eq_u32 s64, 0\n"
"s_mov_b32 s57, 2304\n"
"s_cselect_b32 s57, 1024, s57\n"
"s_mul_i32 s67, s56, s57\n"
"s_mov_b32 s58, 16384\n"
"s_cselect_b32 s58, 4997120, s58\n"
"s_add_u32 s67, s67, s58\n"
"s_lshl_b32 s68, s57, 6\n"
"s_lshl_b32 s57, s56, 7\n"
"s_add_u32 s57, s57, 7094272\n"
"s_lshl_b32 s58, s65, 13\n"
"s_lshl_b32 s59, s53, 9\n"
"s_add_u32 s58, s58, s59\n"
"s_lshl_b32 s59, s52, 2\n"
"s_add_u32 s58, s58, s59\n"
"s_add_u32 s58, s58, 4734976\n"
"s_cmp_eq_u32 s64, 0\n"
"s_cselect_b32 s69, s57, s58\n"
"s_mov_b32 s70, 8192\n"
"s_cselect_b32 s57, 2, 4\n"
"v_lshlrev_b32 v216, 4, v228\n"
"v_lshlrev_b32 v217, s57, v208\n"
"v_lshl_add_u32 v217, v209, 13, v217\n"
"v_mov_b32 v219, 0x7f000000\n"
"s_cmp_gt_u32 s66, 0\n"
"s_cselect_b32 s60, s67, 0x7f000000\n"
"buffer_store_dwordx4 v[0:3], v216, s[48:51], s60 offen sc1\n"
"v_mov_b32 v220, v209\n"
"v_cmp_gt_u32 vcc, s66, v220\n"
"s_nop 1\n"
"v_cndmask_b32 v220, v219, v217\n"
"buffer_store_dword v4, v220, s[48:51], s69 offen sc1\n"
"s_add_u32 s69, s69, 16384\n"
"s_add_u32 s67, s67, s68\n"
"s_cmp_gt_u32 s66, 1\n"
"s_cselect_b32 s60, s67, 0x7f000000\n"
"s_waitcnt lgkmcnt(13)\n"
"buffer_store_dwordx4 v[6:9], v216, s[48:51], s60 offen sc1\n"
"s_add_u32 s67, s67, s68\n"
"s_cmp_gt_u32 s66, 2\n"
"s_cselect_b32 s60, s67, 0x7f000000\n"
"s_waitcnt lgkmcnt(11)\n"
"buffer_store_dwordx4 v[12:15], v216, s[48:51], s60 offen sc1\n"
"v_add_u32 v220, 2, v209\n"
"v_cmp_gt_u32 vcc, s66, v220\n"
"s_nop 1\n"
"v_cndmask_b32 v220, v219, v217\n"
"s_waitcnt lgkmcnt(10)\n"
"buffer_store_dword v16, v220, s[48:51], s69 offen sc1\n"
"s_add_u32 s69, s69, 16384\n"
"s_add_u32 s67, s67, s68\n"
"s_cmp_gt_u32 s66, 3\n"
"s_cselect_b32 s60, s67, 0x7f000000\n"
"s_waitcnt lgkmcnt(8)\n"
"buffer_store_dwordx4 v[18:21], v216, s[48:51], s60 offen sc1\n"
"s_add_u32 s67, s67, s68\n"
"s_cmp_gt_u32 s66, 4\n"
"s_cselect_b32 s60, s67, 0x7f000000\n"
"s_waitcnt lgkmcnt(6)\n"
"buffer_store_dwordx4 v[24:27], v216, s[48:51], s60 offen sc1\n"
"v_add_u32_e64 v220, 4, v209\n"
"v_cmp_gt_u32 vcc, s66, v220\n"
"s_nop 1\n"
"v_cndmask_b32 v220, v219, v217\n"
"s_waitcnt lgkmcnt(5)\n"
"buffer_store_dword v28, v220, s[48:51], s69 offen sc1\n"
"s_add_u32 s69, s69, 16384\n"
"s_add_u32 s67, s67, s68\n"
"s_cmp_gt_u32 s66, 5\n"
"s_cselect_b32 s60, s67, 0x7f000000\n"
"s_waitcnt lgkmcnt(3)\n"
"buffer_store_dwordx4 v[176:179], v216, s[48:51], s60 offen sc1\n"
"s_add_u32 s67, s67, s68\n"
"s_cmp_gt_u32 s66, 6\n"
"s_cselect_b32 s60, s67, 0x7f000000\n"
"s_waitcnt lgkmcnt(1)\n"
"buffer_store_dwordx4 v[182:185], v216, s[48:51], s60 offen sc1\n"
"v_add_u32_e64 v220, 6, v209\n"
"v_cmp_gt_u32 vcc, s66, v220\n"
"s_nop 1\n"
"v_cndmask_b32 v220, v219, v217\n"
"s_waitcnt lgkmcnt(0)\n"
"buffer_store_dword v186, v220, s[48:51], s69 offen sc1\n"
"s_branch LQKV_DONE_%=\n"
"LQKV_V_%=:\n"
"v_lshrrev_b32 v217, 5, v228\n"
"v_lshlrev_b32_e64 v217, 4, v217\n"
"v_lshrrev_b32 v218, 4, v228\n"
"v_and_b32 v218, 1, v218\n"
"v_lshl_add_u32 v217, v218, 2, v217\n"
"v_lshrrev_b32_e64 v218, 2, v228\n"
"v_and_b32 v218, 3, v218\n"
"v_add_u32 v217, v217, v218\n"
"v_mul_u32_u24 v217, 264, v217\n"
"v_and_b32_e64 v218, 3, v228\n"
"v_lshl_add_u32 v217, v218, 3, v217\n"
"s_mul_i32 s56, s52, 8448\n"
"s_add_u32 s56, s56, 0\n"
"v_add_u32 v216, s56, v217\n"
"ds_read_b64_tr_b16 v[0:1], v216\n"
"ds_read_b64_tr_b16 v[2:3], v216 offset:2112\n"
"ds_read_b64_tr_b16 v[4:5], v216 offset:32\n"
"ds_read_b64_tr_b16 v[6:7], v216 offset:2144\n"
"ds_read_b64_tr_b16 v[8:9], v216 offset:64\n"
"ds_read_b64_tr_b16 v[10:11], v216 offset:2176\n"
"ds_read_b64_tr_b16 v[12:13], v216 offset:96\n"
"ds_read_b64_tr_b16 v[14:15], v216 offset:2208\n"
"ds_read_b64_tr_b16 v[16:17], v216 offset:128\n"
"ds_read_b64_tr_b16 v[18:19], v216 offset:2240\n"
"ds_read_b64_tr_b16 v[20:21], v216 offset:160\n"
"ds_read_b64_tr_b16 v[22:23], v216 offset:2272\n"
"ds_read_b64_tr_b16 v[24:25], v216 offset:192\n"
"ds_read_b64_tr_b16 v[26:27], v216 offset:2304\n"
"ds_read_b64_tr_b16 v[28:29], v216 offset:224\n"
"ds_read_b64_tr_b16 v[30:31], v216 offset:2336\n"
"s_waitcnt lgkmcnt(15)\n"
"s_lshl_b32 s56, s65, 6\n"
"s_add_u32 s56, s56, s73\n"
"s_mul_i32 s67, s56, 2304\n"
"s_add_u32 s67, s67, 17408\n"
"v_lshrrev_b32 v217, 4, v228\n"
"v_mul_u32_u24 v217, 320, v217\n"
"v_and_b32 v218, 15, v228\n"
"s_lshr_b32 s56, s33, 1\n"
"v_subrev_u32 v218, s56, v218\n"
"s_mul_i32 s57, s66, 18\n"
"s_mov_b32 s58, 147168\n"
"v_mov_b32 v223, 0x7f000000\n"
"v_and_b32 v219, 0xff, v218\n"
"v_mul_u32_u24_e64 v220, 57, v219\n"
"v_cmp_gt_u32 vcc, s57, v219\n"
"v_lshrrev_b32 v220, 10, v220\n"
"v_lshl_add_u32 v221, v219, 4, v217\n"
"v_mad_u32_u24 v221, v220, s58, v221\n"
"v_cndmask_b32 v221, v223, v221\n"
"s_waitcnt lgkmcnt(14)\n"
"buffer_store_dwordx4 v[0:3], v221, s[48:51], s67 offen sc1\n"
"v_add_u32 v219, 16, v218\n"
"v_mul_u32_u24 v220, 57, v219\n"
"v_cmp_gt_u32 vcc, s57, v219\n"
"v_lshrrev_b32 v220, 10, v220\n"
"v_lshl_add_u32 v222, v219, 4, v217\n"
"v_mad_u32_u24 v222, v220, s58, v222\n"
"v_cndmask_b32 v222, v223, v222\n"
"s_waitcnt lgkmcnt(12)\n"
"buffer_store_dwordx4 v[4:7], v222, s[48:51], s67 offen sc1\n"
"v_add_u32 v219, 32, v218\n"
"v_mul_u32_u24 v220, 57, v219\n"
"v_cmp_gt_u32 vcc, s57, v219\n"
"v_lshrrev_b32 v220, 10, v220\n"
"v_lshl_add_u32 v221, v219, 4, v217\n"
"v_mad_u32_u24 v221, v220, s58, v221\n"
"v_cndmask_b32 v221, v223, v221\n"
"s_waitcnt lgkmcnt(10)\n"
"buffer_store_dwordx4 v[8:11], v221, s[48:51], s67 offen sc1\n"
"v_add_u32 v219, 48, v218\n"
"v_mul_u32_u24 v220, 57, v219\n"
"v_cmp_gt_u32 vcc, s57, v219\n"
"v_lshrrev_b32 v220, 10, v220\n"
"v_lshl_add_u32 v222, v219, 4, v217\n"
"v_mad_u32_u24 v222, v220, s58, v222\n"
"v_cndmask_b32 v222, v223, v222\n"
"s_waitcnt lgkmcnt(8)\n"
"buffer_store_dwordx4 v[12:15], v222, s[48:51], s67 offen sc1\n"
"v_add_u32 v219, 64, v218\n"
"v_mul_u32_u24 v220, 57, v219\n"
"v_cmp_gt_u32 vcc, s57, v219\n"
"v_lshrrev_b32 v220, 10, v220\n"
"v_lshl_add_u32 v221, v219, 4, v217\n"
"v_mad_u32_u24 v221, v220, s58, v221\n"
"v_cndmask_b32 v221, v223, v221\n"
"s_waitcnt lgkmcnt(6)\n"
"buffer_store_dwordx4 v[16:19], v221, s[48:51], s67 offen sc1\n"
"v_add_u32 v219, 80, v218\n"
"v_mul_u32_u24_e64 v220, 57, v219\n"
"v_cmp_gt_u32 vcc, s57, v219\n"
"v_lshrrev_b32 v220, 10, v220\n"
"v_lshl_add_u32 v222, v219, 4, v217\n"
"v_mad_u32_u24 v222, v220, s58, v222\n"
"v_cndmask_b32 v222, v223, v222\n"
"s_waitcnt lgkmcnt(4)\n"
"buffer_store_dwordx4 v[20:23], v222, s[48:51], s67 offen sc1\n"
"v_add_u32 v219, 96, v218\n"
"v_mul_u32_u24_e64 v220, 57, v219\n"
"v_cmp_gt_u32 vcc, s57, v219\n"
"v_lshrrev_b32 v220, 10, v220\n"
"v_lshl_add_u32 v221, v219, 4, v217\n"
"v_mad_u32_u24 v221, v220, s58, v221\n"
"v_cndmask_b32 v221, v223, v221\n"
"s_waitcnt lgkmcnt(2)\n"
"buffer_store_dwordx4 v[24:27], v221, s[48:51], s67 offen sc1\n"
"v_add_u32 v219, 112, v218\n"
"v_mul_u32_u24_e64 v220, 57, v219\n"
"v_cmp_gt_u32 vcc, s57, v219\n"
"v_lshrrev_b32 v220, 10, v220\n"
"v_lshl_add_u32 v222, v219, 4, v217\n"
"v_mad_u32_u24 v222, v220, s58, v222\n"
"v_cndmask_b32 v222, v223, v222\n"
"s_waitcnt lgkmcnt(0)\n"
"buffer_store_dwordx4 v[28:31], v222, s[48:51], s67 offen sc1\n"
"v_lshrrev_b32_e64 v219, 3, v228\n"
"v_lshrrev_b32 v220, 1, v228\n"
"v_and_b32 v220, 3, v220\n"
"v_mul_u32_u24 v220, 320, v220\n"
"v_and_b32_e64 v221, 1, v228\n"
"v_lshl_add_u32 v220, v221, 4, v220\n"
"v_add_u32 v220, 288, v220\n"
"v_cmp_gt_u32 vcc, s66, v219\n"
"v_mul_u32_u24 v219, 147456, v219\n"
"v_add_u32 v220, v220, v219\n"
"v_mov_b32 v219, 0x7f000000\n"
"v_cndmask_b32 v220, v219, v220\n"
"v_cmp_eq_u32 vcc, 0, v221\n"
"v_mov_b32 v222, 0x3f803f80\n"
"s_nop 0\n"
"v_cndmask_b32 v176, 0, v222\n"
"v_mov_b32_e64 v177, v176\n"
"v_mov_b32 v178, v176\n"
"v_mov_b32 v179, v176\n"
"buffer_store_dwordx4 v[176:179], v220, s[48:51], s67 offen sc1\n"
"s_mov_b32 s60, 0x7f000000\n"
"buffer_store_dword v176, v220, s[48:51], s60 offen sc1\n"
"buffer_store_dword v176, v220, s[48:51], s60 offen sc1\n"
"LQKV_DONE_%=:\n"
"v_lshlrev_b32_e64 v235, 4, v228\n"
"ds_read_b128 v[0:3], v248\n"
"ds_read_b128 v[4:7], v248 offset:32\n"
"ds_read_b128 v[8:11], v248 offset:64\n"
"ds_read_b128 v[12:15], v248 offset:96\n"
"s_add_u32 s18, s18, 1\n"
"s_cmp_eq_u32 s18, 9\n"
"s_mov_b32 s21, 128\n"
"s_cselect_b32 s21, -1024, s21\n"
"s_cselect_b32 s18, 0, s18\n"
"v_add_u32 v248, s21, v248\n"
"s_waitcnt lgkmcnt(3)\n"
"v_mul_f32 v0, v249, v0\n"
"v_mul_f32 v1, v249, v1\n"
"v_mul_f32 v2, v249, v2\n"
"v_mul_f32 v3, v249, v3\n"
"s_waitcnt lgkmcnt(2)\n"
"v_mul_f32 v4, v249, v4\n"
"v_mul_f32 v5, v249, v5\n"
"v_mul_f32 v6, v249, v6\n"
"v_mul_f32 v7, v249, v7\n"
"s_waitcnt lgkmcnt(1)\n"
"v_mul_f32 v8, v249, v8\n"
"v_mul_f32 v9, v249, v9\n"
"v_mul_f32 v10, v249, v10\n"
"v_mul_f32 v11, v249, v11\n"
"s_waitcnt lgkmcnt(0)\n"
"v_mul_f32 v12, v249, v12\n"
"v_mul_f32 v13, v249, v13\n"
"v_mul_f32 v14, v249, v14\n"
"v_mul_f32 v15, v249, v15\n"
"s_waitcnt vmcnt(21)\n"
"s_barrier\n"
"ds_read_b64_tr_b16 v[224:225], v239\n"
"ds_read_b64_tr_b16 v[226:227], v239 offset:512\n"
"ds_read_b64_tr_b16 v[208:209], v239 offset:1024\n"
"ds_read_b64_tr_b16 v[210:211], v239 offset:1536\n"
"ds_read_b64_tr_b16 v[212:213], v239 offset:2048\n"
"ds_read_b64_tr_b16 v[214:215], v239 offset:2560\n"
"s_add_u32 m0, s55, 86016\n"
"ds_read_b64_tr_b16 v[216:217], v239 offset:3072\n"
"ds_read_b64_tr_b16 v[218:219], v239 offset:3584\n"
"s_waitcnt lgkmcnt(4)\n"
"v_mfma_f32_32x32x16_bf16 v[0:15], v[224:227], v[32:35], v[0:15]\n"
"ds_read_b64_tr_b16 v[220:221], v239 offset:4096\n"
"ds_read_b64_tr_b16 v[222:223], v239 offset:4608\n"
"s_add_u32 s60, s19, 1769472\n"
"v_mfma_f32_32x32x16_bf16 v[0:15], v[208:211], v[36:39], v[0:15]\n"
"ds_read_b64_tr_b16 v[224:225], v239 offset:5120\n"
"ds_read_b64_tr_b16 v[226:227], v239 offset:5632\n"
"s_waitcnt lgkmcnt(4)\n"
"v_mfma_f32_32x32x16_bf16 v[0:15], v[212:215], v[40:43], v[0:15]\n"
"ds_read_b64_tr_b16 v[208:209], v239 offset:6144\n"
"ds_read_b64_tr_b16 v[210:211], v239 offset:6656\n"
"buffer_load_dwordx4 v229, s[40:43], s60 offen lds\n"
"ds_read_b128 v[176:179], v248\n"
"ds_read_b128 v[180:183], v248 offset:32\n"
"v_mfma_f32_32x32x16_bf16 v[0:15], v[216:219], v[44:47], v[0:15]\n"
"ds_read_b64_tr_b16 v[212:213], v239 offset:7168\n"
"ds_read_b64_tr_b16 v[214:215], v239 offset:7680\n"
"ds_read_b128 v[184:187], v248 offset:64\n"
"ds_read_b128 v[188:191], v248 offset:96\n"
"s_add_u32 s18, s18, 1\n"
"s_cmp_eq_u32 s18, 9\n"
"s_mov_b32 s21, 128\n"
"s_cselect_b32 s21, -1024, s21\n"
"s_cselect_b32 s18, 0, s18\n"
"v_add_u32 v248, s21, v248\n"
"s_waitcnt lgkmcnt(8)\n"
"v_mfma_f32_32x32x16_bf16 v[0:15], v[220:223], v[48:51], v[0:15]\n"
"ds_read_b64_tr_b16 v[216:217], v239 offset:8192\n"
"ds_read_b64_tr_b16 v[218:219], v239 offset:8704\n"
"buffer_load_dwordx4 v230, s[40:43], s60 offen offset:1024 lds\n"
"s_waitcnt lgkmcnt(7)\n"
"v_mul_f32 v176, v249, v176\n"
"v_mul_f32 v177, v249, v177\n"
"v_mfma_f32_32x32x16_bf16 v[0:15], v[224:227], v[52:55], v[0:15]\n"
"ds_read_b64_tr_b16 v[220:221], v239 offset:9216\n"
"ds_read_b64_tr_b16 v[222:223], v239 offset:9728\n"
"v_mul_f32_e64 v178, v249, v178\n"
"v_mul_f32 v179, v249, v179\n"
"s_waitcnt lgkmcnt(8)\n"
"v_mul_f32 v180, v249, v180\n"
"s_waitcnt lgkmcnt(6)\n"
"v_mfma_f32_32x32x16_bf16 v[0:15], v[208:211], v[56:59], v[0:15]\n"
"ds_read_b64_tr_b16 v[224:225], v239 offset:10240\n"
"ds_read_b64_tr_b16 v[226:227], v239 offset:10752\n"
"buffer_load_dwordx4 v231, s[40:43], s60 offen offset:2048 lds\n"
"v_mul_f32 v181, v249, v181\n"
"s_waitcnt vmcnt(21)\n"
"v_mul_f32_e64 v182, v249, v182\n"
"v_mfma_f32_32x32x16_bf16 v[0:15], v[212:215], v[60:63], v[0:15]\n"
"ds_read_b64_tr_b16 v[208:209], v239 offset:11264\n"
"ds_read_b64_tr_b16 v[210:211], v239 offset:11776\n"
"v_mul_f32 v183, v249, v183\n"
"s_waitcnt lgkmcnt(9)\n"
"v_mul_f32 v184, v249, v184\n"
"s_waitcnt lgkmcnt(4)\n"
"v_mfma_f32_32x32x16_bf16 v[0:15], v[216:219], v[64:67], v[0:15]\n"
"s_barrier\n"
"ds_read_b64_tr_b16 v[212:213], v239 offset:12288\n"
"ds_read_b64_tr_b16 v[214:215], v239 offset:12800\n"
"v_mul_f32 v185, v249, v185\n"
"v_mul_f32_e64 v186, v249, v186\n"
"v_mfma_f32_32x32x16_bf16 v[0:15], v[220:223], v[68:71], v[0:15]\n"
"ds_read_b64_tr_b16 v[216:217], v239 offset:13312\n"
"ds_read_b64_tr_b16 v[218:219], v239 offset:13824\n"
"v_mul_f32 v187, v249, v187\n"
"v_mul_f32 v188, v249, v188\n"
"v_mul_f32 v189, v249, v189\n"
"s_waitcnt lgkmcnt(4)\n"
"v_mfma_f32_32x32x16_bf16 v[0:15], v[224:227], v[72:75], v[0:15]\n"
"ds_read_b64_tr_b16 v[220:221], v239 offset:14336\n"
"ds_read_b64_tr_b16 v[222:223], v239 offset:14848\n"
"s_add_u32 m0, s55, 98304\n"
"v_mul_f32 v190, v249, v190\n"
"v_mul_f32 v191, v249, v191\n"
"v_mfma_f32_32x32x16_bf16 v[0:15], v[208:211], v[76:79], v[0:15]\n"
"ds_read_b64_tr_b16 v[224:225], v239 offset:15360\n"
"ds_read_b64_tr_b16 v[226:227], v239 offset:15872\n"
"s_waitcnt lgkmcnt(4)\n"
"v_mfma_f32_32x32x16_bf16 v[0:15], v[212:215], v[80:83], v[0:15]\n"
"ds_read_b64_tr_b16 v[208:209], v239 offset:16384\n"
"ds_read_b64_tr_b16 v[210:211], v239 offset:16896\n"
"s_add_u32 s60, s19, 3538944\n"
"v_mfma_f32_32x32x16_bf16 v[0:15], v[216:219], v[84:87], v[0:15]\n"
"ds_read_b64_tr_b16 v[212:213], v239 offset:17408\n"
"ds_read_b64_tr_b16 v[214:215], v239 offset:17920\n"
"s_waitcnt lgkmcnt(4)\n"
"v_mfma_f32_32x32x16_bf16 v[0:15], v[220:223], v[88:91], v[0:15]\n"
"ds_read_b64_tr_b16 v[216:217], v239 offset:18432\n"
"ds_read_b64_tr_b16 v[218:219], v239 offset:18944\n"
"buffer_load_dwordx4 v229, s[40:43], s60 offen lds\n"
"v_mfma_f32_32x32x16_bf16 v[0:15], v[224:227], v[92:95], v[0:15]\n"
"ds_read_b64_tr_b16 v[220:221], v239 offset:19456\n"
"ds_read_b64_tr_b16 v[222:223], v239 offset:19968\n"
"s_waitcnt lgkmcnt(4)\n"
"v_mfma_f32_32x32x16_bf16 v[0:15], v[208:211], v[96:99], v[0:15]\n"
"ds_read_b64_tr_b16 v[224:225], v239 offset:20480\n"
"ds_read_b64_tr_b16 v[226:227], v239 offset:20992\n"
"buffer_load_dwordx4 v230, s[40:43], s60 offen offset:1024 lds\n"
"v_mfma_f32_32x32x16_bf16 v[0:15], v[212:215], v[100:103], v[0:15]\n"
"ds_read_b64_tr_b16 v[208:209], v239 offset:21504\n"
"ds_read_b64_tr_b16 v[210:211], v239 offset:22016\n"
"s_waitcnt lgkmcnt(4)\n"
"v_mfma_f32_32x32x16_bf16 v[0:15], v[216:219], v[104:107], v[0:15]\n"
"ds_read_b64_tr_b16 v[212:213], v239 offset:22528\n"
"ds_read_b64_tr_b16 v[214:215], v239 offset:23040\n"
"buffer_load_dwordx4 v231, s[40:43], s60 offen offset:2048 lds\n"
"v_mfma_f32_32x32x16_bf16 v[0:15], v[220:223], v[108:111], v[0:15]\n"
"s_waitcnt vmcnt(21)\n"
"ds_read_b64_tr_b16 v[216:217], v239 offset:23552\n"
"ds_read_b64_tr_b16 v[218:219], v239 offset:24064\n"
"s_waitcnt lgkmcnt(4)\n"
"v_mfma_f32_32x32x16_bf16 v[0:15], v[224:227], v[112:115], v[0:15]\n"
"s_barrier\n"
"ds_read_b64_tr_b16 v[220:221], v239 offset:24576\n"
"ds_read_b64_tr_b16 v[222:223], v239 offset:25088\n"
"v_mfma_f32_32x32x16_bf16 v[0:15], v[208:211], v[116:119], v[0:15]\n"
"ds_read_b64_tr_b16 v[224:225], v239 offset:25600\n"
"ds_read_b64_tr_b16 v[226:227], v239 offset:26112\n"
"s_waitcnt lgkmcnt(4)\n"
"v_mfma_f32_32x32x16_bf16 v[0:15], v[212:215], v[120:123], v[0:15]\n"
"ds_read_b64_tr_b16 v[208:209], v239 offset:26624\n"
"ds_read_b64_tr_b16 v[210:211], v239 offset:27136\n"
"s_add_u32 m0, s55, 110592\n"
"v_mfma_f32_32x32x16_bf16 v[0:15], v[216:219], v[124:127], v[0:15]\n"
"ds_read_b64_tr_b16 v[212:213], v239 offset:27648\n"
"ds_read_b64_tr_b16 v[214:215], v239 offset:28160\n"
"s_waitcnt lgkmcnt(4)\n"
"v_mfma_f32_32x32x16_bf16 v[0:15], v[220:223], v[128:131], v[0:15]\n"
"ds_read_b64_tr_b16 v[216:217], v239 offset:28672\n"
"ds_read_b64_tr_b16 v[218:219], v239 offset:29184\n"
"s_mul_i32 s20, s17, 36864\n"
"s_add_u32 s60, s20, 0\n"
"v_mfma_f32_32x32x16_bf16 v[0:15], v[224:227], v[132:135], v[0:15]\n"
"ds_read_b64_tr_b16 v[220:221], v239 offset:29696\n"
"ds_read_b64_tr_b16 v[222:223], v239 offset:30208\n"
"s_waitcnt lgkmcnt(4)\n"
"v_mfma_f32_32x32x16_bf16 v[0:15], v[208:211], v[136:139], v[0:15]\n"
"ds_read_b64_tr_b16 v[224:225], v239 offset:30720\n"
"ds_read_b64_tr_b16 v[226:227], v239 offset:31232\n"
"buffer_load_dwordx4 v232, s[44:47], s60 offen lds\n"
"v_mfma_f32_32x32x16_bf16 v[0:15], v[212:215], v[140:143], v[0:15]\n"
"ds_read_b64_tr_b16 v[208:209], v239 offset:31744\n"
"ds_read_b64_tr_b16 v[210:211], v239 offset:32256\n"
"s_waitcnt lgkmcnt(4)\n"
"v_mfma_f32_32x32x16_bf16 v[0:15], v[216:219], v[144:147], v[0:15]\n"
"ds_read_b64_tr_b16 v[212:213], v239 offset:32768\n"
"ds_read_b64_tr_b16 v[214:215], v239 offset:33280\n"
"buffer_load_dwordx4 v233, s[44:47], s60 offen offset:1024 lds\n"
"v_mfma_f32_32x32x16_bf16 v[0:15], v[220:223], v[148:151], v[0:15]\n"
"ds_read_b64_tr_b16 v[216:217], v239 offset:33792\n"
"ds_read_b64_tr_b16 v[218:219], v239 offset:34304\n"
"s_waitcnt lgkmcnt(4)\n"
"v_mfma_f32_32x32x16_bf16 v[0:15], v[224:227], v[152:155], v[0:15]\n"
"ds_read_b64_tr_b16 v[220:221], v239 offset:34816\n"
"ds_read_b64_tr_b16 v[222:223], v239 offset:35328\n"
"buffer_load_dwordx4 v234, s[44:47], s60 offen offset:2048 lds\n"
"v_mfma_f32_32x32x16_bf16 v[0:15], v[208:211], v[156:159], v[0:15]\n"
"s_waitcnt vmcnt(21)\n"
"ds_read_b64_tr_b16 v[224:225], v239 offset:35840\n"
"ds_read_b64_tr_b16 v[226:227], v239 offset:36352\n"
"s_waitcnt lgkmcnt(4)\n"
"v_mfma_f32_32x32x16_bf16 v[0:15], v[212:215], v[160:163], v[0:15]\n"
"s_barrier\n"
"ds_read_b64_tr_b16 v[208:209], v238\n"
"ds_read_b64_tr_b16 v[210:211], v238 offset:512\n"
"v_mfma_f32_32x32x16_bf16 v[0:15], v[216:219], v[164:167], v[0:15]\n"
"ds_read_b64_tr_b16 v[212:213], v238 offset:1024\n"
"ds_read_b64_tr_b16 v[214:215], v238 offset:1536\n"
"s_waitcnt lgkmcnt(4)\n"
"v_mfma_f32_32x32x16_bf16 v[0:15], v[220:223], v[168:171], v[0:15]\n"
"ds_read_b64_tr_b16 v[216:217], v238 offset:2048\n"
"ds_read_b64_tr_b16 v[218:219], v238 offset:2560\n"
"s_add_u32 m0, s55, 122880\n"
"v_mfma_f32_32x32x16_bf16 v[0:15], v[224:227], v[172:175], v[0:15]\n"
"ds_read_b64_tr_b16 v[220:221], v238 offset:3072\n"
"ds_read_b64_tr_b16 v[222:223], v238 offset:3584\n"
"s_waitcnt lgkmcnt(4)\n"
"v_mfma_f32_32x32x16_bf16 v[176:191], v[208:211], v[32:35], v[176:191]\n"
"ds_read_b64_tr_b16 v[224:225], v238 offset:4096\n"
"ds_read_b64_tr_b16 v[226:227], v238 offset:4608\n"
"s_add_u32 s60, s20, 384\n"
"v_mfma_f32_32x32x16_bf16 v[176:191], v[212:215], v[36:39], v[176:191]\n"
"ds_read_b64_tr_b16 v[208:209], v238 offset:5120\n"
"ds_read_b64_tr_b16 v[210:211], v238 offset:5632\n"
"s_waitcnt lgkmcnt(4)\n"
"v_mfma_f32_32x32x16_bf16 v[176:191], v[216:219], v[40:43], v[176:191]\n"
"ds_read_b64_tr_b16 v[212:213], v238 offset:6144\n"
"ds_read_b64_tr_b16 v[214:215], v238 offset:6656\n"
"buffer_load_dwordx4 v232, s[44:47], s60 offen lds\n"
"v_mfma_f32_32x32x16_bf16 v[176:191], v[220:223], v[44:47], v[176:191]\n"
"ds_read_b64_tr_b16 v[216:217], v238 offset:7168\n"
"ds_read_b64_tr_b16 v[218:219], v238 offset:7680\n"
"v_max_f32 v0, 0, v0\n"
"v_max_f32_e64 v1, 0, v1\n"
"v_max_f32 v2, 0, v2\n"
"s_waitcnt lgkmcnt(4)\n"
"v_mfma_f32_32x32x16_bf16 v[176:191], v[224:227], v[48:51], v[176:191]\n"
"ds_read_b64_tr_b16 v[220:221], v238 offset:8192\n"
"ds_read_b64_tr_b16 v[222:223], v238 offset:8704\n"
"buffer_load_dwordx4 v233, s[44:47], s60 offen offset:1024 lds\n"
"v_max_f32 v3, 0, v3\n"
"v_mul_f32 v0, v0, v0\n"
"v_mfma_f32_32x32x16_bf16 v[176:191], v[208:211], v[52:55], v[176:191]\n"
"ds_read_b64_tr_b16 v[224:225], v238 offset:9216\n"
"ds_read_b64_tr_b16 v[226:227], v238 offset:9728\n"
"v_mul_f32 v1, v1, v1\n"
"v_max_f32 v4, 0, v4\n"
"v_max_f32 v5, 0, v5\n"
"s_waitcnt lgkmcnt(4)\n"
"v_mfma_f32_32x32x16_bf16 v[176:191], v[212:215], v[56:59], v[176:191]\n"
"ds_read_b64_tr_b16 v[208:209], v238 offset:10240\n"
"ds_read_b64_tr_b16 v[210:211], v238 offset:10752\n"
"buffer_load_dwordx4 v234, s[44:47], s60 offen offset:2048 lds\n"
"v_mul_f32 v2, v2, v2\n"
"s_waitcnt vmcnt(9)\n"
"v_mul_f32_e64 v3, v3, v3\n"
"v_mfma_f32_32x32x16_bf16 v[176:191], v[216:219], v[60:63], v[176:191]\n"
"ds_read_b64_tr_b16 v[212:213], v238 offset:11264\n"
"ds_read_b64_tr_b16 v[214:215], v238 offset:11776\n"
"v_cvt_pk_bf16_f32 v192, v0, v1\n"
"v_max_f32 v6, 0, v6\n"
"s_waitcnt lgkmcnt(4)\n"
"v_mfma_f32_32x32x16_bf16 v[176:191], v[220:223], v[64:67], v[176:191]\n"
"s_barrier\n"
"ds_read_b64_tr_b16 v[216:217], v238 offset:12288\n"
"ds_read_b64_tr_b16 v[218:219], v238 offset:12800\n"
"v_max_f32 v7, 0, v7\n"
"v_mul_f32_e64 v4, v4, v4\n"
"v_mfma_f32_32x32x16_bf16 v[176:191], v[224:227], v[68:71], v[176:191]\n"
"ds_read_b64_tr_b16 v[220:221], v238 offset:13312\n"
"ds_read_b64_tr_b16 v[222:223], v238 offset:13824\n"
"v_mul_f32_e64 v5, v5, v5\n"
"v_cvt_pk_bf16_f32 v193, v2, v3\n"
"v_max_f32 v8, 0, v8\n"
"s_waitcnt lgkmcnt(4)\n"
"v_mfma_f32_32x32x16_bf16 v[176:191], v[208:211], v[72:75], v[176:191]\n"
"ds_read_b64_tr_b16 v[224:225], v238 offset:14336\n"
"ds_read_b64_tr_b16 v[226:227], v238 offset:14848\n"
"s_add_u32 m0, s55, 135168\n"
"v_max_f32 v9, 0, v9\n"
"v_mul_f32 v6, v6, v6\n"
"v_mfma_f32_32x32x16_bf16 v[176:191], v[212:215], v[76:79], v[176:191]\n"
"ds_read_b64_tr_b16 v[208:209], v238 offset:15360\n"
"ds_read_b64_tr_b16 v[210:211], v238 offset:15872\n"
"v_mul_f32_e64 v7, v7, v7\n"
"v_cvt_pk_bf16_f32 v194, v4, v5\n"
"v_max_f32 v10, 0, v10\n"
"s_waitcnt lgkmcnt(4)\n"
"v_mfma_f32_32x32x16_bf16 v[176:191], v[216:219], v[80:83], v[176:191]\n"
"ds_read_b64_tr_b16 v[212:213], v238 offset:16384\n"
"ds_read_b64_tr_b16 v[214:215], v238 offset:16896\n"
"s_add_u32 s60, s20, 768\n"
"v_max_f32 v11, 0, v11\n"
"v_mul_f32 v8, v8, v8\n"
"v_mfma_f32_32x32x16_bf16 v[176:191], v[220:223], v[84:87], v[176:191]\n"
"ds_read_b64_tr_b16 v[216:217], v238 offset:17408\n"
"ds_read_b64_tr_b16 v[218:219], v238 offset:17920\n"
"v_mul_f32_e64 v9, v9, v9\n"
"v_cvt_pk_bf16_f32 v195, v6, v7\n"
"v_max_f32 v12, 0, v12\n"
"s_waitcnt lgkmcnt(4)\n"
"v_mfma_f32_32x32x16_bf16 v[176:191], v[224:227], v[88:91], v[176:191]\n"
"ds_read_b64_tr_b16 v[220:221], v238 offset:18432\n"
"ds_read_b64_tr_b16 v[222:223], v238 offset:18944\n"
"buffer_load_dwordx4 v232, s[44:47], s60 offen lds\n"
"v_max_f32 v13, 0, v13\n"
"v_mul_f32 v10, v10, v10\n"
"v_mfma_f32_32x32x16_bf16 v[176:191], v[208:211], v[92:95], v[176:191]\n"
"ds_read_b64_tr_b16 v[224:225], v238 offset:19456\n"
"ds_read_b64_tr_b16 v[226:227], v238 offset:19968\n"
"v_mul_f32_e64 v11, v11, v11\n"
"v_cvt_pk_bf16_f32 v196, v8, v9\n"
"v_max_f32 v14, 0, v14\n"
"s_waitcnt lgkmcnt(4)\n"
"v_mfma_f32_32x32x16_bf16 v[176:191], v[212:215], v[96:99], v[176:191]\n"
"ds_read_b64_tr_b16 v[208:209], v238 offset:20480\n"
"ds_read_b64_tr_b16 v[210:211], v238 offset:20992\n"
"buffer_load_dwordx4 v233, s[44:47], s60 offen offset:1024 lds\n"
"v_max_f32 v15, 0, v15\n"
"v_mul_f32 v12, v12, v12\n"
"v_mfma_f32_32x32x16_bf16 v[176:191], v[216:219], v[100:103], v[176:191]\n"
"ds_read_b64_tr_b16 v[212:213], v238 offset:21504\n"
"ds_read_b64_tr_b16 v[214:215], v238 offset:22016\n"
"v_mul_f32_e64 v13, v13, v13\n"
"v_cvt_pk_bf16_f32 v197, v10, v11\n"
"v_mul_f32 v14, v14, v14\n"
"s_waitcnt lgkmcnt(4)\n"
"v_mfma_f32_32x32x16_bf16 v[176:191], v[220:223], v[104:107], v[176:191]\n"
"ds_read_b64_tr_b16 v[216:217], v238 offset:22528\n"
"ds_read_b64_tr_b16 v[218:219], v238 offset:23040\n"
"buffer_load_dwordx4 v234, s[44:47], s60 offen offset:2048 lds\n"
"v_mul_f32_e64 v15, v15, v15\n"
"v_cvt_pk_bf16_f32 v198, v12, v13\n"
"v_mfma_f32_32x32x16_bf16 v[176:191], v[224:227], v[108:111], v[176:191]\n"
"s_waitcnt vmcnt(9)\n"
"ds_read_b64_tr_b16 v[220:221], v238 offset:23552\n"
"ds_read_b64_tr_b16 v[222:223], v238 offset:24064\n"
"v_cvt_pk_bf16_f32 v199, v14, v15\n"
"s_waitcnt lgkmcnt(4)\n"
"v_mfma_f32_32x32x16_bf16 v[176:191], v[208:211], v[112:115], v[176:191]\n"
"s_barrier\n"
"s_lshl_b32 s56, s54, 4\n"
"s_add_u32 s56, s56, s53\n"
"s_lshl_b32 s56, s56, 3\n"
"s_add_u32 s56, s56, 1024\n"
"s_cmp_eq_u32 s52, 0\n"
"s_cselect_b32 s61, s56, 0x7f000000\n"
"v_mov_b32 v250, s62\n"
"v_mov_b32 v251, s63\n"
"v_mov_b32 v253, 0\n"
"buffer_store_dwordx2 v[250:251], v253, s[48:51], s61 offen sc1\n"
"ds_read_b64_tr_b16 v[224:225], v238 offset:24576\n"
"ds_read_b64_tr_b16 v[226:227], v238 offset:25088\n"
"v_mfma_f32_32x32x16_bf16 v[176:191], v[212:215], v[116:119], v[176:191]\n"
"ds_read_b64_tr_b16 v[208:209], v238 offset:25600\n"
"ds_read_b64_tr_b16 v[210:211], v238 offset:26112\n"
"s_waitcnt lgkmcnt(4)\n"
"v_mfma_f32_32x32x16_bf16 v[176:191], v[216:219], v[120:123], v[176:191]\n"
"ds_read_b64_tr_b16 v[212:213], v238 offset:26624\n"
"ds_read_b64_tr_b16 v[214:215], v238 offset:27136\n"
"s_add_u32 m0, s55, 73728\n"
"v_mfma_f32_32x32x16_bf16 v[176:191], v[220:223], v[124:127], v[176:191]\n"
"ds_read_b64_tr_b16 v[216:217], v238 offset:27648\n"
"ds_read_b64_tr_b16 v[218:219], v238 offset:28160\n"
"s_waitcnt lgkmcnt(4)\n"
"v_mfma_f32_32x32x16_bf16 v[176:191], v[224:227], v[128:131], v[176:191]\n"
"ds_read_b64_tr_b16 v[220:221], v238 offset:28672\n"
"ds_read_b64_tr_b16 v[222:223], v238 offset:29184\n"
"s_add_u32 s16, s16, 1\n"
"s_cmp_eq_u32 s16, 9\n"
"s_cselect_b32 s16, 0, s16\n"
"s_mul_i32 s19, s16, 64\n"
"s_add_u32 s60, s19, 0\n"
"v_mfma_f32_32x32x16_bf16 v[176:191], v[208:211], v[132:135], v[176:191]\n"
"ds_read_b64_tr_b16 v[224:225], v238 offset:29696\n"
"ds_read_b64_tr_b16 v[226:227], v238 offset:30208\n"
"s_waitcnt lgkmcnt(4)\n"
"v_mfma_f32_32x32x16_bf16 v[176:191], v[212:215], v[136:139], v[176:191]\n"
"ds_read_b64_tr_b16 v[208:209], v238 offset:30720\n"
"ds_read_b64_tr_b16 v[210:211], v238 offset:31232\n"
"buffer_load_dwordx4 v229, s[40:43], s60 offen lds\n"
"v_mfma_f32_32x32x16_bf16 v[176:191], v[216:219], v[140:143], v[176:191]\n"
"ds_read_b64_tr_b16 v[212:213], v238 offset:31744\n"
"ds_read_b64_tr_b16 v[214:215], v238 offset:32256\n"
"s_waitcnt lgkmcnt(4)\n"
"v_mfma_f32_32x32x16_bf16 v[176:191], v[220:223], v[144:147], v[176:191]\n"
"ds_read_b64_tr_b16 v[216:217], v238 offset:32768\n"
"ds_read_b64_tr_b16 v[218:219], v238 offset:33280\n"
"buffer_load_dwordx4 v230, s[40:43], s60 offen offset:1024 lds\n"
"v_mfma_f32_32x32x16_bf16 v[176:191], v[224:227], v[148:151], v[176:191]\n"
"ds_read_b64_tr_b16 v[220:221], v238 offset:33792\n"
"ds_read_b64_tr_b16 v[222:223], v238 offset:34304\n"
"s_waitcnt lgkmcnt(4)\n"
"v_mfma_f32_32x32x16_bf16 v[176:191], v[208:211], v[152:155], v[176:191]\n"
"ds_read_b64_tr_b16 v[224:225], v238 offset:34816\n"
"ds_read_b64_tr_b16 v[226:227], v238 offset:35328\n"
"buffer_load_dwordx4 v231, s[40:43], s60 offen offset:2048 lds\n"
"v_mfma_f32_32x32x16_bf16 v[176:191], v[212:215], v[156:159], v[176:191]\n"
"s_waitcnt vmcnt(10)\n"
"ds_read_b64_tr_b16 v[208:209], v238 offset:35840\n"
"ds_read_b64_tr_b16 v[210:211], v238 offset:36352\n"
"s_waitcnt lgkmcnt(4)\n"
"v_mfma_f32_32x32x16_bf16 v[176:191], v[216:219], v[160:163], v[176:191]\n"
"s_barrier\n"
"ds_read_b64_tr_b16 v[212:213], v242\n"
"ds_read_b64_tr_b16 v[214:215], v242 offset:3072\n"
"v_mfma_f32_32x32x16_bf16 v[176:191], v[220:223], v[164:167], v[176:191]\n"
"ds_read_b64_tr_b16 v[216:217], v242 offset:6144\n"
"ds_read_b64_tr_b16 v[218:219], v242 offset:9216\n"
"s_waitcnt lgkmcnt(4)\n"
"v_mfma_f32_32x32x16_bf16 v[176:191], v[224:227], v[168:171], v[176:191]\n"
"ds_read_b64_tr_b16 v[220:221], v243 offset:64\n"
"ds_read_b64_tr_b16 v[222:223], v243 offset:3136\n"
"s_add_u32 m0, s55, 86016\n"
"v_mfma_f32_32x32x16_bf16 v[176:191], v[208:211], v[172:175], v[176:191]\n"
"ds_read_b64_tr_b16 v[224:225], v243 offset:6208\n"
"ds_read_b64_tr_b16 v[226:227], v243 offset:9280\n"
"s_waitcnt lgkmcnt(4)\n"
"v_mfma_f32_32x32x16_bf16 a[0:15], v[212:215], v[192:195], 0\n"
"ds_read_b64_tr_b16 v[208:209], v242 offset:128\n"
"ds_read_b64_tr_b16 v[210:211], v242 offset:3200\n"
"s_add_u32 s60, s19, 1769472\n"
"v_mfma_f32_32x32x16_bf16 a[0:15], v[216:219], v[196:199], a[0:15]\n"
"ds_read_b64_tr_b16 v[212:213], v242 offset:6272\n"
"ds_read_b64_tr_b16 v[214:215], v242 offset:9344\n"
"s_waitcnt lgkmcnt(4)\n"
"v_mfma_f32_32x32x16_bf16 a[16:31], v[220:223], v[192:195], 0\n"
"ds_read_b64_tr_b16 v[216:217], v243 offset:192\n"
"ds_read_b64_tr_b16 v[218:219], v243 offset:3264\n"
"buffer_load_dwordx4 v229, s[40:43], s60 offen lds\n"
"v_mfma_f32_32x32x16_bf16 a[16:31], v[224:227], v[196:199], a[16:31]\n"
"ds_read_b64_tr_b16 v[220:221], v243 offset:6336\n"
"ds_read_b64_tr_b16 v[222:223], v243 offset:9408\n"
"v_max_f32 v176, 0, v176\n"
"v_max_f32 v177, 0, v177\n"
"v_max_f32 v178, 0, v178\n"
"s_waitcnt lgkmcnt(4)\n"
"v_mfma_f32_32x32x16_bf16 a[32:47], v[208:211], v[192:195], 0\n"
"ds_read_b64_tr_b16 v[224:225], v242 offset:256\n"
"ds_read_b64_tr_b16 v[226:227], v242 offset:3328\n"
"buffer_load_dwordx4 v230, s[40:43], s60 offen offset:1024 lds\n"
"v_max_f32 v179, 0, v179\n"
"v_mul_f32 v176, v176, v176\n"
"v_mfma_f32_32x32x16_bf16 a[32:47], v[212:215], v[196:199], a[32:47]\n"
"ds_read_b64_tr_b16 v[208:209], v242 offset:6400\n"
"ds_read_b64_tr_b16 v[210:211], v242 offset:9472\n"
"v_mul_f32 v177, v177, v177\n"
"v_max_f32 v180, 0, v180\n"
"v_max_f32 v181, 0, v181\n"
"s_waitcnt lgkmcnt(4)\n"
"v_mfma_f32_32x32x16_bf16 a[48:63], v[216:219], v[192:195], 0\n"
"ds_read_b64_tr_b16 v[212:213], v243 offset:320\n"
"ds_read_b64_tr_b16 v[214:215], v243 offset:3392\n"
"buffer_load_dwordx4 v231, s[40:43], s60 offen offset:2048 lds\n"
"v_mul_f32 v178, v178, v178\n"
"s_waitcnt vmcnt(10)\n"
"v_mul_f32_e64 v179, v179, v179\n"
"v_mfma_f32_32x32x16_bf16 a[48:63], v[220:223], v[196:199], a[48:63]\n"
"ds_read_b64_tr_b16 v[216:217], v243 offset:6464\n"
"ds_read_b64_tr_b16 v[218:219], v243 offset:9536\n"
"v_cvt_pk_bf16_f32 v200, v176, v177\n"
"v_max_f32 v182, 0, v182\n"
"s_waitcnt lgkmcnt(4)\n"
"v_mfma_f32_32x32x16_bf16 a[64:79], v[224:227], v[192:195], 0\n"
"s_barrier\n"
"ds_read_b64_tr_b16 v[220:221], v242 offset:12288\n"
"ds_read_b64_tr_b16 v[222:223], v242 offset:15360\n"
"v_max_f32 v183, 0, v183\n"
"v_mul_f32_e64 v180, v180, v180\n"
"v_mfma_f32_32x32x16_bf16 a[64:79], v[208:211], v[196:199], a[64:79]\n"
"ds_read_b64_tr_b16 v[224:225], v242 offset:18432\n"
"ds_read_b64_tr_b16 v[226:227], v242 offset:21504\n"
"v_mul_f32_e64 v181, v181, v181\n"
"v_cvt_pk_bf16_f32 v201, v178, v179\n"
"v_max_f32 v184, 0, v184\n"
"s_waitcnt lgkmcnt(4)\n"
"v_mfma_f32_32x32x16_bf16 a[80:95], v[212:215], v[192:195], 0\n"
"ds_read_b64_tr_b16 v[208:209], v243 offset:12352\n"
"ds_read_b64_tr_b16 v[210:211], v243 offset:15424\n"
"s_add_u32 m0, s55, 98304\n"
"v_max_f32 v185, 0, v185\n"
"v_mul_f32 v182, v182, v182\n"
"v_mfma_f32_32x32x16_bf16 a[80:95], v[216:219], v[196:199], a[80:95]\n"
"ds_read_b64_tr_b16 v[212:213], v243 offset:18496\n"
"ds_read_b64_tr_b16 v[214:215], v243 offset:21568\n"
"v_mul_f32_e64 v183, v183, v183\n"
"v_cvt_pk_bf16_f32 v202, v180, v181\n"
"v_max_f32 v186, 0, v186\n"
"s_waitcnt lgkmcnt(4)\n"
"v_mfma_f32_32x32x16_bf16 a[96:111], v[220:223], v[192:195], 0\n"
"ds_read_b64_tr_b16 v[216:217], v242 offset:12416\n"
"ds_read_b64_tr_b16 v[218:219], v242 offset:15488\n"
"s_add_u32 s60, s19, 3538944\n"
"v_max_f32 v187, 0, v187\n"
"v_mul_f32 v184, v184, v184\n"
"v_mfma_f32_32x32x16_bf16 a[96:111], v[224:227], v[196:199], a[96:111]\n"
"ds_read_b64_tr_b16 v[220:221], v242 offset:18560\n"
"ds_read_b64_tr_b16 v[222:223], v242 offset:21632\n"
"v_mul_f32_e64 v185, v185, v185\n"
"v_cvt_pk_bf16_f32 v203, v182, v183\n"
"v_max_f32 v188, 0, v188\n"
"s_waitcnt lgkmcnt(4)\n"
"v_mfma_f32_32x32x16_bf16 a[112:127], v[208:211], v[192:195], 0\n"
"ds_read_b64_tr_b16 v[224:225], v243 offset:12480\n"
"ds_read_b64_tr_b16 v[226:227], v243 offset:15552\n"
"buffer_load_dwordx4 v229, s[40:43], s60 offen lds\n"
"v_max_f32 v189, 0, v189\n"
"v_mul_f32 v186, v186, v186\n"
"v_mfma_f32_32x32x16_bf16 a[112:127], v[212:215], v[196:199], a[112:127]\n"
"ds_read_b64_tr_b16 v[208:209], v243 offset:18624\n"
"ds_read_b64_tr_b16 v[210:211], v243 offset:21696\n"
"v_mul_f32_e64 v187, v187, v187\n"
"v_cvt_pk_bf16_f32 v204, v184, v185\n"
"v_max_f32 v190, 0, v190\n"
"s_waitcnt lgkmcnt(4)\n"
"v_mfma_f32_32x32x16_bf16 a[128:143], v[216:219], v[192:195], 0\n"
"ds_read_b64_tr_b16 v[212:213], v242 offset:12544\n"
"ds_read_b64_tr_b16 v[214:215], v242 offset:15616\n"
"buffer_load_dwordx4 v230, s[40:43], s60 offen offset:1024 lds\n"
"v_max_f32 v191, 0, v191\n"
"v_mul_f32 v188, v188, v188\n"
"v_mfma_f32_32x32x16_bf16 a[128:143], v[220:223], v[196:199], a[128:143]\n"
"ds_read_b64_tr_b16 v[216:217], v242 offset:18688\n"
"ds_read_b64_tr_b16 v[218:219], v242 offset:21760\n"
"v_mul_f32_e64 v189, v189, v189\n"
"v_cvt_pk_bf16_f32 v205, v186, v187\n"
"v_mul_f32 v190, v190, v190\n"
"s_waitcnt lgkmcnt(4)\n"
"v_mfma_f32_32x32x16_bf16 a[144:159], v[224:227], v[192:195], 0\n"
"ds_read_b64_tr_b16 v[220:221], v243 offset:12608\n"
"ds_read_b64_tr_b16 v[222:223], v243 offset:15680\n"
"buffer_load_dwordx4 v231, s[40:43], s60 offen offset:2048 lds\n"
"v_mul_f32_e64 v191, v191, v191\n"
"v_cvt_pk_bf16_f32 v206, v188, v189\n"
"v_mfma_f32_32x32x16_bf16 a[144:159], v[208:211], v[196:199], a[144:159]\n"
"s_waitcnt vmcnt(10)\n"
"ds_read_b64_tr_b16 v[224:225], v243 offset:18752\n"
"ds_read_b64_tr_b16 v[226:227], v243 offset:21824\n"
"v_cvt_pk_bf16_f32 v207, v190, v191\n"
"ds_read_b128 v[176:179], v248\n"
"s_waitcnt lgkmcnt(5)\n"
"v_mfma_f32_32x32x16_bf16 a[160:175], v[212:215], v[192:195], 0\n"
"s_barrier\n"
"ds_read_b64_tr_b16 v[208:209], v242 offset:24576\n"
"ds_read_b64_tr_b16 v[210:211], v242 offset:27648\n"
"ds_read_b128 v[180:183], v248 offset:32\n"
"ds_read_b128 v[184:187], v248 offset:64\n"
"v_mfma_f32_32x32x16_bf16 a[160:175], v[216:219], v[196:199], a[160:175]\n"
"ds_read_b64_tr_b16 v[212:213], v242 offset:30720\n"
"ds_read_b64_tr_b16 v[214:215], v242 offset:33792\n"
"ds_read_b128 v[188:191], v248 offset:96\n"
"s_add_u32 s18, s18, 1\n"
"s_cmp_eq_u32 s18, 9\n"
"s_mov_b32 s21, 128\n"
"s_cselect_b32 s21, -1024, s21\n"
"s_cselect_b32 s18, 0, s18\n"
"v_add_u32_e64 v248, s21, v248\n"
"s_waitcnt lgkmcnt(7)\n"
"v_mul_f32 v176, v249, v176\n"
"v_mfma_f32_32x32x16_bf16 a[176:191], v[220:223], v[192:195], 0\n"
"ds_read_b64_tr_b16 v[216:217], v243 offset:24640\n"
"ds_read_b64_tr_b16 v[218:219], v243 offset:27712\n"
"s_add_u32 m0, s55, 110592\n"
"v_mul_f32 v177, v249, v177\n"
"v_mul_f32 v178, v249, v178\n"
"v_mfma_f32_32x32x16_bf16 a[176:191], v[224:227], v[196:199], a[176:191]\n"
"ds_read_b64_tr_b16 v[220:221], v243 offset:30784\n"
"ds_read_b64_tr_b16 v[222:223], v243 offset:33856\n"
"v_mul_f32_e64 v179, v249, v179\n"
"s_waitcnt lgkmcnt(8)\n"
"v_mul_f32 v180, v249, v180\n"
"v_mul_f32 v181, v249, v181\n"
"s_waitcnt lgkmcnt(5)\n"
"v_mfma_f32_32x32x16_bf16 a[192:207], v[208:211], v[192:195], 0\n"
"ds_read_b64_tr_b16 v[224:225], v242 offset:24704\n"
"ds_read_b64_tr_b16 v[226:227], v242 offset:27776\n"
"s_add_u32 s17, s17, 1\n"
"s_cmp_eq_u32 s17, 9\n"
"s_cselect_b32 s17, 0, s17\n"
"s_mul_i32 s20, s17, 36864\n"
"s_add_u32 s60, s20, 0\n"
"v_mul_f32 v182, v249, v182\n"
"v_mul_f32 v183, v249, v183\n"
"v_mfma_f32_32x32x16_bf16 a[192:207], v[212:215], v[196:199], a[192:207]\n"
"ds_read_b64_tr_b16 v[208:209], v242 offset:30848\n"
"ds_read_b64_tr_b16 v[210:211], v242 offset:33920\n"
"v_mul_f32 v184, v249, v184\n"
"v_mul_f32 v185, v249, v185\n"
"v_mul_f32 v186, v249, v186\n"
"s_waitcnt lgkmcnt(4)\n"
"v_mfma_f32_32x32x16_bf16 a[208:223], v[216:219], v[192:195], 0\n"
"ds_read_b64_tr_b16 v[212:213], v243 offset:24768\n"
"ds_read_b64_tr_b16 v[214:215], v243 offset:27840\n"
"buffer_load_dwordx4 v232, s[44:47], s60 offen lds\n"
"v_mul_f32 v187, v249, v187\n"
"v_mul_f32 v188, v249, v188\n"
"v_mfma_f32_32x32x16_bf16 a[208:223], v[220:223], v[196:199], a[208:223]\n"
"ds_read_b64_tr_b16 v[216:217], v243 offset:30912\n"
"ds_read_b64_tr_b16 v[218:219], v243 offset:33984\n"
"v_mul_f32 v189, v249, v189\n"
"v_mul_f32 v190, v249, v190\n"
"v_mul_f32 v191, v249, v191\n"
"s_waitcnt lgkmcnt(4)\n"
"v_mfma_f32_32x32x16_bf16 a[224:239], v[224:227], v[192:195], 0\n"
"ds_read_b64_tr_b16 v[220:221], v242 offset:24832\n"
"ds_read_b64_tr_b16 v[222:223], v242 offset:27904\n"
"buffer_load_dwordx4 v233, s[44:47], s60 offen offset:1024 lds\n"
"v_mfma_f32_32x32x16_bf16 a[224:239], v[208:211], v[196:199], a[224:239]\n"
"ds_read_b64_tr_b16 v[224:225], v242 offset:30976\n"
"ds_read_b64_tr_b16 v[226:227], v242 offset:34048\n"
"s_waitcnt lgkmcnt(4)\n"
"v_mfma_f32_32x32x16_bf16 a[240:255], v[212:215], v[192:195], 0\n"
"ds_read_b64_tr_b16 v[208:209], v243 offset:24896\n"
"ds_read_b64_tr_b16 v[210:211], v243 offset:27968\n"
"buffer_load_dwordx4 v234, s[44:47], s60 offen offset:2048 lds\n"
"v_mfma_f32_32x32x16_bf16 a[240:255], v[216:219], v[196:199], a[240:255]\n"
"s_waitcnt vmcnt(9)\n"
"ds_read_b64_tr_b16 v[212:213], v243 offset:31040\n"
"ds_read_b64_tr_b16 v[214:215], v243 offset:34112\n"
"s_waitcnt lgkmcnt(4)\n"
"v_mfma_f32_32x32x16_bf16 v[0:15], v[220:223], v[192:195], 0\n"
"s_barrier\n"
"ds_read_b64_tr_b16 v[216:217], v238\n"
"ds_read_b64_tr_b16 v[218:219], v238 offset:512\n"
"v_mfma_f32_32x32x16_bf16 v[0:15], v[224:227], v[196:199], v[0:15]\n"
"ds_read_b64_tr_b16 v[220:221], v238 offset:1024\n"
"ds_read_b64_tr_b16 v[222:223], v238 offset:1536\n"
"s_waitcnt lgkmcnt(4)\n"
"v_mfma_f32_32x32x16_bf16 v[16:31], v[208:211], v[192:195], 0\n"
"ds_read_b64_tr_b16 v[224:225], v238 offset:2048\n"
"ds_read_b64_tr_b16 v[226:227], v238 offset:2560\n"
"s_add_u32 m0, s55, 122880\n"
"v_mfma_f32_32x32x16_bf16 v[16:31], v[212:215], v[196:199], v[16:31]\n"
"ds_read_b64_tr_b16 v[208:209], v238 offset:3072\n"
"ds_read_b64_tr_b16 v[210:211], v238 offset:3584\n"
"s_waitcnt lgkmcnt(4)\n"
"v_mfma_f32_32x32x16_bf16 v[176:191], v[216:219], v[32:35], v[176:191]\n"
"ds_read_b64_tr_b16 v[212:213], v238 offset:4096\n"
"ds_read_b64_tr_b16 v[214:215], v238 offset:4608\n"
"s_add_u32 s60, s20, 384\n"
"v_mfma_f32_32x32x16_bf16 v[176:191], v[220:223], v[36:39], v[176:191]\n"
"ds_read_b64_tr_b16 v[216:217], v238 offset:5120\n"
"ds_read_b64_tr_b16 v[218:219], v238 offset:5632\n"
"s_waitcnt lgkmcnt(4)\n"
"v_mfma_f32_32x32x16_bf16 v[176:191], v[224:227], v[40:43], v[176:191]\n"
"ds_read_b64_tr_b16 v[220:221], v238 offset:6144\n"
"ds_read_b64_tr_b16 v[222:223], v238 offset:6656\n"
"buffer_load_dwordx4 v232, s[44:47], s60 offen lds\n"
"v_mfma_f32_32x32x16_bf16 v[176:191], v[208:211], v[44:47], v[176:191]\n"
"ds_read_b64_tr_b16 v[224:225], v238 offset:7168\n"
"ds_read_b64_tr_b16 v[226:227], v238 offset:7680\n"
"s_waitcnt lgkmcnt(4)\n"
"v_mfma_f32_32x32x16_bf16 v[176:191], v[212:215], v[48:51], v[176:191]\n"
"ds_read_b64_tr_b16 v[208:209], v238 offset:8192\n"
"ds_read_b64_tr_b16 v[210:211], v238 offset:8704\n"
"buffer_load_dwordx4 v233, s[44:47], s60 offen offset:1024 lds\n"
"v_mfma_f32_32x32x16_bf16 v[176:191], v[216:219], v[52:55], v[176:191]\n"
"ds_read_b64_tr_b16 v[212:213], v238 offset:9216\n"
"ds_read_b64_tr_b16 v[214:215], v238 offset:9728\n"
"s_waitcnt lgkmcnt(4)\n"
"v_mfma_f32_32x32x16_bf16 v[176:191], v[220:223], v[56:59], v[176:191]\n"
"ds_read_b64_tr_b16 v[216:217], v238 offset:10240\n"
"ds_read_b64_tr_b16 v[218:219], v238 offset:10752\n"
"buffer_load_dwordx4 v234, s[44:47], s60 offen offset:2048 lds\n"
"v_mfma_f32_32x32x16_bf16 v[176:191], v[224:227], v[60:63], v[176:191]\n"
"s_waitcnt vmcnt(9)\n"
"ds_read_b64_tr_b16 v[220:221], v238 offset:11264\n"
"ds_read_b64_tr_b16 v[222:223], v238 offset:11776\n"
"s_waitcnt lgkmcnt(4)\n"
"v_mfma_f32_32x32x16_bf16 v[176:191], v[208:211], v[64:67], v[176:191]\n"
"s_barrier\n"
"ds_read_b64_tr_b16 v[224:225], v238 offset:12288\n"
"ds_read_b64_tr_b16 v[226:227], v238 offset:12800\n"
"v_mfma_f32_32x32x16_bf16 v[176:191], v[212:215], v[68:71], v[176:191]\n"
"ds_read_b64_tr_b16 v[208:209], v238 offset:13312\n"
"ds_read_b64_tr_b16 v[210:211], v238 offset:13824\n"
"s_waitcnt lgkmcnt(4)\n"
"v_mfma_f32_32x32x16_bf16 v[176:191], v[216:219], v[72:75], v[176:191]\n"
"ds_read_b64_tr_b16 v[212:213], v238 offset:14336\n"
"ds_read_b64_tr_b16 v[214:215], v238 offset:14848\n"
"s_add_u32 m0, s55, 135168\n"
"v_mfma_f32_32x32x16_bf16 v[176:191], v[220:223], v[76:79], v[176:191]\n"
"ds_read_b64_tr_b16 v[216:217], v238 offset:15360\n"
"ds_read_b64_tr_b16 v[218:219], v238 offset:15872\n"
"s_waitcnt lgkmcnt(4)\n"
"v_mfma_f32_32x32x16_bf16 v[176:191], v[224:227], v[80:83], v[176:191]\n"
"ds_read_b64_tr_b16 v[220:221], v238 offset:16384\n"
"ds_read_b64_tr_b16 v[222:223], v238 offset:16896\n"
"s_add_u32 s60, s20, 768\n"
"v_mfma_f32_32x32x16_bf16 v[176:191], v[208:211], v[84:87], v[176:191]\n"
"ds_read_b64_tr_b16 v[224:225], v238 offset:17408\n"
"ds_read_b64_tr_b16 v[226:227], v238 offset:17920\n"
"s_waitcnt lgkmcnt(4)\n"
"v_mfma_f32_32x32x16_bf16 v[176:191], v[212:215], v[88:91], v[176:191]\n"
"ds_read_b64_tr_b16 v[208:209], v238 offset:18432\n"
"ds_read_b64_tr_b16 v[210:211], v238 offset:18944\n"
"buffer_load_dwordx4 v232, s[44:47], s60 offen lds\n"
"v_mfma_f32_32x32x16_bf16 v[176:191], v[216:219], v[92:95], v[176:191]\n"
"ds_read_b64_tr_b16 v[212:213], v238 offset:19456\n"
"ds_read_b64_tr_b16 v[214:215], v238 offset:19968\n"
"s_waitcnt lgkmcnt(4)\n"
"v_mfma_f32_32x32x16_bf16 v[176:191], v[220:223], v[96:99], v[176:191]\n"
"ds_read_b64_tr_b16 v[216:217], v238 offset:20480\n"
"ds_read_b64_tr_b16 v[218:219], v238 offset:20992\n"
"buffer_load_dwordx4 v233, s[44:47], s60 offen offset:1024 lds\n"
"v_mfma_f32_32x32x16_bf16 v[176:191], v[224:227], v[100:103], v[176:191]\n"
"ds_read_b64_tr_b16 v[220:221], v238 offset:21504\n"
"ds_read_b64_tr_b16 v[222:223], v238 offset:22016\n"
"s_waitcnt lgkmcnt(4)\n"
"v_mfma_f32_32x32x16_bf16 v[176:191], v[208:211], v[104:107], v[176:191]\n"
"ds_read_b64_tr_b16 v[224:225], v238 offset:22528\n"
"ds_read_b64_tr_b16 v[226:227], v238 offset:23040\n"
"buffer_load_dwordx4 v234, s[44:47], s60 offen offset:2048 lds\n"
"v_mfma_f32_32x32x16_bf16 v[176:191], v[212:215], v[108:111], v[176:191]\n"
"s_waitcnt vmcnt(9)\n"
"ds_read_b64_tr_b16 v[208:209], v238 offset:23552\n"
"ds_read_b64_tr_b16 v[210:211], v238 offset:24064\n"
"s_waitcnt lgkmcnt(4)\n"
"v_mfma_f32_32x32x16_bf16 v[176:191], v[216:219], v[112:115], v[176:191]\n"
"s_barrier\n"
"ds_read_b64_tr_b16 v[212:213], v238 offset:24576\n"
"ds_read_b64_tr_b16 v[214:215], v238 offset:25088\n"
"v_mfma_f32_32x32x16_bf16 v[176:191], v[220:223], v[116:119], v[176:191]\n"
"ds_read_b64_tr_b16 v[216:217], v238 offset:25600\n"
"ds_read_b64_tr_b16 v[218:219], v238 offset:26112\n"
"s_waitcnt lgkmcnt(4)\n"
"v_mfma_f32_32x32x16_bf16 v[176:191], v[224:227], v[120:123], v[176:191]\n"
"ds_read_b64_tr_b16 v[220:221], v238 offset:26624\n"
"ds_read_b64_tr_b16 v[222:223], v238 offset:27136\n"
"s_add_u32 m0, s55, 73728\n"
"v_mfma_f32_32x32x16_bf16 v[176:191], v[208:211], v[124:127], v[176:191]\n"
"ds_read_b64_tr_b16 v[224:225], v238 offset:27648\n"
"ds_read_b64_tr_b16 v[226:227], v238 offset:28160\n"
"s_waitcnt lgkmcnt(4)\n"
"v_mfma_f32_32x32x16_bf16 v[176:191], v[212:215], v[128:131], v[176:191]\n"
"ds_read_b64_tr_b16 v[208:209], v238 offset:28672\n"
"ds_read_b64_tr_b16 v[210:211], v238 offset:29184\n"
"s_add_u32 s16, s16, 1\n"
"s_cmp_eq_u32 s16, 9\n"
"s_cselect_b32 s16, 0, s16\n"
"s_mul_i32 s19, s16, 64\n"
"s_add_u32 s60, s19, 0\n"
"v_mfma_f32_32x32x16_bf16 v[176:191], v[216:219], v[132:135], v[176:191]\n"
"ds_read_b64_tr_b16 v[212:213], v238 offset:29696\n"
"ds_read_b64_tr_b16 v[214:215], v238 offset:30208\n"
"s_waitcnt lgkmcnt(4)\n"
"v_mfma_f32_32x32x16_bf16 v[176:191], v[220:223], v[136:139], v[176:191]\n"
"ds_read_b64_tr_b16 v[216:217], v238 offset:30720\n"
"ds_read_b64_tr_b16 v[218:219], v238 offset:31232\n"
"buffer_load_dwordx4 v229, s[40:43], s60 offen lds\n"
"v_mfma_f32_32x32x16_bf16 v[176:191], v[224:227], v[140:143], v[176:191]\n"
"ds_read_b64_tr_b16 v[220:221], v238 offset:31744\n"
"ds_read_b64_tr_b16 v[222:223], v238 offset:32256\n"
"s_waitcnt lgkmcnt(4)\n"
"v_mfma_f32_32x32x16_bf16 v[176:191], v[208:211], v[144:147], v[176:191]\n"
"ds_read_b64_tr_b16 v[224:225], v238 offset:32768\n"
"ds_read_b64_tr_b16 v[226:227], v238 offset:33280\n"
"buffer_load_dwordx4 v230, s[40:43], s60 offen offset:1024 lds\n"
"v_mfma_f32_32x32x16_bf16 v[176:191], v[212:215], v[148:151], v[176:191]\n"
"ds_read_b64_tr_b16 v[208:209], v238 offset:33792\n"
"ds_read_b64_tr_b16 v[210:211], v238 offset:34304\n"
"s_waitcnt lgkmcnt(4)\n"
"v_mfma_f32_32x32x16_bf16 v[176:191], v[216:219], v[152:155], v[176:191]\n"
"ds_read_b64_tr_b16 v[212:213], v238 offset:34816\n"
"ds_read_b64_tr_b16 v[214:215], v238 offset:35328\n"
"buffer_load_dwordx4 v231, s[40:43], s60 offen offset:2048 lds\n"
"v_mfma_f32_32x32x16_bf16 v[176:191], v[220:223], v[156:159], v[176:191]\n"
"s_waitcnt vmcnt(9)\n"
"ds_read_b64_tr_b16 v[216:217], v238 offset:35840\n"
"ds_read_b64_tr_b16 v[218:219], v238 offset:36352\n"
"s_waitcnt lgkmcnt(4)\n"
"v_mfma_f32_32x32x16_bf16 v[176:191], v[224:227], v[160:163], v[176:191]\n"
"s_barrier\n"
"ds_read_b64_tr_b16 v[220:221], v242\n"
"ds_read_b64_tr_b16 v[222:223], v242 offset:3072\n"
"v_mfma_f32_32x32x16_bf16 v[176:191], v[208:211], v[164:167], v[176:191]\n"
"ds_read_b64_tr_b16 v[224:225], v242 offset:6144\n"
"ds_read_b64_tr_b16 v[226:227], v242 offset:9216\n"
"s_waitcnt lgkmcnt(4)\n"
"v_mfma_f32_32x32x16_bf16 v[176:191], v[212:215], v[168:171], v[176:191]\n"
"ds_read_b64_tr_b16 v[208:209], v243 offset:64\n"
"ds_read_b64_tr_b16 v[210:211], v243 offset:3136\n"
"s_add_u32 m0, s55, 86016\n"
"v_mfma_f32_32x32x16_bf16 v[176:191], v[216:219], v[172:175], v[176:191]\n"
"ds_read_b64_tr_b16 v[212:213], v243 offset:6208\n"
"ds_read_b64_tr_b16 v[214:215], v243 offset:9280\n"
"s_waitcnt lgkmcnt(4)\n"
"v_mfma_f32_32x32x16_bf16 a[0:15], v[220:223], v[200:203], a[0:15]\n"
"ds_read_b64_tr_b16 v[216:217], v242 offset:128\n"
"ds_read_b64_tr_b16 v[218:219], v242 offset:3200\n"
"s_add_u32 s60, s19, 1769472\n"
"v_mfma_f32_32x32x16_bf16 a[0:15], v[224:227], v[204:207], a[0:15]\n"
"ds_read_b64_tr_b16 v[220:221], v242 offset:6272\n"
"ds_read_b64_tr_b16 v[222:223], v242 offset:9344\n"
"s_waitcnt lgkmcnt(4)\n"
"v_mfma_f32_32x32x16_bf16 a[16:31], v[208:211], v[200:203], a[16:31]\n"
"ds_read_b64_tr_b16 v[224:225], v243 offset:192\n"
"ds_read_b64_tr_b16 v[226:227], v243 offset:3264\n"
"buffer_load_dwordx4 v229, s[40:43], s60 offen lds\n"
"v_mfma_f32_32x32x16_bf16 a[16:31], v[212:215], v[204:207], a[16:31]\n"
"ds_read_b64_tr_b16 v[208:209], v243 offset:6336\n"
"ds_read_b64_tr_b16 v[210:211], v243 offset:9408\n"
"v_max_f32 v176, 0, v176\n"
"v_max_f32 v177, 0, v177\n"
"v_max_f32 v178, 0, v178\n"
"s_waitcnt lgkmcnt(4)\n"
"v_mfma_f32_32x32x16_bf16 a[32:47], v[216:219], v[200:203], a[32:47]\n"
"ds_read_b64_tr_b16 v[212:213], v242 offset:256\n"
"ds_read_b64_tr_b16 v[214:215], v242 offset:3328\n"
"buffer_load_dwordx4 v230, s[40:43], s60 offen offset:1024 lds\n"
"v_max_f32 v179, 0, v179\n"
"v_mul_f32 v176, v176, v176\n"
"v_mfma_f32_32x32x16_bf16 a[32:47], v[220:223], v[204:207], a[32:47]\n"
"ds_read_b64_tr_b16 v[216:217], v242 offset:6400\n"
"ds_read_b64_tr_b16 v[218:219], v242 offset:9472\n"
"v_mul_f32 v177, v177, v177\n"
"v_max_f32 v180, 0, v180\n"
"v_max_f32 v181, 0, v181\n"
"s_waitcnt lgkmcnt(4)\n"
"v_mfma_f32_32x32x16_bf16 a[48:63], v[224:227], v[200:203], a[48:63]\n"
"ds_read_b64_tr_b16 v[220:221], v243 offset:320\n"
"ds_read_b64_tr_b16 v[222:223], v243 offset:3392\n"
"buffer_load_dwordx4 v231, s[40:43], s60 offen offset:2048 lds\n"
"v_mul_f32 v178, v178, v178\n"
"s_waitcnt vmcnt(9)\n"
"v_mul_f32_e64 v179, v179, v179\n"
"v_mfma_f32_32x32x16_bf16 a[48:63], v[208:211], v[204:207], a[48:63]\n"
"ds_read_b64_tr_b16 v[224:225], v243 offset:6464\n"
"ds_read_b64_tr_b16 v[226:227], v243 offset:9536\n"
"v_cvt_pk_bf16_f32 v192, v176, v177\n"
"v_max_f32 v182, 0, v182\n"
"s_waitcnt lgkmcnt(4)\n"
"v_mfma_f32_32x32x16_bf16 a[64:79], v[212:215], v[200:203], a[64:79]\n"
"s_barrier\n"
"ds_read_b64_tr_b16 v[208:209], v242 offset:12288\n"
"ds_read_b64_tr_b16 v[210:211], v242 offset:15360\n"
"v_max_f32 v183, 0, v183\n"
"v_mul_f32_e64 v180, v180, v180\n"
"v_mfma_f32_32x32x16_bf16 a[64:79], v[216:219], v[204:207], a[64:79]\n"
"ds_read_b64_tr_b16 v[212:213], v242 offset:18432\n"
"ds_read_b64_tr_b16 v[214:215], v242 offset:21504\n"
"v_mul_f32_e64 v181, v181, v181\n"
"v_cvt_pk_bf16_f32 v193, v178, v179\n"
"v_max_f32 v184, 0, v184\n"
"s_waitcnt lgkmcnt(4)\n"
"v_mfma_f32_32x32x16_bf16 a[80:95], v[220:223], v[200:203], a[80:95]\n"
"ds_read_b64_tr_b16 v[216:217], v243 offset:12352\n"
"ds_read_b64_tr_b16 v[218:219], v243 offset:15424\n"
"s_add_u32 m0, s55, 98304\n"
"v_max_f32 v185, 0, v185\n"
"v_mul_f32 v182, v182, v182\n"
"v_mfma_f32_32x32x16_bf16 a[80:95], v[224:227], v[204:207], a[80:95]\n"
"ds_read_b64_tr_b16 v[220:221], v243 offset:18496\n"
"ds_read_b64_tr_b16 v[222:223], v243 offset:21568\n"
"v_mul_f32_e64 v183, v183, v183\n"
"v_cvt_pk_bf16_f32 v194, v180, v181\n"
"v_max_f32 v186, 0, v186\n"
"s_waitcnt lgkmcnt(4)\n"
"v_mfma_f32_32x32x16_bf16 a[96:111], v[208:211], v[200:203], a[96:111]\n"
"ds_read_b64_tr_b16 v[224:225], v242 offset:12416\n"
"ds_read_b64_tr_b16 v[226:227], v242 offset:15488\n"
"s_add_u32 s60, s19, 3538944\n"
"v_max_f32 v187, 0, v187\n"
"v_mul_f32 v184, v184, v184\n"
"v_mfma_f32_32x32x16_bf16 a[96:111], v[212:215], v[204:207], a[96:111]\n"
"ds_read_b64_tr_b16 v[208:209], v242 offset:18560\n"
"ds_read_b64_tr_b16 v[210:211], v242 offset:21632\n"
"v_mul_f32_e64 v185, v185, v185\n"
"v_cvt_pk_bf16_f32 v195, v182, v183\n"
"v_max_f32 v188, 0, v188\n"
"s_waitcnt lgkmcnt(4)\n"
"v_mfma_f32_32x32x16_bf16 a[112:127], v[216:219], v[200:203], a[112:127]\n"
"ds_read_b64_tr_b16 v[212:213], v243 offset:12480\n"
"ds_read_b64_tr_b16 v[214:215], v243 offset:15552\n"
"buffer_load_dwordx4 v229, s[40:43], s60 offen lds\n"
"v_max_f32 v189, 0, v189\n"
"v_mul_f32 v186, v186, v186\n"
"v_mfma_f32_32x32x16_bf16 a[112:127], v[220:223], v[204:207], a[112:127]\n"
"ds_read_b64_tr_b16 v[216:217], v243 offset:18624\n"
"ds_read_b64_tr_b16 v[218:219], v243 offset:21696\n"
"v_mul_f32_e64 v187, v187, v187\n"
"v_cvt_pk_bf16_f32 v196, v184, v185\n"
"v_max_f32 v190, 0, v190\n"
"s_waitcnt lgkmcnt(4)\n"
"v_mfma_f32_32x32x16_bf16 a[128:143], v[224:227], v[200:203], a[128:143]\n"
"ds_read_b64_tr_b16 v[220:221], v242 offset:12544\n"
"ds_read_b64_tr_b16 v[222:223], v242 offset:15616\n"
"buffer_load_dwordx4 v230, s[40:43], s60 offen offset:1024 lds\n"
"v_max_f32 v191, 0, v191\n"
"v_mul_f32 v188, v188, v188\n"
"v_mfma_f32_32x32x16_bf16 a[128:143], v[208:211], v[204:207], a[128:143]\n"
"ds_read_b64_tr_b16 v[224:225], v242 offset:18688\n"
"ds_read_b64_tr_b16 v[226:227], v242 offset:21760\n"
"v_mul_f32_e64 v189, v189, v189\n"
"v_cvt_pk_bf16_f32 v197, v186, v187\n"
"v_mul_f32 v190, v190, v190\n"
"s_waitcnt lgkmcnt(4)\n"
"v_mfma_f32_32x32x16_bf16 a[144:159], v[212:215], v[200:203], a[144:159]\n"
"ds_read_b64_tr_b16 v[208:209], v243 offset:12608\n"
"ds_read_b64_tr_b16 v[210:211], v243 offset:15680\n"
"buffer_load_dwordx4 v231, s[40:43], s60 offen offset:2048 lds\n"
"v_mul_f32_e64 v191, v191, v191\n"
"v_cvt_pk_bf16_f32 v198, v188, v189\n"
"v_mfma_f32_32x32x16_bf16 a[144:159], v[216:219], v[204:207], a[144:159]\n"
"s_waitcnt vmcnt(9)\n"
"ds_read_b64_tr_b16 v[212:213], v243 offset:18752\n"
"ds_read_b64_tr_b16 v[214:215], v243 offset:21824\n"
"v_cvt_pk_bf16_f32 v199, v190, v191\n"
"ds_read_b128 v[176:179], v248\n"
"s_waitcnt lgkmcnt(5)\n"
"v_mfma_f32_32x32x16_bf16 a[160:175], v[220:223], v[200:203], a[160:175]\n"
"s_barrier\n"
"ds_read_b64_tr_b16 v[216:217], v242 offset:24576\n"
"ds_read_b64_tr_b16 v[218:219], v242 offset:27648\n"
"ds_read_b128 v[180:183], v248 offset:32\n"
"ds_read_b128 v[184:187], v248 offset:64\n"
"v_mfma_f32_32x32x16_bf16 a[160:175], v[224:227], v[204:207], a[160:175]\n"
"ds_read_b64_tr_b16 v[220:221], v242 offset:30720\n"
"ds_read_b64_tr_b16 v[222:223], v242 offset:33792\n"
"ds_read_b128 v[188:191], v248 offset:96\n"
"s_add_u32 s18, s18, 1\n"
"s_cmp_eq_u32 s18, 9\n"
"s_mov_b32 s21, 128\n"
"s_cselect_b32 s21, -1024, s21\n"
"s_cselect_b32 s18, 0, s18\n"
"v_add_u32_e64 v248, s21, v248\n"
"s_waitcnt lgkmcnt(7)\n"
"v_mul_f32 v176, v249, v176\n"
"v_mfma_f32_32x32x16_bf16 a[176:191], v[208:211], v[200:203], a[176:191]\n"
"ds_read_b64_tr_b16 v[224:225], v243 offset:24640\n"
"ds_read_b64_tr_b16 v[226:227], v243 offset:27712\n"
"s_add_u32 m0, s55, 110592\n"
"v_mul_f32 v177, v249, v177\n"
"v_mul_f32 v178, v249, v178\n"
"v_mfma_f32_32x32x16_bf16 a[176:191], v[212:215], v[204:207], a[176:191]\n"
"ds_read_b64_tr_b16 v[208:209], v243 offset:30784\n"
"ds_read_b64_tr_b16 v[210:211], v243 offset:33856\n"
"v_mul_f32_e64 v179, v249, v179\n"
"s_waitcnt lgkmcnt(8)\n"
"v_mul_f32 v180, v249, v180\n"
"v_mul_f32 v181, v249, v181\n"
"s_waitcnt lgkmcnt(5)\n"
"v_mfma_f32_32x32x16_bf16 a[192:207], v[216:219], v[200:203], a[192:207]\n"
"ds_read_b64_tr_b16 v[212:213], v242 offset:24704\n"
"ds_read_b64_tr_b16 v[214:215], v242 offset:27776\n"
"s_add_u32 s17, s17, 1\n"
"s_cmp_eq_u32 s17, 9\n"
"s_cselect_b32 s17, 0, s17\n"
"s_mul_i32 s20, s17, 36864\n"
"s_add_u32 s60, s20, 0\n"
"v_mul_f32 v182, v249, v182\n"
"v_mul_f32 v183, v249, v183\n"
"v_mfma_f32_32x32x16_bf16 a[192:207], v[220:223], v[204:207], a[192:207]\n"
"ds_read_b64_tr_b16 v[216:217], v242 offset:30848\n"
"ds_read_b64_tr_b16 v[218:219], v242 offset:33920\n"
"v_mul_f32 v184, v249, v184\n"
"v_mul_f32 v185, v249, v185\n"
"v_mul_f32 v186, v249, v186\n"
"s_waitcnt lgkmcnt(4)\n"
"v_mfma_f32_32x32x16_bf16 a[208:223], v[224:227], v[200:203], a[208:223]\n"
"ds_read_b64_tr_b16 v[220:221], v243 offset:24768\n"
"ds_read_b64_tr_b16 v[222:223], v243 offset:27840\n"
"buffer_load_dwordx4 v232, s[44:47], s60 offen lds\n"
"v_mul_f32 v187, v249, v187\n"
"v_mul_f32 v188, v249, v188\n"
"v_mfma_f32_32x32x16_bf16 a[208:223], v[208:211], v[204:207], a[208:223]\n"
"ds_read_b64_tr_b16 v[224:225], v243 offset:30912\n"
"ds_read_b64_tr_b16 v[226:227], v243 offset:33984\n"
"v_mul_f32 v189, v249, v189\n"
"v_mul_f32 v190, v249, v190\n"
"v_mul_f32 v191, v249, v191\n"
"s_waitcnt lgkmcnt(4)\n"
"v_mfma_f32_32x32x16_bf16 a[224:239], v[212:215], v[200:203], a[224:239]\n"
"ds_read_b64_tr_b16 v[208:209], v242 offset:24832\n"
"ds_read_b64_tr_b16 v[210:211], v242 offset:27904\n"
"buffer_load_dwordx4 v233, s[44:47], s60 offen offset:1024 lds\n"
"v_mfma_f32_32x32x16_bf16 a[224:239], v[216:219], v[204:207], a[224:239]\n"
"ds_read_b64_tr_b16 v[212:213], v242 offset:30976\n"
"ds_read_b64_tr_b16 v[214:215], v242 offset:34048\n"
"s_waitcnt lgkmcnt(4)\n"
"v_mfma_f32_32x32x16_bf16 a[240:255], v[220:223], v[200:203], a[240:255]\n"
"ds_read_b64_tr_b16 v[216:217], v243 offset:24896\n"
"ds_read_b64_tr_b16 v[218:219], v243 offset:27968\n"
"buffer_load_dwordx4 v234, s[44:47], s60 offen offset:2048 lds\n"
"v_mfma_f32_32x32x16_bf16 a[240:255], v[224:227], v[204:207], a[240:255]\n"
"s_waitcnt vmcnt(9)\n"
"ds_read_b64_tr_b16 v[220:221], v243 offset:31040\n"
"ds_read_b64_tr_b16 v[222:223], v243 offset:34112\n"
"s_waitcnt lgkmcnt(4)\n"
"v_mfma_f32_32x32x16_bf16 v[0:15], v[208:211], v[200:203], v[0:15]\n"
"s_barrier\n"
"ds_read_b64_tr_b16 v[224:225], v238\n"
"ds_read_b64_tr_b16 v[226:227], v238 offset:512\n"
"v_mfma_f32_32x32x16_bf16 v[0:15], v[212:215], v[204:207], v[0:15]\n"
"ds_read_b64_tr_b16 v[208:209], v238 offset:1024\n"
"ds_read_b64_tr_b16 v[210:211], v238 offset:1536\n"
"s_waitcnt lgkmcnt(4)\n"
"v_mfma_f32_32x32x16_bf16 v[16:31], v[216:219], v[200:203], v[16:31]\n"
"ds_read_b64_tr_b16 v[212:213], v238 offset:2048\n"
"ds_read_b64_tr_b16 v[214:215], v238 offset:2560\n"
"s_add_u32 m0, s55, 122880\n"
"v_mfma_f32_32x32x16_bf16 v[16:31], v[220:223], v[204:207], v[16:31]\n"
"ds_read_b64_tr_b16 v[216:217], v238 offset:3072\n"
"ds_read_b64_tr_b16 v[218:219], v238 offset:3584\n"
"s_waitcnt lgkmcnt(4)\n"
"v_mfma_f32_32x32x16_bf16 v[176:191], v[224:227], v[32:35], v[176:191]\n"
"ds_read_b64_tr_b16 v[220:221], v238 offset:4096\n"
"ds_read_b64_tr_b16 v[222:223], v238 offset:4608\n"
"s_add_u32 s60, s20, 384\n"
"v_mfma_f32_32x32x16_bf16 v[176:191], v[208:211], v[36:39], v[176:191]\n"
"ds_read_b64_tr_b16 v[224:225], v238 offset:5120\n"
"ds_read_b64_tr_b16 v[226:227], v238 offset:5632\n"
"s_waitcnt lgkmcnt(4)\n"
"v_mfma_f32_32x32x16_bf16 v[176:191], v[212:215], v[40:43], v[176:191]\n"
"ds_read_b64_tr_b16 v[208:209], v238 offset:6144\n"
"ds_read_b64_tr_b16 v[210:211], v238 offset:6656\n"
"buffer_load_dwordx4 v232, s[44:47], s60 offen lds\n"
"v_mfma_f32_32x32x16_bf16 v[176:191], v[216:219], v[44:47], v[176:191]\n"
"ds_read_b64_tr_b16 v[212:213], v238 offset:7168\n"
"ds_read_b64_tr_b16 v[214:215], v238 offset:7680\n"
"s_waitcnt lgkmcnt(4)\n"
"v_mfma_f32_32x32x16_bf16 v[176:191], v[220:223], v[48:51], v[176:191]\n"
"ds_read_b64_tr_b16 v[216:217], v238 offset:8192\n"
"ds_read_b64_tr_b16 v[218:219], v238 offset:8704\n"
"buffer_load_dwordx4 v233, s[44:47], s60 offen offset:1024 lds\n"
"v_mfma_f32_32x32x16_bf16 v[176:191], v[224:227], v[52:55], v[176:191]\n"
"ds_read_b64_tr_b16 v[220:221], v238 offset:9216\n"
"ds_read_b64_tr_b16 v[222:223], v238 offset:9728\n"
"s_waitcnt lgkmcnt(4)\n"
"v_mfma_f32_32x32x16_bf16 v[176:191], v[208:211], v[56:59], v[176:191]\n"
"ds_read_b64_tr_b16 v[224:225], v238 offset:10240\n"
"ds_read_b64_tr_b16 v[226:227], v238 offset:10752\n"
"buffer_load_dwordx4 v234, s[44:47], s60 offen offset:2048 lds\n"
"v_mfma_f32_32x32x16_bf16 v[176:191], v[212:215], v[60:63], v[176:191]\n"
"s_waitcnt vmcnt(9)\n"
"ds_read_b64_tr_b16 v[208:209], v238 offset:11264\n"
"ds_read_b64_tr_b16 v[210:211], v238 offset:11776\n"
"s_waitcnt lgkmcnt(4)\n"
"v_mfma_f32_32x32x16_bf16 v[176:191], v[216:219], v[64:67], v[176:191]\n"
"s_barrier\n"
"ds_read_b64_tr_b16 v[212:213], v238 offset:12288\n"
"ds_read_b64_tr_b16 v[214:215], v238 offset:12800\n"
"v_mfma_f32_32x32x16_bf16 v[176:191], v[220:223], v[68:71], v[176:191]\n"
"ds_read_b64_tr_b16 v[216:217], v238 offset:13312\n"
"ds_read_b64_tr_b16 v[218:219], v238 offset:13824\n"
"s_waitcnt lgkmcnt(4)\n"
"v_mfma_f32_32x32x16_bf16 v[176:191], v[224:227], v[72:75], v[176:191]\n"
"ds_read_b64_tr_b16 v[220:221], v238 offset:14336\n"
"ds_read_b64_tr_b16 v[222:223], v238 offset:14848\n"
"s_add_u32 m0, s55, 135168\n"
"v_mfma_f32_32x32x16_bf16 v[176:191], v[208:211], v[76:79], v[176:191]\n"
"ds_read_b64_tr_b16 v[224:225], v238 offset:15360\n"
"ds_read_b64_tr_b16 v[226:227], v238 offset:15872\n"
"s_waitcnt lgkmcnt(4)\n"
"v_mfma_f32_32x32x16_bf16 v[176:191], v[212:215], v[80:83], v[176:191]\n"
"ds_read_b64_tr_b16 v[208:209], v238 offset:16384\n"
"ds_read_b64_tr_b16 v[210:211], v238 offset:16896\n"
"s_add_u32 s60, s20, 768\n"
"v_mfma_f32_32x32x16_bf16 v[176:191], v[216:219], v[84:87], v[176:191]\n"
"ds_read_b64_tr_b16 v[212:213], v238 offset:17408\n"
"ds_read_b64_tr_b16 v[214:215], v238 offset:17920\n"
"s_waitcnt lgkmcnt(4)\n"
"v_mfma_f32_32x32x16_bf16 v[176:191], v[220:223], v[88:91], v[176:191]\n"
"ds_read_b64_tr_b16 v[216:217], v238 offset:18432\n"
"ds_read_b64_tr_b16 v[218:219], v238 offset:18944\n"
"buffer_load_dwordx4 v232, s[44:47], s60 offen lds\n"
"v_mfma_f32_32x32x16_bf16 v[176:191], v[224:227], v[92:95], v[176:191]\n"
"ds_read_b64_tr_b16 v[220:221], v238 offset:19456\n"
"ds_read_b64_tr_b16 v[222:223], v238 offset:19968\n"
"s_waitcnt lgkmcnt(4)\n"
"v_mfma_f32_32x32x16_bf16 v[176:191], v[208:211], v[96:99], v[176:191]\n"
"ds_read_b64_tr_b16 v[224:225], v238 offset:20480\n"
"ds_read_b64_tr_b16 v[226:227], v238 offset:20992\n"
"buffer_load_dwordx4 v233, s[44:47], s60 offen offset:1024 lds\n"
"v_mfma_f32_32x32x16_bf16 v[176:191], v[212:215], v[100:103], v[176:191]\n"
"ds_read_b64_tr_b16 v[208:209], v238 offset:21504\n"
"ds_read_b64_tr_b16 v[210:211], v238 offset:22016\n"
"s_waitcnt lgkmcnt(4)\n"
"v_mfma_f32_32x32x16_bf16 v[176:191], v[216:219], v[104:107], v[176:191]\n"
"ds_read_b64_tr_b16 v[212:213], v238 offset:22528\n"
"ds_read_b64_tr_b16 v[214:215], v238 offset:23040\n"
"buffer_load_dwordx4 v234, s[44:47], s60 offen offset:2048 lds\n"
"v_mfma_f32_32x32x16_bf16 v[176:191], v[220:223], v[108:111], v[176:191]\n"
"s_waitcnt vmcnt(9)\n"
"ds_read_b64_tr_b16 v[216:217], v238 offset:23552\n"
"ds_read_b64_tr_b16 v[218:219], v238 offset:24064\n"
"s_waitcnt lgkmcnt(4)\n"
"v_mfma_f32_32x32x16_bf16 v[176:191], v[224:227], v[112:115], v[176:191]\n"
"s_barrier\n"
"ds_read_b64_tr_b16 v[220:221], v238 offset:24576\n"
"ds_read_b64_tr_b16 v[222:223], v238 offset:25088\n"
"v_mfma_f32_32x32x16_bf16 v[176:191], v[208:211], v[116:119], v[176:191]\n"
"ds_read_b64_tr_b16 v[224:225], v238 offset:25600\n"
"ds_read_b64_tr_b16 v[226:227], v238 offset:26112\n"
"s_waitcnt lgkmcnt(4)\n"
"v_mfma_f32_32x32x16_bf16 v[176:191], v[212:215], v[120:123], v[176:191]\n"
"ds_read_b64_tr_b16 v[208:209], v238 offset:26624\n"
"ds_read_b64_tr_b16 v[210:211], v238 offset:27136\n"
"s_add_u32 m0, s55, 73728\n"
"v_mfma_f32_32x32x16_bf16 v[176:191], v[216:219], v[124:127], v[176:191]\n"
"ds_read_b64_tr_b16 v[212:213], v238 offset:27648\n"
"ds_read_b64_tr_b16 v[214:215], v238 offset:28160\n"
"s_waitcnt lgkmcnt(4)\n"
"v_mfma_f32_32x32x16_bf16 v[176:191], v[220:223], v[128:131], v[176:191]\n"
"ds_read_b64_tr_b16 v[216:217], v238 offset:28672\n"
"ds_read_b64_tr_b16 v[218:219], v238 offset:29184\n"
"s_add_u32 s16, s16, 1\n"
"s_cmp_eq_u32 s16, 9\n"
"s_cselect_b32 s16, 0, s16\n"
"s_mul_i32 s19, s16, 64\n"
"s_add_u32 s60, s19, 0\n"
"v_mfma_f32_32x32x16_bf16 v[176:191], v[224:227], v[132:135], v[176:191]\n"
"ds_read_b64_tr_b16 v[220:221], v238 offset:29696\n"
"ds_read_b64_tr_b16 v[222:223], v238 offset:30208\n"
"s_waitcnt lgkmcnt(4)\n"
"v_mfma_f32_32x32x16_bf16 v[176:191], v[208:211], v[136:139], v[176:191]\n"
"ds_read_b64_tr_b16 v[224:225], v238 offset:30720\n"
"ds_read_b64_tr_b16 v[226:227], v238 offset:31232\n"
"buffer_load_dwordx4 v229, s[40:43], s60 offen lds\n"
"v_mfma_f32_32x32x16_bf16 v[176:191], v[212:215], v[140:143], v[176:191]\n"
"ds_read_b64_tr_b16 v[208:209], v238 offset:31744\n"
"ds_read_b64_tr_b16 v[210:211], v238 offset:32256\n"
"s_waitcnt lgkmcnt(4)\n"
"v_mfma_f32_32x32x16_bf16 v[176:191], v[216:219], v[144:147], v[176:191]\n"
"ds_read_b64_tr_b16 v[212:213], v238 offset:32768\n"
"ds_read_b64_tr_b16 v[214:215], v238 offset:33280\n"
"buffer_load_dwordx4 v230, s[40:43], s60 offen offset:1024 lds\n"
"v_mfma_f32_32x32x16_bf16 v[176:191], v[220:223], v[148:151], v[176:191]\n"
"ds_read_b64_tr_b16 v[216:217], v238 offset:33792\n"
"ds_read_b64_tr_b16 v[218:219], v238 offset:34304\n"
"s_waitcnt lgkmcnt(4)\n"
"v_mfma_f32_32x32x16_bf16 v[176:191], v[224:227], v[152:155], v[176:191]\n"
"ds_read_b64_tr_b16 v[220:221], v238 offset:34816\n"
"ds_read_b64_tr_b16 v[222:223], v238 offset:35328\n"
"buffer_load_dwordx4 v231, s[40:43], s60 offen offset:2048 lds\n"
"v_mfma_f32_32x32x16_bf16 v[176:191], v[208:211], v[156:159], v[176:191]\n"
"s_waitcnt vmcnt(9)\n"
"ds_read_b64_tr_b16 v[224:225], v238 offset:35840\n"
"ds_read_b64_tr_b16 v[226:227], v238 offset:36352\n"
"s_waitcnt lgkmcnt(4)\n"
"v_mfma_f32_32x32x16_bf16 v[176:191], v[212:215], v[160:163], v[176:191]\n"
"s_barrier\n"
"ds_read_b64_tr_b16 v[208:209], v242\n"
"ds_read_b64_tr_b16 v[210:211], v242 offset:3072\n"
"v_mfma_f32_32x32x16_bf16 v[176:191], v[216:219], v[164:167], v[176:191]\n"
"ds_read_b64_tr_b16 v[212:213], v242 offset:6144\n"
"ds_read_b64_tr_b16 v[214:215], v242 offset:9216\n"
"s_waitcnt lgkmcnt(4)\n"
"v_mfma_f32_32x32x16_bf16 v[176:191], v[220:223], v[168:171], v[176:191]\n"
"ds_read_b64_tr_b16 v[216:217], v243 offset:64\n"
"ds_read_b64_tr_b16 v[218:219], v243 offset:3136\n"
"s_add_u32 m0, s55, 86016\n"
"v_mfma_f32_32x32x16_bf16 v[176:191], v[224:227], v[172:175], v[176:191]\n"
"ds_read_b64_tr_b16 v[220:221], v243 offset:6208\n"
"ds_read_b64_tr_b16 v[222:223], v243 offset:9280\n"
"s_waitcnt lgkmcnt(4)\n"
"v_mfma_f32_32x32x16_bf16 a[0:15], v[208:211], v[192:195], a[0:15]\n"
"ds_read_b64_tr_b16 v[224:225], v242 offset:128\n"
"ds_read_b64_tr_b16 v[226:227], v242 offset:3200\n"
"s_add_u32 s60, s19, 1769472\n"
"v_mfma_f32_32x32x16_bf16 a[0:15], v[212:215], v[196:199], a[0:15]\n"
"ds_read_b64_tr_b16 v[208:209], v242 offset:6272\n"
"ds_read_b64_tr_b16 v[210:211], v242 offset:9344\n"
"s_waitcnt lgkmcnt(4)\n"
"v_mfma_f32_32x32x16_bf16 a[16:31], v[216:219], v[192:195], a[16:31]\n"
"ds_read_b64_tr_b16 v[212:213], v243 offset:192\n"
"ds_read_b64_tr_b16 v[214:215], v243 offset:3264\n"
"buffer_load_dwordx4 v229, s[40:43], s60 offen lds\n"
"v_mfma_f32_32x32x16_bf16 a[16:31], v[220:223], v[196:199], a[16:31]\n"
"ds_read_b64_tr_b16 v[216:217], v243 offset:6336\n"
"ds_read_b64_tr_b16 v[218:219], v243 offset:9408\n"
"v_max_f32 v176, 0, v176\n"
"v_max_f32 v177, 0, v177\n"
"v_max_f32 v178, 0, v178\n"
"s_waitcnt lgkmcnt(4)\n"
"v_mfma_f32_32x32x16_bf16 a[32:47], v[224:227], v[192:195], a[32:47]\n"
"ds_read_b64_tr_b16 v[220:221], v242 offset:256\n"
"ds_read_b64_tr_b16 v[222:223], v242 offset:3328\n"
"buffer_load_dwordx4 v230, s[40:43], s60 offen offset:1024 lds\n"
"v_max_f32 v179, 0, v179\n"
"v_mul_f32 v176, v176, v176\n"
"v_mfma_f32_32x32x16_bf16 a[32:47], v[208:211], v[196:199], a[32:47]\n"
"ds_read_b64_tr_b16 v[224:225], v242 offset:6400\n"
"ds_read_b64_tr_b16 v[226:227], v242 offset:9472\n"
"v_mul_f32 v177, v177, v177\n"
"v_max_f32 v180, 0, v180\n"
"v_max_f32 v181, 0, v181\n"
"s_waitcnt lgkmcnt(4)\n"
"v_mfma_f32_32x32x16_bf16 a[48:63], v[212:215], v[192:195], a[48:63]\n"
"ds_read_b64_tr_b16 v[208:209], v243 offset:320\n"
"ds_read_b64_tr_b16 v[210:211], v243 offset:3392\n"
"buffer_load_dwordx4 v231, s[40:43], s60 offen offset:2048 lds\n"
"v_mul_f32 v178, v178, v178\n"
"s_waitcnt vmcnt(9)\n"
"v_mul_f32_e64 v179, v179, v179\n"
"v_mfma_f32_32x32x16_bf16 a[48:63], v[216:219], v[196:199], a[48:63]\n"
"ds_read_b64_tr_b16 v[212:213], v243 offset:6464\n"
"ds_read_b64_tr_b16 v[214:215], v243 offset:9536\n"
"v_cvt_pk_bf16_f32 v200, v176, v177\n"
"v_max_f32 v182, 0, v182\n"
"s_waitcnt lgkmcnt(4)\n"
"v_mfma_f32_32x32x16_bf16 a[64:79], v[220:223], v[192:195], a[64:79]\n"
"s_barrier\n"
"ds_read_b64_tr_b16 v[216:217], v242 offset:12288\n"
"ds_read_b64_tr_b16 v[218:219], v242 offset:15360\n"
"v_max_f32 v183, 0, v183\n"
"v_mul_f32_e64 v180, v180, v180\n"
"v_mfma_f32_32x32x16_bf16 a[64:79], v[224:227], v[196:199], a[64:79]\n"
"ds_read_b64_tr_b16 v[220:221], v242 offset:18432\n"
"ds_read_b64_tr_b16 v[222:223], v242 offset:21504\n"
"v_mul_f32_e64 v181, v181, v181\n"
"v_cvt_pk_bf16_f32 v201, v178, v179\n"
"v_max_f32 v184, 0, v184\n"
"s_waitcnt lgkmcnt(4)\n"
"v_mfma_f32_32x32x16_bf16 a[80:95], v[208:211], v[192:195], a[80:95]\n"
"ds_read_b64_tr_b16 v[224:225], v243 offset:12352\n"
"ds_read_b64_tr_b16 v[226:227], v243 offset:15424\n"
"s_add_u32 m0, s55, 98304\n"
"v_max_f32 v185, 0, v185\n"
"v_mul_f32 v182, v182, v182\n"
"v_mfma_f32_32x32x16_bf16 a[80:95], v[212:215], v[196:199], a[80:95]\n"
"ds_read_b64_tr_b16 v[208:209], v243 offset:18496\n"
"ds_read_b64_tr_b16 v[210:211], v243 offset:21568\n"
"v_mul_f32_e64 v183, v183, v183\n"
"v_cvt_pk_bf16_f32 v202, v180, v181\n"
"v_max_f32 v186, 0, v186\n"
"s_waitcnt lgkmcnt(4)\n"
"v_mfma_f32_32x32x16_bf16 a[96:111], v[216:219], v[192:195], a[96:111]\n"
"ds_read_b64_tr_b16 v[212:213], v242 offset:12416\n"
"ds_read_b64_tr_b16 v[214:215], v242 offset:15488\n"
"s_add_u32 s60, s19, 3538944\n"
"v_max_f32 v187, 0, v187\n"
"v_mul_f32 v184, v184, v184\n"
"v_mfma_f32_32x32x16_bf16 a[96:111], v[220:223], v[196:199], a[96:111]\n"
"ds_read_b64_tr_b16 v[216:217], v242 offset:18560\n"
"ds_read_b64_tr_b16 v[218:219], v242 offset:21632\n"
"v_mul_f32_e64 v185, v185, v185\n"
"v_cvt_pk_bf16_f32 v203, v182, v183\n"
"v_max_f32 v188, 0, v188\n"
"s_waitcnt lgkmcnt(4)\n"
"v_mfma_f32_32x32x16_bf16 a[112:127], v[224:227], v[192:195], a[112:127]\n"
"ds_read_b64_tr_b16 v[220:221], v243 offset:12480\n"
"ds_read_b64_tr_b16 v[222:223], v243 offset:15552\n"
"buffer_load_dwordx4 v229, s[40:43], s60 offen lds\n"
"v_max_f32 v189, 0, v189\n"
"v_mul_f32 v186, v186, v186\n"
"v_mfma_f32_32x32x16_bf16 a[112:127], v[208:211], v[196:199], a[112:127]\n"
"ds_read_b64_tr_b16 v[224:225], v243 offset:18624\n"
"ds_read_b64_tr_b16 v[226:227], v243 offset:21696\n"
"v_mul_f32_e64 v187, v187, v187\n"
"v_cvt_pk_bf16_f32 v204, v184, v185\n"
"v_max_f32 v190, 0, v190\n"
"s_waitcnt lgkmcnt(4)\n"
"v_mfma_f32_32x32x16_bf16 a[128:143], v[212:215], v[192:195], a[128:143]\n"
"ds_read_b64_tr_b16 v[208:209], v242 offset:12544\n"
"ds_read_b64_tr_b16 v[210:211], v242 offset:15616\n"
"buffer_load_dwordx4 v230, s[40:43], s60 offen offset:1024 lds\n"
"v_max_f32 v191, 0, v191\n"
"v_mul_f32 v188, v188, v188\n"
"v_mfma_f32_32x32x16_bf16 a[128:143], v[216:219], v[196:199], a[128:143]\n"
"ds_read_b64_tr_b16 v[212:213], v242 offset:18688\n"
"ds_read_b64_tr_b16 v[214:215], v242 offset:21760\n"
"v_mul_f32_e64 v189, v189, v189\n"
"v_cvt_pk_bf16_f32 v205, v186, v187\n"
"v_mul_f32 v190, v190, v190\n"
"s_waitcnt lgkmcnt(4)\n"
"v_mfma_f32_32x32x16_bf16 a[144:159], v[220:223], v[192:195], a[144:159]\n"
"ds_read_b64_tr_b16 v[216:217], v243 offset:12608\n"
"ds_read_b64_tr_b16 v[218:219], v243 offset:15680\n"
"buffer_load_dwordx4 v231, s[40:43], s60 offen offset:2048 lds\n"
"v_mul_f32_e64 v191, v191, v191\n"
"v_cvt_pk_bf16_f32 v206, v188, v189\n"
"v_mfma_f32_32x32x16_bf16 a[144:159], v[224:227], v[196:199], a[144:159]\n"
"s_waitcnt vmcnt(9)\n"
"ds_read_b64_tr_b16 v[220:221], v243 offset:18752\n"
"ds_read_b64_tr_b16 v[222:223], v243 offset:21824\n"
"v_cvt_pk_bf16_f32 v207, v190, v191\n"
"ds_read_b128 v[176:179], v248\n"
"s_waitcnt lgkmcnt(5)\n"
"v_mfma_f32_32x32x16_bf16 a[160:175], v[208:211], v[192:195], a[160:175]\n"
"s_barrier\n"
"ds_read_b64_tr_b16 v[224:225], v242 offset:24576\n"
"ds_read_b64_tr_b16 v[226:227], v242 offset:27648\n"
"ds_read_b128 v[180:183], v248 offset:32\n"
"ds_read_b128 v[184:187], v248 offset:64\n"
"v_mfma_f32_32x32x16_bf16 a[160:175], v[212:215], v[196:199], a[160:175]\n"
"ds_read_b64_tr_b16 v[208:209], v242 offset:30720\n"
"ds_read_b64_tr_b16 v[210:211], v242 offset:33792\n"
"ds_read_b128 v[188:191], v248 offset:96\n"
"s_add_u32 s18, s18, 1\n"
"s_cmp_eq_u32 s18, 9\n"
"s_mov_b32 s21, 128\n"
"s_cselect_b32 s21, -1024, s21\n"
"s_cselect_b32 s18, 0, s18\n"
"v_add_u32_e64 v248, s21, v248\n"
"s_waitcnt lgkmcnt(7)\n"
"v_mul_f32 v176, v249, v176\n"
"v_mfma_f32_32x32x16_bf16 a[176:191], v[216:219], v[192:195], a[176:191]\n"
"ds_read_b64_tr_b16 v[212:213], v243 offset:24640\n"
"ds_read_b64_tr_b16 v[214:215], v243 offset:27712\n"
"s_add_u32 m0, s55, 110592\n"
"v_mul_f32 v177, v249, v177\n"
"v_mul_f32 v178, v249, v178\n"
"v_mfma_f32_32x32x16_bf16 a[176:191], v[220:223], v[196:199], a[176:191]\n"
"ds_read_b64_tr_b16 v[216:217], v243 offset:30784\n"
"ds_read_b64_tr_b16 v[218:219], v243 offset:33856\n"
"v_mul_f32_e64 v179, v249, v179\n"
"s_waitcnt lgkmcnt(8)\n"
"v_mul_f32 v180, v249, v180\n"
"v_mul_f32 v181, v249, v181\n"
"s_waitcnt lgkmcnt(5)\n"
"v_mfma_f32_32x32x16_bf16 a[192:207], v[224:227], v[192:195], a[192:207]\n"
"ds_read_b64_tr_b16 v[220:221], v242 offset:24704\n"
"ds_read_b64_tr_b16 v[222:223], v242 offset:27776\n"
"s_add_u32 s17, s17, 1\n"
"s_cmp_eq_u32 s17, 9\n"
"s_cselect_b32 s17, 0, s17\n"
"s_mul_i32 s20, s17, 36864\n"
"s_add_u32 s60, s20, 0\n"
"v_mul_f32 v182, v249, v182\n"
"v_mul_f32 v183, v249, v183\n"
"v_mfma_f32_32x32x16_bf16 a[192:207], v[208:211], v[196:199], a[192:207]\n"
"ds_read_b64_tr_b16 v[224:225], v242 offset:30848\n"
"ds_read_b64_tr_b16 v[226:227], v242 offset:33920\n"
"v_mul_f32 v184, v249, v184\n"
"v_mul_f32 v185, v249, v185\n"
"v_mul_f32 v186, v249, v186\n"
"s_waitcnt lgkmcnt(4)\n"
"v_mfma_f32_32x32x16_bf16 a[208:223], v[212:215], v[192:195], a[208:223]\n"
"ds_read_b64_tr_b16 v[208:209], v243 offset:24768\n"
"ds_read_b64_tr_b16 v[210:211], v243 offset:27840\n"
"buffer_load_dwordx4 v232, s[44:47], s60 offen lds\n"
"v_mul_f32 v187, v249, v187\n"
"v_mul_f32 v188, v249, v188\n"
"v_mfma_f32_32x32x16_bf16 a[208:223], v[216:219], v[196:199], a[208:223]\n"
"ds_read_b64_tr_b16 v[212:213], v243 offset:30912\n"
"ds_read_b64_tr_b16 v[214:215], v243 offset:33984\n"
"v_mul_f32 v189, v249, v189\n"
"v_mul_f32 v190, v249, v190\n"
"v_mul_f32 v191, v249, v191\n"
"s_waitcnt lgkmcnt(4)\n"
"v_mfma_f32_32x32x16_bf16 a[224:239], v[220:223], v[192:195], a[224:239]\n"
"ds_read_b64_tr_b16 v[216:217], v242 offset:24832\n"
"ds_read_b64_tr_b16 v[218:219], v242 offset:27904\n"
"buffer_load_dwordx4 v233, s[44:47], s60 offen offset:1024 lds\n"
"v_mfma_f32_32x32x16_bf16 a[224:239], v[224:227], v[196:199], a[224:239]\n"
"ds_read_b64_tr_b16 v[220:221], v242 offset:30976\n"
"ds_read_b64_tr_b16 v[222:223], v242 offset:34048\n"
"s_waitcnt lgkmcnt(4)\n"
"v_mfma_f32_32x32x16_bf16 a[240:255], v[208:211], v[192:195], a[240:255]\n"
"ds_read_b64_tr_b16 v[224:225], v243 offset:24896\n"
"ds_read_b64_tr_b16 v[226:227], v243 offset:27968\n"
"buffer_load_dwordx4 v234, s[44:47], s60 offen offset:2048 lds\n"
"v_mfma_f32_32x32x16_bf16 a[240:255], v[212:215], v[196:199], a[240:255]\n"
"s_waitcnt vmcnt(9)\n"
"ds_read_b64_tr_b16 v[208:209], v243 offset:31040\n"
"ds_read_b64_tr_b16 v[210:211], v243 offset:34112\n"
"s_waitcnt lgkmcnt(4)\n"
"v_mfma_f32_32x32x16_bf16 v[0:15], v[216:219], v[192:195], v[0:15]\n"
"s_barrier\n"
"ds_read_b64_tr_b16 v[212:213], v238\n"
"ds_read_b64_tr_b16 v[214:215], v238 offset:512\n"
"v_mfma_f32_32x32x16_bf16 v[0:15], v[220:223], v[196:199], v[0:15]\n"
"ds_read_b64_tr_b16 v[216:217], v238 offset:1024\n"
"ds_read_b64_tr_b16 v[218:219], v238 offset:1536\n"
"s_waitcnt lgkmcnt(4)\n"
"v_mfma_f32_32x32x16_bf16 v[16:31], v[224:227], v[192:195], v[16:31]\n"
"ds_read_b64_tr_b16 v[220:221], v238 offset:2048\n"
"ds_read_b64_tr_b16 v[222:223], v238 offset:2560\n"
"s_add_u32 m0, s55, 122880\n"
"v_mfma_f32_32x32x16_bf16 v[16:31], v[208:211], v[196:199], v[16:31]\n"
"ds_read_b64_tr_b16 v[224:225], v238 offset:3072\n"
"ds_read_b64_tr_b16 v[226:227], v238 offset:3584\n"
"s_waitcnt lgkmcnt(4)\n"
"v_mfma_f32_32x32x16_bf16 v[176:191], v[212:215], v[32:35], v[176:191]\n"
"ds_read_b64_tr_b16 v[208:209], v238 offset:4096\n"
"ds_read_b64_tr_b16 v[210:211], v238 offset:4608\n"
"s_add_u32 s60, s20, 384\n"
"v_mfma_f32_32x32x16_bf16 v[176:191], v[216:219], v[36:39], v[176:191]\n"
"ds_read_b64_tr_b16 v[212:213], v238 offset:5120\n"
"ds_read_b64_tr_b16 v[214:215], v238 offset:5632\n"
"s_and_b32 s56, s53, 1\n"
"s_lshl_b32 s71, s54, 1\n"
"s_waitcnt lgkmcnt(4)\n"
"v_mfma_f32_32x32x16_bf16 v[176:191], v[220:223], v[40:43], v[176:191]\n"
"ds_read_b64_tr_b16 v[216:217], v238 offset:6144\n"
"ds_read_b64_tr_b16 v[218:219], v238 offset:6656\n"
"buffer_load_dwordx4 v232, s[44:47], s60 offen lds\n"
"v_mfma_f32_32x32x16_bf16 v[176:191], v[224:227], v[44:47], v[176:191]\n"
"ds_read_b64_tr_b16 v[220:221], v238 offset:7168\n"
"ds_read_b64_tr_b16 v[222:223], v238 offset:7680\n"
"s_add_u32 s71, s71, s56\n"
"s_lshr_b32 s72, s53, 1\n"
"s_waitcnt lgkmcnt(4)\n"
"v_mfma_f32_32x32x16_bf16 v[176:191], v[208:211], v[48:51], v[176:191]\n"
"ds_read_b64_tr_b16 v[224:225], v238 offset:8192\n"
"ds_read_b64_tr_b16 v[226:227], v238 offset:8704\n"
"buffer_load_dwordx4 v233, s[44:47], s60 offen offset:1024 lds\n"
"v_mfma_f32_32x32x16_bf16 v[176:191], v[212:215], v[52:55], v[176:191]\n"
"ds_read_b64_tr_b16 v[208:209], v238 offset:9216\n"
"ds_read_b64_tr_b16 v[210:211], v238 offset:9728\n"
"s_mov_b32 s56, 0\n"
"s_waitcnt lgkmcnt(4)\n"
"v_mfma_f32_32x32x16_bf16 v[176:191], v[216:219], v[56:59], v[176:191]\n"
"ds_read_b64_tr_b16 v[212:213], v238 offset:10240\n"
"ds_read_b64_tr_b16 v[214:215], v238 offset:10752\n"
"buffer_load_dwordx4 v234, s[44:47], s60 offen offset:2048 lds\n"
"v_mfma_f32_32x32x16_bf16 v[176:191], v[220:223], v[60:63], v[176:191]\n"
"s_waitcnt vmcnt(9)\n"
"ds_read_b64_tr_b16 v[216:217], v238 offset:11264\n"
"ds_read_b64_tr_b16 v[218:219], v238 offset:11776\n"
"s_waitcnt lgkmcnt(4)\n"
"v_mfma_f32_32x32x16_bf16 v[176:191], v[224:227], v[64:67], v[176:191]\n"
"s_barrier\n"
"ds_read_b64_tr_b16 v[220:221], v238 offset:12288\n"
"ds_read_b64_tr_b16 v[222:223], v238 offset:12800\n"
"v_mfma_f32_32x32x16_bf16 v[176:191], v[208:211], v[68:71], v[176:191]\n"
"ds_read_b64_tr_b16 v[224:225], v238 offset:13312\n"
"ds_read_b64_tr_b16 v[226:227], v238 offset:13824\n"
"s_cmp_ge_u32 s71, 7\n"
"s_addc_u32 s56, s56, 0\n"
"s_waitcnt lgkmcnt(4)\n"
"v_mfma_f32_32x32x16_bf16 v[176:191], v[212:215], v[72:75], v[176:191]\n"
"ds_read_b64_tr_b16 v[208:209], v238 offset:14336\n"
"ds_read_b64_tr_b16 v[210:211], v238 offset:14848\n"
"s_add_u32 m0, s55, 135168\n"
"v_mfma_f32_32x32x16_bf16 v[176:191], v[216:219], v[76:79], v[176:191]\n"
"ds_read_b64_tr_b16 v[212:213], v238 offset:15360\n"
"ds_read_b64_tr_b16 v[214:215], v238 offset:15872\n"
"s_cmp_ge_u32 s71, 13\n"
"s_addc_u32 s56, s56, 0\n"
"s_waitcnt lgkmcnt(4)\n"
"v_mfma_f32_32x32x16_bf16 v[176:191], v[220:223], v[80:83], v[176:191]\n"
"ds_read_b64_tr_b16 v[216:217], v238 offset:16384\n"
"ds_read_b64_tr_b16 v[218:219], v238 offset:16896\n"
"s_add_u32 s60, s20, 768\n"
"v_mfma_f32_32x32x16_bf16 v[176:191], v[224:227], v[84:87], v[176:191]\n"
"ds_read_b64_tr_b16 v[220:221], v238 offset:17408\n"
"ds_read_b64_tr_b16 v[222:223], v238 offset:17920\n"
"s_cmp_ge_u32 s71, 20\n"
"s_addc_u32 s56, s56, 0\n"
"s_waitcnt lgkmcnt(4)\n"
"v_mfma_f32_32x32x16_bf16 v[176:191], v[208:211], v[88:91], v[176:191]\n"
"ds_read_b64_tr_b16 v[224:225], v238 offset:18432\n"
"ds_read_b64_tr_b16 v[226:227], v238 offset:18944\n"
"buffer_load_dwordx4 v232, s[44:47], s60 offen lds\n"
"v_mfma_f32_32x32x16_bf16 v[176:191], v[212:215], v[92:95], v[176:191]\n"
"ds_read_b64_tr_b16 v[208:209], v238 offset:19456\n"
"ds_read_b64_tr_b16 v[210:211], v238 offset:19968\n"
"s_cmp_ge_u32 s71, 26\n"
"s_addc_u32 s56, s56, 0\n"
"s_waitcnt lgkmcnt(4)\n"
"v_mfma_f32_32x32x16_bf16 v[176:191], v[216:219], v[96:99], v[176:191]\n"
"ds_read_b64_tr_b16 v[212:213], v238 offset:20480\n"
"ds_read_b64_tr_b16 v[214:215], v238 offset:20992\n"
"buffer_load_dwordx4 v233, s[44:47], s60 offen offset:1024 lds\n"
"v_mfma_f32_32x32x16_bf16 v[176:191], v[220:223], v[100:103], v[176:191]\n"
"ds_read_b64_tr_b16 v[216:217], v238 offset:21504\n"
"ds_read_b64_tr_b16 v[218:219], v238 offset:22016\n"
"s_lshl_b32 s56, s56, 4\n"
"s_lshl_b32 s57, s72, 1\n"
"s_waitcnt lgkmcnt(4)\n"
"v_mfma_f32_32x32x16_bf16 v[176:191], v[224:227], v[104:107], v[176:191]\n"
"ds_read_b64_tr_b16 v[220:221], v238 offset:22528\n"
"ds_read_b64_tr_b16 v[222:223], v238 offset:23040\n"
"buffer_load_dwordx4 v234, s[44:47], s60 offen offset:2048 lds\n"
"v_mfma_f32_32x32x16_bf16 v[176:191], v[208:211], v[108:111], v[176:191]\n"
"s_waitcnt vmcnt(9)\n"
"ds_read_b64_tr_b16 v[224:225], v238 offset:23552\n"
"ds_read_b64_tr_b16 v[226:227], v238 offset:24064\n"
"s_waitcnt lgkmcnt(4)\n"
"v_mfma_f32_32x32x16_bf16 v[176:191], v[212:215], v[112:115], v[176:191]\n"
"s_barrier\n"
"ds_read_b64_tr_b16 v[208:209], v238 offset:24576\n"
"ds_read_b64_tr_b16 v[210:211], v238 offset:25088\n"
"v_mfma_f32_32x32x16_bf16 v[176:191], v[216:219], v[116:119], v[176:191]\n"
"ds_read_b64_tr_b16 v[212:213], v238 offset:25600\n"
"ds_read_b64_tr_b16 v[214:215], v238 offset:26112\n"
"s_add_u32 s57, s57, s56\n"
"v_and_b32 v244, 1, v228\n"
"s_waitcnt lgkmcnt(4)\n"
"v_mfma_f32_32x32x16_bf16 v[176:191], v[220:223], v[120:123], v[176:191]\n"
"ds_read_b64_tr_b16 v[216:217], v238 offset:26624\n"
"ds_read_b64_tr_b16 v[218:219], v238 offset:27136\n"
"s_add_u32 m0, s55, 73728\n"
"v_mfma_f32_32x32x16_bf16 v[176:191], v[224:227], v[124:127], v[176:191]\n"
"ds_read_b64_tr_b16 v[220:221], v238 offset:27648\n"
"ds_read_b64_tr_b16 v[222:223], v238 offset:28160\n"
"v_add_u32_e64 v244, s57, v244\n"
"v_and_b32 v245, 15, v228\n"
"s_waitcnt lgkmcnt(4)\n"
"v_mfma_f32_32x32x16_bf16 v[176:191], v[208:211], v[128:131], v[176:191]\n"
"ds_read_b64_tr_b16 v[224:225], v238 offset:28672\n"
"ds_read_b64_tr_b16 v[226:227], v238 offset:29184\n"
"s_add_u32 s16, s16, 1\n"
"s_cmp_eq_u32 s16, 9\n"
"s_cselect_b32 s16, 0, s16\n"
"s_mul_i32 s19, s16, 64\n"
"s_add_u32 s60, s19, 0\n"
"v_mfma_f32_32x32x16_bf16 v[176:191], v[212:215], v[132:135], v[176:191]\n"
"ds_read_b64_tr_b16 v[208:209], v238 offset:29696\n"
"ds_read_b64_tr_b16 v[210:211], v238 offset:30208\n"
"v_add_u32 v245, s56, v245\n"
"v_lshrrev_b32 v253, 4, v228\n"
"s_waitcnt lgkmcnt(4)\n"
"v_mfma_f32_32x32x16_bf16 v[176:191], v[216:219], v[136:139], v[176:191]\n"
"ds_read_b64_tr_b16 v[212:213], v238 offset:30720\n"
"ds_read_b64_tr_b16 v[214:215], v238 offset:31232\n"
"buffer_load_dwordx4 v229, s[40:43], s60 offen lds\n"
"v_mfma_f32_32x32x16_bf16 v[176:191], v[220:223], v[140:143], v[176:191]\n"
"ds_read_b64_tr_b16 v[216:217], v238 offset:31744\n"
"ds_read_b64_tr_b16 v[218:219], v238 offset:32256\n"
"v_mul_u32_u24 v253, 80, v253\n"
"v_add_u32 v245, v245, v253\n"
"s_waitcnt lgkmcnt(4)\n"
"v_mfma_f32_32x32x16_bf16 v[176:191], v[224:227], v[144:147], v[176:191]\n"
"ds_read_b64_tr_b16 v[220:221], v238 offset:32768\n"
"ds_read_b64_tr_b16 v[222:223], v238 offset:33280\n"
"buffer_load_dwordx4 v230, s[40:43], s60 offen offset:1024 lds\n"
"v_mfma_f32_32x32x16_bf16 v[176:191], v[208:211], v[148:151], v[176:191]\n"
"ds_read_b64_tr_b16 v[224:225], v238 offset:33792\n"
"ds_read_b64_tr_b16 v[226:227], v238 offset:34304\n"
"v_add_u32 v245, 80, v245\n"
"v_cmp_gt_u32 vcc, 32, v228\n"
"s_waitcnt lgkmcnt(4)\n"
"v_mfma_f32_32x32x16_bf16 v[176:191], v[212:215], v[152:155], v[176:191]\n"
"ds_read_b64_tr_b16 v[208:209], v238 offset:34816\n"
"ds_read_b64_tr_b16 v[210:211], v238 offset:35328\n"
"buffer_load_dwordx4 v231, s[40:43], s60 offen offset:2048 lds\n"
"v_mfma_f32_32x32x16_bf16 v[176:191], v[216:219], v[156:159], v[176:191]\n"
"s_waitcnt vmcnt(9)\n"
"ds_read_b64_tr_b16 v[212:213], v238 offset:35840\n"
"ds_read_b64_tr_b16 v[214:215], v238 offset:36352\n"
"s_waitcnt lgkmcnt(4)\n"
"v_mfma_f32_32x32x16_bf16 v[176:191], v[220:223], v[160:163], v[176:191]\n"
"s_barrier\n"
"ds_read_b64_tr_b16 v[216:217], v242\n"
"ds_read_b64_tr_b16 v[218:219], v242 offset:3072\n"
"v_mfma_f32_32x32x16_bf16 v[176:191], v[224:227], v[164:167], v[176:191]\n"
"ds_read_b64_tr_b16 v[220:221], v242 offset:6144\n"
"ds_read_b64_tr_b16 v[222:223], v242 offset:9216\n"
"s_nop 1\n"
"v_cndmask_b32 v244, v244, v245\n"
"s_waitcnt lgkmcnt(4)\n"
"v_mfma_f32_32x32x16_bf16 v[176:191], v[208:211], v[168:171], v[176:191]\n"
"ds_read_b64_tr_b16 v[224:225], v243 offset:64\n"
"ds_read_b64_tr_b16 v[226:227], v243 offset:3136\n"
"s_add_u32 m0, s55, 86016\n"
"v_mfma_f32_32x32x16_bf16 v[176:191], v[212:215], v[172:175], v[176:191]\n"
"ds_read_b64_tr_b16 v[208:209], v243 offset:6208\n"
"ds_read_b64_tr_b16 v[210:211], v243 offset:9280\n"
"v_lshlrev_b32_e64 v244, 3, v244\n"
"v_add_u32 v253, 1024, v244\n"
"buffer_load_dwordx2 v[250:251], v253, s[48:51], 0 offen sc1\n"
"s_nop 1\n"
"s_waitcnt lgkmcnt(4)\n"
"v_mfma_f32_32x32x16_bf16 a[0:15], v[216:219], v[200:203], a[0:15]\n"
"ds_read_b64_tr_b16 v[212:213], v242 offset:128\n"
"ds_read_b64_tr_b16 v[214:215], v242 offset:3200\n"
"s_add_u32 s60, s19, 1769472\n"
"v_mfma_f32_32x32x16_bf16 a[0:15], v[220:223], v[204:207], a[0:15]\n"
"ds_read_b64_tr_b16 v[216:217], v242 offset:6272\n"
"ds_read_b64_tr_b16 v[218:219], v242 offset:9344\n"
"s_waitcnt lgkmcnt(4)\n"
"v_mfma_f32_32x32x16_bf16 a[16:31], v[224:227], v[200:203], a[16:31]\n"
"ds_read_b64_tr_b16 v[220:221], v243 offset:192\n"
"ds_read_b64_tr_b16 v[222:223], v243 offset:3264\n"
"buffer_load_dwordx4 v229, s[40:43], s60 offen lds\n"
"v_mfma_f32_32x32x16_bf16 a[16:31], v[208:211], v[204:207], a[16:31]\n"
"ds_read_b64_tr_b16 v[224:225], v243 offset:6336\n"
"ds_read_b64_tr_b16 v[226:227], v243 offset:9408\n"
"v_max_f32 v176, 0, v176\n"
"v_max_f32_e64 v177, 0, v177\n"
"v_max_f32 v178, 0, v178\n"
"s_waitcnt lgkmcnt(4)\n"
"v_mfma_f32_32x32x16_bf16 a[32:47], v[212:215], v[200:203], a[32:47]\n"
"ds_read_b64_tr_b16 v[208:209], v242 offset:256\n"
"ds_read_b64_tr_b16 v[210:211], v242 offset:3328\n"
"buffer_load_dwordx4 v230, s[40:43], s60 offen offset:1024 lds\n"
"v_max_f32 v179, 0, v179\n"
"v_mul_f32 v176, v176, v176\n"
"v_mfma_f32_32x32x16_bf16 a[32:47], v[216:219], v[204:207], a[32:47]\n"
"ds_read_b64_tr_b16 v[212:213], v242 offset:6400\n"
"ds_read_b64_tr_b16 v[214:215], v242 offset:9472\n"
"v_mul_f32 v177, v177, v177\n"
"v_max_f32 v180, 0, v180\n"
"v_max_f32 v181, 0, v181\n"
"s_waitcnt lgkmcnt(4)\n"
"v_mfma_f32_32x32x16_bf16 a[48:63], v[220:223], v[200:203], a[48:63]\n"
"ds_read_b64_tr_b16 v[216:217], v243 offset:320\n"
"ds_read_b64_tr_b16 v[218:219], v243 offset:3392\n"
"buffer_load_dwordx4 v231, s[40:43], s60 offen offset:2048 lds\n"
"v_mul_f32 v178, v178, v178\n"
"s_waitcnt vmcnt(10)\n"
"v_mul_f32_e64 v179, v179, v179\n"
"v_mfma_f32_32x32x16_bf16 a[48:63], v[224:227], v[204:207], a[48:63]\n"
"ds_read_b64_tr_b16 v[220:221], v243 offset:6464\n"
"ds_read_b64_tr_b16 v[222:223], v243 offset:9536\n"
"v_cvt_pk_bf16_f32 v192, v176, v177\n"
"v_max_f32 v182, 0, v182\n"
"s_waitcnt lgkmcnt(4)\n"
"v_mfma_f32_32x32x16_bf16 a[64:79], v[208:211], v[200:203], a[64:79]\n"
"s_barrier\n"
"ds_read_b64_tr_b16 v[224:225], v242 offset:12288\n"
"ds_read_b64_tr_b16 v[226:227], v242 offset:15360\n"
"v_max_f32 v183, 0, v183\n"
"v_mul_f32_e64 v180, v180, v180\n"
"v_mfma_f32_32x32x16_bf16 a[64:79], v[212:215], v[204:207], a[64:79]\n"
"ds_read_b64_tr_b16 v[208:209], v242 offset:18432\n"
"ds_read_b64_tr_b16 v[210:211], v242 offset:21504\n"
"v_mul_f32_e64 v181, v181, v181\n"
"v_cvt_pk_bf16_f32 v193, v178, v179\n"
"v_max_f32 v184, 0, v184\n"
"s_waitcnt lgkmcnt(4)\n"
"v_mfma_f32_32x32x16_bf16 a[80:95], v[216:219], v[200:203], a[80:95]\n"
"ds_read_b64_tr_b16 v[212:213], v243 offset:12352\n"
"ds_read_b64_tr_b16 v[214:215], v243 offset:15424\n"
"s_add_u32 m0, s55, 98304\n"
"v_max_f32 v185, 0, v185\n"
"v_mul_f32 v182, v182, v182\n"
"v_mfma_f32_32x32x16_bf16 a[80:95], v[220:223], v[204:207], a[80:95]\n"
"ds_read_b64_tr_b16 v[216:217], v243 offset:18496\n"
"ds_read_b64_tr_b16 v[218:219], v243 offset:21568\n"
"v_mul_f32_e64 v183, v183, v183\n"
"v_cvt_pk_bf16_f32 v194, v180, v181\n"
"v_max_f32 v186, 0, v186\n"
"s_waitcnt lgkmcnt(4)\n"
"v_mfma_f32_32x32x16_bf16 a[96:111], v[224:227], v[200:203], a[96:111]\n"
"ds_read_b64_tr_b16 v[220:221], v242 offset:12416\n"
"ds_read_b64_tr_b16 v[222:223], v242 offset:15488\n"
"s_add_u32 s60, s19, 3538944\n"
"v_max_f32 v187, 0, v187\n"
"v_mul_f32 v184, v184, v184\n"
"v_mfma_f32_32x32x16_bf16 a[96:111], v[208:211], v[204:207], a[96:111]\n"
"ds_read_b64_tr_b16 v[224:225], v242 offset:18560\n"
"ds_read_b64_tr_b16 v[226:227], v242 offset:21632\n"
"v_mul_f32_e64 v185, v185, v185\n"
"v_cvt_pk_bf16_f32 v195, v182, v183\n"
"v_max_f32 v188, 0, v188\n"
"s_waitcnt lgkmcnt(4)\n"
"v_mfma_f32_32x32x16_bf16 a[112:127], v[212:215], v[200:203], a[112:127]\n"
"ds_read_b64_tr_b16 v[208:209], v243 offset:12480\n"
"ds_read_b64_tr_b16 v[210:211], v243 offset:15552\n"
"buffer_load_dwordx4 v229, s[40:43], s60 offen lds\n"
"v_max_f32 v189, 0, v189\n"
"v_mul_f32 v186, v186, v186\n"
"v_mfma_f32_32x32x16_bf16 a[112:127], v[216:219], v[204:207], a[112:127]\n"
"ds_read_b64_tr_b16 v[212:213], v243 offset:18624\n"
"ds_read_b64_tr_b16 v[214:215], v243 offset:21696\n"
"v_mul_f32_e64 v187, v187, v187\n"
"v_cvt_pk_bf16_f32 v196, v184, v185\n"
"v_max_f32 v190, 0, v190\n"
"s_waitcnt lgkmcnt(4)\n"
"v_mfma_f32_32x32x16_bf16 a[128:143], v[220:223], v[200:203], a[128:143]\n"
"ds_read_b64_tr_b16 v[216:217], v242 offset:12544\n"
"ds_read_b64_tr_b16 v[218:219], v242 offset:15616\n"
"buffer_load_dwordx4 v230, s[40:43], s60 offen offset:1024 lds\n"
"v_max_f32 v191, 0, v191\n"
"v_mul_f32 v188, v188, v188\n"
"v_mfma_f32_32x32x16_bf16 a[128:143], v[224:227], v[204:207], a[128:143]\n"
"ds_read_b64_tr_b16 v[220:221], v242 offset:18688\n"
"ds_read_b64_tr_b16 v[222:223], v242 offset:21760\n"
"v_mul_f32_e64 v189, v189, v189\n"
"v_cvt_pk_bf16_f32 v197, v186, v187\n"
"v_mul_f32 v190, v190, v190\n"
"s_waitcnt lgkmcnt(4)\n"
"v_mfma_f32_32x32x16_bf16 a[144:159], v[208:211], v[200:203], a[144:159]\n"
"ds_read_b64_tr_b16 v[224:225], v243 offset:12608\n"
"ds_read_b64_tr_b16 v[226:227], v243 offset:15680\n"
"buffer_load_dwordx4 v231, s[40:43], s60 offen offset:2048 lds\n"
"v_mul_f32_e64 v191, v191, v191\n"
"v_cvt_pk_bf16_f32 v198, v188, v189\n"
"v_mfma_f32_32x32x16_bf16 a[144:159], v[212:215], v[204:207], a[144:159]\n"
"s_waitcnt vmcnt(10)\n"
"ds_read_b64_tr_b16 v[208:209], v243 offset:18752\n"
"ds_read_b64_tr_b16 v[210:211], v243 offset:21824\n"
"v_cvt_pk_bf16_f32 v199, v190, v191\n"
"ds_read_b128 v[176:179], v248\n"
"s_waitcnt lgkmcnt(5)\n"
"v_mfma_f32_32x32x16_bf16 a[160:175], v[216:219], v[200:203], a[160:175]\n"
"s_barrier\n"
"ds_read_b64_tr_b16 v[212:213], v242 offset:24576\n"
"ds_read_b64_tr_b16 v[214:215], v242 offset:27648\n"
"ds_read_b128 v[180:183], v248 offset:32\n"
"ds_read_b128 v[184:187], v248 offset:64\n"
"v_mfma_f32_32x32x16_bf16 a[160:175], v[220:223], v[204:207], a[160:175]\n"
"ds_read_b64_tr_b16 v[216:217], v242 offset:30720\n"
"ds_read_b64_tr_b16 v[218:219], v242 offset:33792\n"
"ds_read_b128 v[188:191], v248 offset:96\n"
"s_add_u32 s18, s18, 1\n"
"s_cmp_eq_u32 s18, 9\n"
"s_mov_b32 s21, 128\n"
"s_cselect_b32 s21, -1024, s21\n"
"s_cselect_b32 s18, 0, s18\n"
"v_add_u32_e64 v248, s21, v248\n"
"s_waitcnt lgkmcnt(7)\n"
"v_mul_f32 v176, v249, v176\n"
"v_mfma_f32_32x32x16_bf16 a[176:191], v[224:227], v[200:203], a[176:191]\n"
"ds_read_b64_tr_b16 v[220:221], v243 offset:24640\n"
"ds_read_b64_tr_b16 v[222:223], v243 offset:27712\n"
"s_add_u32 m0, s55, 110592\n"
"v_mul_f32 v177, v249, v177\n"
"v_mul_f32 v178, v249, v178\n"
"v_mfma_f32_32x32x16_bf16 a[176:191], v[208:211], v[204:207], a[176:191]\n"
"ds_read_b64_tr_b16 v[224:225], v243 offset:30784\n"
"ds_read_b64_tr_b16 v[226:227], v243 offset:33856\n"
"v_mul_f32_e64 v179, v249, v179\n"
"s_waitcnt lgkmcnt(8)\n"
"v_mul_f32 v180, v249, v180\n"
"v_mul_f32 v181, v249, v181\n"
"s_waitcnt lgkmcnt(5)\n"
"v_mfma_f32_32x32x16_bf16 a[192:207], v[212:215], v[200:203], a[192:207]\n"
"ds_read_b64_tr_b16 v[208:209], v242 offset:24704\n"
"ds_read_b64_tr_b16 v[210:211], v242 offset:27776\n"
"s_add_u32 s17, s17, 1\n"
"s_cmp_eq_u32 s17, 9\n"
"s_cselect_b32 s17, 0, s17\n"
"s_mul_i32 s20, s17, 36864\n"
"s_add_u32 s60, s20, 0\n"
"v_mul_f32 v182, v249, v182\n"
"v_mul_f32 v183, v249, v183\n"
"v_mfma_f32_32x32x16_bf16 a[192:207], v[216:219], v[204:207], a[192:207]\n"
"ds_read_b64_tr_b16 v[212:213], v242 offset:30848\n"
"ds_read_b64_tr_b16 v[214:215], v242 offset:33920\n"
"v_mul_f32 v184, v249, v184\n"
"v_mul_f32 v185, v249, v185\n"
"v_mul_f32 v186, v249, v186\n"
"s_waitcnt lgkmcnt(4)\n"
"v_mfma_f32_32x32x16_bf16 a[208:223], v[220:223], v[200:203], a[208:223]\n"
"ds_read_b64_tr_b16 v[216:217], v243 offset:24768\n"
"ds_read_b64_tr_b16 v[218:219], v243 offset:27840\n"
"buffer_load_dwordx4 v232, s[44:47], s60 offen lds\n"
"v_mul_f32 v187, v249, v187\n"
"v_mul_f32 v188, v249, v188\n"
"v_mfma_f32_32x32x16_bf16 a[208:223], v[224:227], v[204:207], a[208:223]\n"
"ds_read_b64_tr_b16 v[220:221], v243 offset:30912\n"
"ds_read_b64_tr_b16 v[222:223], v243 offset:33984\n"
"v_mul_f32 v189, v249, v189\n"
"v_mul_f32 v190, v249, v190\n"
"v_mul_f32 v191, v249, v191\n"
"s_waitcnt lgkmcnt(4)\n"
"v_mfma_f32_32x32x16_bf16 a[224:239], v[208:211], v[200:203], a[224:239]\n"
"ds_read_b64_tr_b16 v[224:225], v242 offset:24832\n"
"ds_read_b64_tr_b16 v[226:227], v242 offset:27904\n"
"buffer_load_dwordx4 v233, s[44:47], s60 offen offset:1024 lds\n"
"v_mfma_f32_32x32x16_bf16 a[224:239], v[212:215], v[204:207], a[224:239]\n"
"ds_read_b64_tr_b16 v[208:209], v242 offset:30976\n"
"ds_read_b64_tr_b16 v[210:211], v242 offset:34048\n"
"s_waitcnt lgkmcnt(4)\n"
"v_mfma_f32_32x32x16_bf16 a[240:255], v[216:219], v[200:203], a[240:255]\n"
"ds_read_b64_tr_b16 v[212:213], v243 offset:24896\n"
"ds_read_b64_tr_b16 v[214:215], v243 offset:27968\n"
"buffer_load_dwordx4 v234, s[44:47], s60 offen offset:2048 lds\n"
"v_mfma_f32_32x32x16_bf16 a[240:255], v[220:223], v[204:207], a[240:255]\n"
"s_waitcnt vmcnt(10)\n"
"ds_read_b64_tr_b16 v[216:217], v243 offset:31040\n"
"ds_read_b64_tr_b16 v[218:219], v243 offset:34112\n"
"s_waitcnt lgkmcnt(4)\n"
"v_mfma_f32_32x32x16_bf16 v[0:15], v[224:227], v[200:203], v[0:15]\n"
"s_barrier\n"
"ds_read_b64_tr_b16 v[220:221], v238\n"
"ds_read_b64_tr_b16 v[222:223], v238 offset:512\n"
"v_mfma_f32_32x32x16_bf16 v[0:15], v[208:211], v[204:207], v[0:15]\n"
"ds_read_b64_tr_b16 v[224:225], v238 offset:1024\n"
"ds_read_b64_tr_b16 v[226:227], v238 offset:1536\n"
"s_waitcnt lgkmcnt(4)\n"
"v_mfma_f32_32x32x16_bf16 v[16:31], v[212:215], v[200:203], v[16:31]\n"
"ds_read_b64_tr_b16 v[208:209], v238 offset:2048\n"
"ds_read_b64_tr_b16 v[210:211], v238 offset:2560\n"
"s_add_u32 m0, s55, 122880\n"
"v_mfma_f32_32x32x16_bf16 v[16:31], v[216:219], v[204:207], v[16:31]\n"
"ds_read_b64_tr_b16 v[212:213], v238 offset:3072\n"
"ds_read_b64_tr_b16 v[214:215], v238 offset:3584\n"
"s_waitcnt lgkmcnt(4)\n"
"v_mfma_f32_32x32x16_bf16 v[176:191], v[220:223], v[32:35], v[176:191]\n"
"ds_read_b64_tr_b16 v[216:217], v238 offset:4096\n"
"ds_read_b64_tr_b16 v[218:219], v238 offset:4608\n"
"s_add_u32 s60, s20, 384\n"
"v_mfma_f32_32x32x16_bf16 v[176:191], v[224:227], v[36:39], v[176:191]\n"
"ds_read_b64_tr_b16 v[220:221], v238 offset:5120\n"
"ds_read_b64_tr_b16 v[222:223], v238 offset:5632\n"
"s_waitcnt lgkmcnt(4)\n"
"v_mfma_f32_32x32x16_bf16 v[176:191], v[208:211], v[40:43], v[176:191]\n"
"ds_read_b64_tr_b16 v[224:225], v238 offset:6144\n"
"ds_read_b64_tr_b16 v[226:227], v238 offset:6656\n"
"buffer_load_dwordx4 v232, s[44:47], s60 offen lds\n"
"v_mfma_f32_32x32x16_bf16 v[176:191], v[212:215], v[44:47], v[176:191]\n"
"ds_read_b64_tr_b16 v[208:209], v238 offset:7168\n"
"ds_read_b64_tr_b16 v[210:211], v238 offset:7680\n"
"s_waitcnt lgkmcnt(4)\n"
"v_mfma_f32_32x32x16_bf16 v[176:191], v[216:219], v[48:51], v[176:191]\n"
"ds_read_b64_tr_b16 v[212:213], v238 offset:8192\n"
"ds_read_b64_tr_b16 v[214:215], v238 offset:8704\n"
"buffer_load_dwordx4 v233, s[44:47], s60 offen offset:1024 lds\n"
"v_mfma_f32_32x32x16_bf16 v[176:191], v[220:223], v[52:55], v[176:191]\n"
"ds_read_b64_tr_b16 v[216:217], v238 offset:9216\n"
"ds_read_b64_tr_b16 v[218:219], v238 offset:9728\n"
"s_waitcnt lgkmcnt(4)\n"
"v_mfma_f32_32x32x16_bf16 v[176:191], v[224:227], v[56:59], v[176:191]\n"
"ds_read_b64_tr_b16 v[220:221], v238 offset:10240\n"
"ds_read_b64_tr_b16 v[222:223], v238 offset:10752\n"
"buffer_load_dwordx4 v234, s[44:47], s60 offen offset:2048 lds\n"
"v_mfma_f32_32x32x16_bf16 v[176:191], v[208:211], v[60:63], v[176:191]\n"
"s_waitcnt vmcnt(9)\n"
"ds_read_b64_tr_b16 v[224:225], v238 offset:11264\n"
"ds_read_b64_tr_b16 v[226:227], v238 offset:11776\n"
"s_waitcnt lgkmcnt(4)\n"
"v_mfma_f32_32x32x16_bf16 v[176:191], v[212:215], v[64:67], v[176:191]\n"
"s_barrier\n"
"v_cmp_eq_u64 vcc, s[62:63], v[250:251]\n"
"s_nop 3\n"
"s_cmp_eq_u64 vcc, exec\n"
"s_cbranch_scc1 LPOLL_OK_%=\n"
"s_mov_b32 s56, 0x4000\n"
"LPOLL_%=:\n"
"s_sleep 8\n"
"buffer_load_dwordx2 v[250:251], v253, s[48:51], 0 offen sc1\n"
"s_waitcnt vmcnt(0)\n"
"v_cmp_eq_u64 vcc, s[62:63], v[250:251]\n"
"s_nop 3\n"
"s_cmp_eq_u64 vcc, exec\n"
"s_cbranch_scc1 LPOLL_OK_%=\n"
"s_sub_u32 s56, s56, 1\n"
"s_cmp_lg_u32 s56, 0\n"
"s_cbranch_scc1 LPOLL_%=\n"
"s_or_b32 s75, s75, 2\n"
"LPOLL_OK_%=:\n"
"ds_read_b64_tr_b16 v[208:209], v238 offset:12288\n"
"ds_read_b64_tr_b16 v[210:211], v238 offset:12800\n"
"v_mfma_f32_32x32x16_bf16 v[176:191], v[216:219], v[68:71], v[176:191]\n"
"ds_read_b64_tr_b16 v[212:213], v238 offset:13312\n"
"ds_read_b64_tr_b16 v[214:215], v238 offset:13824\n"
"s_waitcnt lgkmcnt(4)\n"
"v_mfma_f32_32x32x16_bf16 v[176:191], v[220:223], v[72:75], v[176:191]\n"
"ds_read_b64_tr_b16 v[216:217], v238 offset:14336\n"
"ds_read_b64_tr_b16 v[218:219], v238 offset:14848\n"
"s_add_u32 m0, s55, 135168\n"
"v_mfma_f32_32x32x16_bf16 v[176:191], v[224:227], v[76:79], v[176:191]\n"
"ds_read_b64_tr_b16 v[220:221], v238 offset:15360\n"
"ds_read_b64_tr_b16 v[222:223], v238 offset:15872\n"
"s_waitcnt lgkmcnt(4)\n"
"v_mfma_f32_32x32x16_bf16 v[176:191], v[208:211], v[80:83], v[176:191]\n"
"ds_read_b64_tr_b16 v[224:225], v238 offset:16384\n"
"ds_read_b64_tr_b16 v[226:227], v238 offset:16896\n"
"s_add_u32 s60, s20, 768\n"
"v_mfma_f32_32x32x16_bf16 v[176:191], v[212:215], v[84:87], v[176:191]\n"
"ds_read_b64_tr_b16 v[208:209], v238 offset:17408\n"
"ds_read_b64_tr_b16 v[210:211], v238 offset:17920\n"
"s_waitcnt lgkmcnt(4)\n"
"v_mfma_f32_32x32x16_bf16 v[176:191], v[216:219], v[88:91], v[176:191]\n"
"ds_read_b64_tr_b16 v[212:213], v238 offset:18432\n"
"ds_read_b64_tr_b16 v[214:215], v238 offset:18944\n"
"buffer_load_dwordx4 v232, s[44:47], s60 offen lds\n"
"v_mfma_f32_32x32x16_bf16 v[176:191], v[220:223], v[92:95], v[176:191]\n"
"ds_read_b64_tr_b16 v[216:217], v238 offset:19456\n"
"ds_read_b64_tr_b16 v[218:219], v238 offset:19968\n"
"s_waitcnt lgkmcnt(4)\n"
"v_mfma_f32_32x32x16_bf16 v[176:191], v[224:227], v[96:99], v[176:191]\n"
"ds_read_b64_tr_b16 v[220:221], v238 offset:20480\n"
"ds_read_b64_tr_b16 v[222:223], v238 offset:20992\n"
"buffer_load_dwordx4 v233, s[44:47], s60 offen offset:1024 lds\n"
"v_mfma_f32_32x32x16_bf16 v[176:191], v[208:211], v[100:103], v[176:191]\n"
"ds_read_b64_tr_b16 v[224:225], v238 offset:21504\n"
"ds_read_b64_tr_b16 v[226:227], v238 offset:22016\n"
"s_waitcnt lgkmcnt(4)\n"
"v_mfma_f32_32x32x16_bf16 v[176:191], v[212:215], v[104:107], v[176:191]\n"
"ds_read_b64_tr_b16 v[208:209], v238 offset:22528\n"
"ds_read_b64_tr_b16 v[210:211], v238 offset:23040\n"
"buffer_load_dwordx4 v234, s[44:47], s60 offen offset:2048 lds\n"
"v_mfma_f32_32x32x16_bf16 v[176:191], v[216:219], v[108:111], v[176:191]\n"
"s_waitcnt vmcnt(9)\n"
"ds_read_b64_tr_b16 v[212:213], v238 offset:23552\n"
"ds_read_b64_tr_b16 v[214:215], v238 offset:24064\n"
"s_waitcnt lgkmcnt(4)\n"
"v_mfma_f32_32x32x16_bf16 v[176:191], v[220:223], v[112:115], v[176:191]\n"
"s_barrier\n"
"ds_read_b64_tr_b16 v[216:217], v238 offset:24576\n"
"ds_read_b64_tr_b16 v[218:219], v238 offset:25088\n"
"v_mfma_f32_32x32x16_bf16 v[176:191], v[224:227], v[116:119], v[176:191]\n"
"ds_read_b64_tr_b16 v[220:221], v238 offset:25600\n"
"ds_read_b64_tr_b16 v[222:223], v238 offset:26112\n"
"s_waitcnt lgkmcnt(4)\n"
"v_mfma_f32_32x32x16_bf16 v[176:191], v[208:211], v[120:123], v[176:191]\n"
"ds_read_b64_tr_b16 v[224:225], v238 offset:26624\n"
"ds_read_b64_tr_b16 v[226:227], v238 offset:27136\n"
"s_add_u32 m0, s55, 73728\n"
"v_mfma_f32_32x32x16_bf16 v[176:191], v[212:215], v[124:127], v[176:191]\n"
"ds_read_b64_tr_b16 v[208:209], v238 offset:27648\n"
"ds_read_b64_tr_b16 v[210:211], v238 offset:28160\n"
"s_waitcnt lgkmcnt(4)\n"
"v_mfma_f32_32x32x16_bf16 v[176:191], v[216:219], v[128:131], v[176:191]\n"
"ds_read_b64_tr_b16 v[212:213], v238 offset:28672\n"
"ds_read_b64_tr_b16 v[214:215], v238 offset:29184\n"
"s_add_u32 s16, s16, 1\n"
"s_cmp_eq_u32 s16, 9\n"
"s_cselect_b32 s16, 0, s16\n"
"s_mul_i32 s19, s16, 64\n"
"s_add_u32 s60, s19, 0\n"
"v_mfma_f32_32x32x16_bf16 v[176:191], v[220:223], v[132:135], v[176:191]\n"
"ds_read_b64_tr_b16 v[216:217], v238 offset:29696\n"
"ds_read_b64_tr_b16 v[218:219], v238 offset:30208\n"
"s_waitcnt lgkmcnt(4)\n"
"v_mfma_f32_32x32x16_bf16 v[176:191], v[224:227], v[136:139], v[176:191]\n"
"ds_read_b64_tr_b16 v[220:221], v238 offset:30720\n"
"ds_read_b64_tr_b16 v[222:223], v238 offset:31232\n"
"buffer_load_dwordx4 v229, s[40:43], s60 offen lds\n"
"v_mfma_f32_32x32x16_bf16 v[176:191], v[208:211], v[140:143], v[176:191]\n"
"ds_read_b64_tr_b16 v[224:225], v238 offset:31744\n"
"ds_read_b64_tr_b16 v[226:227], v238 offset:32256\n"
"s_waitcnt lgkmcnt(4)\n"
"v_mfma_f32_32x32x16_bf16 v[176:191], v[212:215], v[144:147], v[176:191]\n"
"ds_read_b64_tr_b16 v[208:209], v238 offset:32768\n"
"ds_read_b64_tr_b16 v[210:211], v238 offset:33280\n"
"buffer_load_dwordx4 v230, s[40:43], s60 offen offset:1024 lds\n"
"v_mfma_f32_32x32x16_bf16 v[176:191], v[216:219], v[148:151], v[176:191]\n"
"ds_read_b64_tr_b16 v[212:213], v238 offset:33792\n"
"ds_read_b64_tr_b16 v[214:215], v238 offset:34304\n"
"s_waitcnt lgkmcnt(4)\n"
"v_mfma_f32_32x32x16_bf16 v[176:191], v[220:223], v[152:155], v[176:191]\n"
"ds_read_b64_tr_b16 v[216:217], v238 offset:34816\n"
"ds_read_b64_tr_b16 v[218:219], v238 offset:35328\n"
"buffer_load_dwordx4 v231, s[40:43], s60 offen offset:2048 lds\n"
"v_mfma_f32_32x32x16_bf16 v[176:191], v[224:227], v[156:159], v[176:191]\n"
"s_waitcnt vmcnt(9)\n"
"ds_read_b64_tr_b16 v[220:221], v238 offset:35840\n"
"ds_read_b64_tr_b16 v[222:223], v238 offset:36352\n"
"s_waitcnt lgkmcnt(4)\n"
"v_mfma_f32_32x32x16_bf16 v[176:191], v[208:211], v[160:163], v[176:191]\n"
"s_barrier\n"
"ds_read_b64_tr_b16 v[224:225], v242\n"
"ds_read_b64_tr_b16 v[226:227], v242 offset:3072\n"
"v_mfma_f32_32x32x16_bf16 v[176:191], v[212:215], v[164:167], v[176:191]\n"
"ds_read_b64_tr_b16 v[208:209], v242 offset:6144\n"
"ds_read_b64_tr_b16 v[210:211], v242 offset:9216\n"
"s_waitcnt lgkmcnt(4)\n"
"v_mfma_f32_32x32x16_bf16 v[176:191], v[216:219], v[168:171], v[176:191]\n"
"ds_read_b64_tr_b16 v[212:213], v243 offset:64\n"
"ds_read_b64_tr_b16 v[214:215], v243 offset:3136\n"
"s_add_u32 m0, s55, 86016\n"
"v_mfma_f32_32x32x16_bf16 v[176:191], v[220:223], v[172:175], v[176:191]\n"
"ds_read_b64_tr_b16 v[216:217], v243 offset:6208\n"
"ds_read_b64_tr_b16 v[218:219], v243 offset:9280\n"
"s_waitcnt lgkmcnt(4)\n"
"v_mfma_f32_32x32x16_bf16 a[0:15], v[224:227], v[192:195], a[0:15]\n"
"ds_read_b64_tr_b16 v[220:221], v242 offset:128\n"
"ds_read_b64_tr_b16 v[222:223], v242 offset:3200\n"
"s_add_u32 s60, s19, 1769472\n"
"v_mfma_f32_32x32x16_bf16 a[0:15], v[208:211], v[196:199], a[0:15]\n"
"ds_read_b64_tr_b16 v[224:225], v242 offset:6272\n"
"ds_read_b64_tr_b16 v[226:227], v242 offset:9344\n"
"s_waitcnt lgkmcnt(4)\n"
"v_mfma_f32_32x32x16_bf16 a[16:31], v[212:215], v[192:195], a[16:31]\n"
"ds_read_b64_tr_b16 v[208:209], v243 offset:192\n"
"ds_read_b64_tr_b16 v[210:211], v243 offset:3264\n"
"buffer_load_dwordx4 v229, s[40:43], s60 offen lds\n"
"v_mfma_f32_32x32x16_bf16 a[16:31], v[216:219], v[196:199], a[16:31]\n"
"ds_read_b64_tr_b16 v[212:213], v243 offset:6336\n"
"ds_read_b64_tr_b16 v[214:215], v243 offset:9408\n"
"v_max_f32 v176, 0, v176\n"
"v_max_f32 v177, 0, v177\n"
"v_max_f32 v178, 0, v178\n"
"s_waitcnt lgkmcnt(4)\n"
"v_mfma_f32_32x32x16_bf16 a[32:47], v[220:223], v[192:195], a[32:47]\n"
"ds_read_b64_tr_b16 v[216:217], v242 offset:256\n"
"ds_read_b64_tr_b16 v[218:219], v242 offset:3328\n"
"buffer_load_dwordx4 v230, s[40:43], s60 offen offset:1024 lds\n"
"v_max_f32 v179, 0, v179\n"
"v_mul_f32 v176, v176, v176\n"
"v_mfma_f32_32x32x16_bf16 a[32:47], v[224:227], v[196:199], a[32:47]\n"
"ds_read_b64_tr_b16 v[220:221], v242 offset:6400\n"
"ds_read_b64_tr_b16 v[222:223], v242 offset:9472\n"
"v_mul_f32 v177, v177, v177\n"
"v_max_f32 v180, 0, v180\n"
"v_max_f32 v181, 0, v181\n"
"s_waitcnt lgkmcnt(4)\n"
"v_mfma_f32_32x32x16_bf16 a[48:63], v[208:211], v[192:195], a[48:63]\n"
"ds_read_b64_tr_b16 v[224:225], v243 offset:320\n"
"ds_read_b64_tr_b16 v[226:227], v243 offset:3392\n"
"buffer_load_dwordx4 v231, s[40:43], s60 offen offset:2048 lds\n"
"v_mul_f32 v178, v178, v178\n"
"s_waitcnt vmcnt(9)\n"
"v_mul_f32_e64 v179, v179, v179\n"
"v_mfma_f32_32x32x16_bf16 a[48:63], v[212:215], v[196:199], a[48:63]\n"
"ds_read_b64_tr_b16 v[208:209], v243 offset:6464\n"
"ds_read_b64_tr_b16 v[210:211], v243 offset:9536\n"
"v_cvt_pk_bf16_f32 v200, v176, v177\n"
"v_max_f32 v182, 0, v182\n"
"s_waitcnt lgkmcnt(4)\n"
"v_mfma_f32_32x32x16_bf16 a[64:79], v[216:219], v[192:195], a[64:79]\n"
"s_barrier\n"
"ds_read_b64_tr_b16 v[212:213], v242 offset:12288\n"
"ds_read_b64_tr_b16 v[214:215], v242 offset:15360\n"
"v_max_f32 v183, 0, v183\n"
"v_mul_f32_e64 v180, v180, v180\n"
"v_mfma_f32_32x32x16_bf16 a[64:79], v[220:223], v[196:199], a[64:79]\n"
"ds_read_b64_tr_b16 v[216:217], v242 offset:18432\n"
"ds_read_b64_tr_b16 v[218:219], v242 offset:21504\n"
"v_mul_f32_e64 v181, v181, v181\n"
"v_cvt_pk_bf16_f32 v201, v178, v179\n"
"v_max_f32 v184, 0, v184\n"
"s_waitcnt lgkmcnt(4)\n"
"v_mfma_f32_32x32x16_bf16 a[80:95], v[224:227], v[192:195], a[80:95]\n"
"ds_read_b64_tr_b16 v[220:221], v243 offset:12352\n"
"ds_read_b64_tr_b16 v[222:223], v243 offset:15424\n"
"s_add_u32 m0, s55, 98304\n"
"v_max_f32 v185, 0, v185\n"
"v_mul_f32 v182, v182, v182\n"
"v_mfma_f32_32x32x16_bf16 a[80:95], v[208:211], v[196:199], a[80:95]\n"
"ds_read_b64_tr_b16 v[224:225], v243 offset:18496\n"
"ds_read_b64_tr_b16 v[226:227], v243 offset:21568\n"
"v_mul_f32_e64 v183, v183, v183\n"
"v_cvt_pk_bf16_f32 v202, v180, v181\n"
"v_max_f32 v186, 0, v186\n"
"s_waitcnt lgkmcnt(4)\n"
"v_mfma_f32_32x32x16_bf16 a[96:111], v[212:215], v[192:195], a[96:111]\n"
"ds_read_b64_tr_b16 v[208:209], v242 offset:12416\n"
"ds_read_b64_tr_b16 v[210:211], v242 offset:15488\n"
"s_add_u32 s60, s19, 3538944\n"
"v_max_f32 v187, 0, v187\n"
"v_mul_f32 v184, v184, v184\n"
"v_mfma_f32_32x32x16_bf16 a[96:111], v[216:219], v[196:199], a[96:111]\n"
"ds_read_b64_tr_b16 v[212:213], v242 offset:18560\n"
"ds_read_b64_tr_b16 v[214:215], v242 offset:21632\n"
"v_mul_f32_e64 v185, v185, v185\n"
"v_cvt_pk_bf16_f32 v203, v182, v183\n"
"v_max_f32 v188, 0, v188\n"
"s_waitcnt lgkmcnt(4)\n"
"v_mfma_f32_32x32x16_bf16 a[112:127], v[220:223], v[192:195], a[112:127]\n"
"ds_read_b64_tr_b16 v[216:217], v243 offset:12480\n"
"ds_read_b64_tr_b16 v[218:219], v243 offset:15552\n"
"buffer_load_dwordx4 v229, s[40:43], s60 offen lds\n"
"v_max_f32 v189, 0, v189\n"
"v_mul_f32 v186, v186, v186\n"
"v_mfma_f32_32x32x16_bf16 a[112:127], v[224:227], v[196:199], a[112:127]\n"
"ds_read_b64_tr_b16 v[220:221], v243 offset:18624\n"
"ds_read_b64_tr_b16 v[222:223], v243 offset:21696\n"
"v_mul_f32_e64 v187, v187, v187\n"
"v_cvt_pk_bf16_f32 v204, v184, v185\n"
"v_max_f32 v190, 0, v190\n"
"s_waitcnt lgkmcnt(4)\n"
"v_mfma_f32_32x32x16_bf16 a[128:143], v[208:211], v[192:195], a[128:143]\n"
"ds_read_b64_tr_b16 v[224:225], v242 offset:12544\n"
"ds_read_b64_tr_b16 v[226:227], v242 offset:15616\n"
"buffer_load_dwordx4 v230, s[40:43], s60 offen offset:1024 lds\n"
"v_max_f32 v191, 0, v191\n"
"v_mul_f32 v188, v188, v188\n"
"v_mfma_f32_32x32x16_bf16 a[128:143], v[212:215], v[196:199], a[128:143]\n"
"ds_read_b64_tr_b16 v[208:209], v242 offset:18688\n"
"ds_read_b64_tr_b16 v[210:211], v242 offset:21760\n"
"v_mul_f32_e64 v189, v189, v189\n"
"v_cvt_pk_bf16_f32 v205, v186, v187\n"
"v_mul_f32 v190, v190, v190\n"
"s_waitcnt lgkmcnt(4)\n"
"v_mfma_f32_32x32x16_bf16 a[144:159], v[216:219], v[192:195], a[144:159]\n"
"ds_read_b64_tr_b16 v[212:213], v243 offset:12608\n"
"ds_read_b64_tr_b16 v[214:215], v243 offset:15680\n"
"buffer_load_dwordx4 v231, s[40:43], s60 offen offset:2048 lds\n"
"v_mul_f32_e64 v191, v191, v191\n"
"v_cvt_pk_bf16_f32 v206, v188, v189\n"
"v_mfma_f32_32x32x16_bf16 a[144:159], v[220:223], v[196:199], a[144:159]\n"
"s_waitcnt vmcnt(9)\n"
"ds_read_b64_tr_b16 v[216:217], v243 offset:18752\n"
"ds_read_b64_tr_b16 v[218:219], v243 offset:21824\n"
"v_cvt_pk_bf16_f32 v207, v190, v191\n"
"ds_read_b128 v[176:179], v248\n"
"s_waitcnt lgkmcnt(5)\n"
"v_mfma_f32_32x32x16_bf16 a[160:175], v[224:227], v[192:195], a[160:175]\n"
"s_barrier\n"
"ds_read_b64_tr_b16 v[220:221], v242 offset:24576\n"
"ds_read_b64_tr_b16 v[222:223], v242 offset:27648\n"
"ds_read_b128 v[180:183], v248 offset:32\n"
"ds_read_b128 v[184:187], v248 offset:64\n"
"v_mfma_f32_32x32x16_bf16 a[160:175], v[208:211], v[196:199], a[160:175]\n"
"ds_read_b64_tr_b16 v[224:225], v242 offset:30720\n"
"ds_read_b64_tr_b16 v[226:227], v242 offset:33792\n"
"ds_read_b128 v[188:191], v248 offset:96\n"
"s_add_u32 s18, s18, 1\n"
"s_cmp_eq_u32 s18, 9\n"
"s_mov_b32 s21, 128\n"
"s_cselect_b32 s21, -1024, s21\n"
"s_cselect_b32 s18, 0, s18\n"
"v_add_u32_e64 v248, s21, v248\n"
"s_waitcnt lgkmcnt(7)\n"
"v_mul_f32 v176, v249, v176\n"
"v_mfma_f32_32x32x16_bf16 a[176:191], v[212:215], v[192:195], a[176:191]\n"
"ds_read_b64_tr_b16 v[208:209], v243 offset:24640\n"
"ds_read_b64_tr_b16 v[210:211], v243 offset:27712\n"
"s_add_u32 m0, s55, 110592\n"
"v_mul_f32 v177, v249, v177\n"
"v_mul_f32 v178, v249, v178\n"
"v_mfma_f32_32x32x16_bf16 a[176:191], v[216:219], v[196:199], a[176:191]\n"
"ds_read_b64_tr_b16 v[212:213], v243 offset:30784\n"
"ds_read_b64_tr_b16 v[214:215], v243 offset:33856\n"
"v_mul_f32_e64 v179, v249, v179\n"
"s_waitcnt lgkmcnt(8)\n"
"v_mul_f32 v180, v249, v180\n"
"v_mul_f32 v181, v249, v181\n"
"s_waitcnt lgkmcnt(5)\n"
"v_mfma_f32_32x32x16_bf16 a[192:207], v[220:223], v[192:195], a[192:207]\n"
"ds_read_b64_tr_b16 v[216:217], v242 offset:24704\n"
"ds_read_b64_tr_b16 v[218:219], v242 offset:27776\n"
"s_add_u32 s17, s17, 1\n"
"s_cmp_eq_u32 s17, 9\n"
"s_cselect_b32 s17, 0, s17\n"
"s_mul_i32 s20, s17, 36864\n"
"s_add_u32 s60, s20, 0\n"
"v_mul_f32 v182, v249, v182\n"
"v_mul_f32 v183, v249, v183\n"
"v_mfma_f32_32x32x16_bf16 a[192:207], v[224:227], v[196:199], a[192:207]\n"
"ds_read_b64_tr_b16 v[220:221], v242 offset:30848\n"
"ds_read_b64_tr_b16 v[222:223], v242 offset:33920\n"
"v_mul_f32 v184, v249, v184\n"
"v_mul_f32 v185, v249, v185\n"
"v_mul_f32 v186, v249, v186\n"
"s_waitcnt lgkmcnt(4)\n"
"v_mfma_f32_32x32x16_bf16 a[208:223], v[208:211], v[192:195], a[208:223]\n"
"ds_read_b64_tr_b16 v[224:225], v243 offset:24768\n"
"ds_read_b64_tr_b16 v[226:227], v243 offset:27840\n"
"buffer_load_dwordx4 v232, s[44:47], s60 offen lds\n"
"v_mul_f32 v187, v249, v187\n"
"v_mul_f32 v188, v249, v188\n"
"v_mfma_f32_32x32x16_bf16 a[208:223], v[212:215], v[196:199], a[208:223]\n"
"ds_read_b64_tr_b16 v[208:209], v243 offset:30912\n"
"ds_read_b64_tr_b16 v[210:211], v243 offset:33984\n"
"v_mul_f32 v189, v249, v189\n"
"v_mul_f32 v190, v249, v190\n"
"v_mul_f32 v191, v249, v191\n"
"s_waitcnt lgkmcnt(4)\n"
"v_mfma_f32_32x32x16_bf16 a[224:239], v[216:219], v[192:195], a[224:239]\n"
"ds_read_b64_tr_b16 v[212:213], v242 offset:24832\n"
"ds_read_b64_tr_b16 v[214:215], v242 offset:27904\n"
"buffer_load_dwordx4 v233, s[44:47], s60 offen offset:1024 lds\n"
"v_mfma_f32_32x32x16_bf16 a[224:239], v[220:223], v[196:199], a[224:239]\n"
"ds_read_b64_tr_b16 v[216:217], v242 offset:30976\n"
"ds_read_b64_tr_b16 v[218:219], v242 offset:34048\n"
"s_waitcnt lgkmcnt(4)\n"
"v_mfma_f32_32x32x16_bf16 a[240:255], v[224:227], v[192:195], a[240:255]\n"
"ds_read_b64_tr_b16 v[220:221], v243 offset:24896\n"
"ds_read_b64_tr_b16 v[222:223], v243 offset:27968\n"
"buffer_load_dwordx4 v234, s[44:47], s60 offen offset:2048 lds\n"
"v_mfma_f32_32x32x16_bf16 a[240:255], v[208:211], v[196:199], a[240:255]\n"
"s_waitcnt vmcnt(9)\n"
"ds_read_b64_tr_b16 v[224:225], v243 offset:31040\n"
"ds_read_b64_tr_b16 v[226:227], v243 offset:34112\n"
"s_mul_i32 s58, s52, 18432\n"
"s_add_u32 m0, s58, 0\n"
"s_mul_i32 s59, s71, 147456\n"
"s_add_u32 s59, s59, s58\n"
"s_add_u32 s61, s59, 16384\n"
"s_waitcnt lgkmcnt(4)\n"
"v_mfma_f32_32x32x16_bf16 v[0:15], v[212:215], v[192:195], v[0:15]\n"
"s_barrier\n"
"ds_read_b64_tr_b16 v[208:209], v238\n"
"ds_read_b64_tr_b16 v[210:211], v238 offset:512\n"
"buffer_load_dwordx4 v235, s[48:51], s61 offen sc1 lds\n"
"v_mfma_f32_32x32x16_bf16 v[0:15], v[216:219], v[196:199], v[0:15]\n"
"ds_read_b64_tr_b16 v[212:213], v238 offset:1024\n"
"ds_read_b64_tr_b16 v[214:215], v238 offset:1536\n"
"buffer_load_dwordx4 v235, s[48:51], s61 offen offset:1024 sc1 lds\n"
"s_waitcnt lgkmcnt(4)\n"
"v_mfma_f32_32x32x16_bf16 v[16:31], v[220:223], v[192:195], v[16:31]\n"
"ds_read_b64_tr_b16 v[216:217], v238 offset:2048\n"
"ds_read_b64_tr_b16 v[218:219], v238 offset:2560\n"
"s_add_u32 m0, s55, 122880\n"
"v_mfma_f32_32x32x16_bf16 v[16:31], v[224:227], v[196:199], v[16:31]\n"
"ds_read_b64_tr_b16 v[220:221], v238 offset:3072\n"
"ds_read_b64_tr_b16 v[222:223], v238 offset:3584\n"
"s_waitcnt lgkmcnt(4)\n"
"v_mfma_f32_32x32x16_bf16 v[176:191], v[208:211], v[32:35], v[176:191]\n"
"ds_read_b64_tr_b16 v[224:225], v238 offset:4096\n"
"ds_read_b64_tr_b16 v[226:227], v238 offset:4608\n"
"s_add_u32 s60, s20, 384\n"
"v_mfma_f32_32x32x16_bf16 v[176:191], v[212:215], v[36:39], v[176:191]\n"
"ds_read_b64_tr_b16 v[208:209], v238 offset:5120\n"
"ds_read_b64_tr_b16 v[210:211], v238 offset:5632\n"
"s_waitcnt lgkmcnt(4)\n"
"v_mfma_f32_32x32x16_bf16 v[176:191], v[216:219], v[40:43], v[176:191]\n"
"ds_read_b64_tr_b16 v[212:213], v238 offset:6144\n"
"ds_read_b64_tr_b16 v[214:215], v238 offset:6656\n"
"buffer_load_dwordx4 v232, s[44:47], s60 offen lds\n"
"v_mfma_f32_32x32x16_bf16 v[176:191], v[220:223], v[44:47], v[176:191]\n"
"ds_read_b64_tr_b16 v[216:217], v238 offset:7168\n"
"ds_read_b64_tr_b16 v[218:219], v238 offset:7680\n"
"s_waitcnt lgkmcnt(4)\n"
"v_mfma_f32_32x32x16_bf16 v[176:191], v[224:227], v[48:51], v[176:191]\n"
"ds_read_b64_tr_b16 v[220:221], v238 offset:8192\n"
"ds_read_b64_tr_b16 v[222:223], v238 offset:8704\n"
"buffer_load_dwordx4 v233, s[44:47], s60 offen offset:1024 lds\n"
"v_mfma_f32_32x32x16_bf16 v[176:191], v[208:211], v[52:55], v[176:191]\n"
"ds_read_b64_tr_b16 v[224:225], v238 offset:9216\n"
"ds_read_b64_tr_b16 v[226:227], v238 offset:9728\n"
"s_waitcnt lgkmcnt(4)\n"
"v_mfma_f32_32x32x16_bf16 v[176:191], v[212:215], v[56:59], v[176:191]\n"
"ds_read_b64_tr_b16 v[208:209], v238 offset:10240\n"
"ds_read_b64_tr_b16 v[210:211], v238 offset:10752\n"
"buffer_load_dwordx4 v234, s[44:47], s60 offen offset:2048 lds\n"
"v_mfma_f32_32x32x16_bf16 v[176:191], v[216:219], v[60:63], v[176:191]\n"
"s_waitcnt vmcnt(11)\n"
"ds_read_b64_tr_b16 v[212:213], v238 offset:11264\n"
"ds_read_b64_tr_b16 v[214:215], v238 offset:11776\n"
"s_mul_i32 s58, s52, 18432\n"
"s_add_u32 m0, s58, 2048\n"
"s_mul_i32 s59, s71, 147456\n"
"s_add_u32 s59, s59, s58\n"
"s_add_u32 s61, s59, 18432\n"
"s_waitcnt lgkmcnt(4)\n"
"v_mfma_f32_32x32x16_bf16 v[176:191], v[220:223], v[64:67], v[176:191]\n"
"s_barrier\n"
"ds_read_b64_tr_b16 v[216:217], v238 offset:12288\n"
"ds_read_b64_tr_b16 v[218:219], v238 offset:12800\n"
"buffer_load_dwordx4 v235, s[48:51], s61 offen sc1 lds\n"
"v_mfma_f32_32x32x16_bf16 v[176:191], v[224:227], v[68:71], v[176:191]\n"
"ds_read_b64_tr_b16 v[220:221], v238 offset:13312\n"
"ds_read_b64_tr_b16 v[222:223], v238 offset:13824\n"
"buffer_load_dwordx4 v235, s[48:51], s61 offen offset:1024 sc1 lds\n"
"s_waitcnt lgkmcnt(4)\n"
"v_mfma_f32_32x32x16_bf16 v[176:191], v[208:211], v[72:75], v[176:191]\n"
"ds_read_b64_tr_b16 v[224:225], v238 offset:14336\n"
"ds_read_b64_tr_b16 v[226:227], v238 offset:14848\n"
"s_add_u32 m0, s55, 135168\n"
"v_mfma_f32_32x32x16_bf16 v[176:191], v[212:215], v[76:79], v[176:191]\n"
"ds_read_b64_tr_b16 v[208:209], v238 offset:15360\n"
"ds_read_b64_tr_b16 v[210:211], v238 offset:15872\n"
"s_waitcnt lgkmcnt(4)\n"
"v_mfma_f32_32x32x16_bf16 v[176:191], v[216:219], v[80:83], v[176:191]\n"
"ds_read_b64_tr_b16 v[212:213], v238 offset:16384\n"
"ds_read_b64_tr_b16 v[214:215], v238 offset:16896\n"
"s_add_u32 s60, s20, 768\n"
"v_mfma_f32_32x32x16_bf16 v[176:191], v[220:223], v[84:87], v[176:191]\n"
"ds_read_b64_tr_b16 v[216:217], v238 offset:17408\n"
"ds_read_b64_tr_b16 v[218:219], v238 offset:17920\n"
"s_waitcnt lgkmcnt(4)\n"
"v_mfma_f32_32x32x16_bf16 v[176:191], v[224:227], v[88:91], v[176:191]\n"
"ds_read_b64_tr_b16 v[220:221], v238 offset:18432\n"
"ds_read_b64_tr_b16 v[222:223], v238 offset:18944\n"
"buffer_load_dwordx4 v232, s[44:47], s60 offen lds\n"
"v_mfma_f32_32x32x16_bf16 v[176:191], v[208:211], v[92:95], v[176:191]\n"
"ds_read_b64_tr_b16 v[224:225], v238 offset:19456\n"
"ds_read_b64_tr_b16 v[226:227], v238 offset:19968\n"
"s_waitcnt lgkmcnt(4)\n"
"v_mfma_f32_32x32x16_bf16 v[176:191], v[212:215], v[96:99], v[176:191]\n"
"ds_read_b64_tr_b16 v[208:209], v238 offset:20480\n"
"ds_read_b64_tr_b16 v[210:211], v238 offset:20992\n"
"buffer_load_dwordx4 v233, s[44:47], s60 offen offset:1024 lds\n"
"v_mfma_f32_32x32x16_bf16 v[176:191], v[216:219], v[100:103], v[176:191]\n"
"ds_read_b64_tr_b16 v[212:213], v238 offset:21504\n"
"ds_read_b64_tr_b16 v[214:215], v238 offset:22016\n"
"s_waitcnt lgkmcnt(4)\n"
"v_mfma_f32_32x32x16_bf16 v[176:191], v[220:223], v[104:107], v[176:191]\n"
"ds_read_b64_tr_b16 v[216:217], v238 offset:22528\n"
"ds_read_b64_tr_b16 v[218:219], v238 offset:23040\n"
"buffer_load_dwordx4 v234, s[44:47], s60 offen offset:2048 lds\n"
"v_mfma_f32_32x32x16_bf16 v[176:191], v[224:227], v[108:111], v[176:191]\n"
"s_waitcnt vmcnt(13)\n"
"ds_read_b64_tr_b16 v[220:221], v238 offset:23552\n"
"ds_read_b64_tr_b16 v[222:223], v238 offset:24064\n"
"s_mul_i32 s58, s52, 18432\n"
"s_add_u32 m0, s58, 4096\n"
"s_mul_i32 s59, s71, 147456\n"
"s_add_u32 s59, s59, s58\n"
"s_add_u32 s61, s59, 20480\n"
"s_waitcnt lgkmcnt(4)\n"
"v_mfma_f32_32x32x16_bf16 v[176:191], v[208:211], v[112:115], v[176:191]\n"
"s_barrier\n"
"ds_read_b64_tr_b16 v[224:225], v238 offset:24576\n"
"ds_read_b64_tr_b16 v[226:227], v238 offset:25088\n"
"buffer_load_dwordx4 v235, s[48:51], s61 offen sc1 lds\n"
"v_mfma_f32_32x32x16_bf16 v[176:191], v[212:215], v[116:119], v[176:191]\n"
"ds_read_b64_tr_b16 v[208:209], v238 offset:25600\n"
"ds_read_b64_tr_b16 v[210:211], v238 offset:26112\n"
"buffer_load_dwordx4 v235, s[48:51], s61 offen offset:1024 sc1 lds\n"
"s_waitcnt lgkmcnt(4)\n"
"v_mfma_f32_32x32x16_bf16 v[176:191], v[216:219], v[120:123], v[176:191]\n"
"ds_read_b64_tr_b16 v[212:213], v238 offset:26624\n"
"ds_read_b64_tr_b16 v[214:215], v238 offset:27136\n"
"s_add_u32 m0, s55, 73728\n"
"v_mfma_f32_32x32x16_bf16 v[176:191], v[220:223], v[124:127], v[176:191]\n"
"ds_read_b64_tr_b16 v[216:217], v238 offset:27648\n"
"ds_read_b64_tr_b16 v[218:219], v238 offset:28160\n"
"s_waitcnt lgkmcnt(4)\n"
"v_mfma_f32_32x32x16_bf16 v[176:191], v[224:227], v[128:131], v[176:191]\n"
"ds_read_b64_tr_b16 v[220:221], v238 offset:28672\n"
"ds_read_b64_tr_b16 v[222:223], v238 offset:29184\n"
"s_add_u32 s16, s16, 1\n"
"s_cmp_eq_u32 s16, 9\n"
"s_cselect_b32 s16, 0, s16\n"
"s_mul_i32 s19, s16, 64\n"
"s_add_u32 s60, s19, 0\n"
"v_mfma_f32_32x32x16_bf16 v[176:191], v[208:211], v[132:135], v[176:191]\n"
"ds_read_b64_tr_b16 v[224:225], v238 offset:29696\n"
"ds_read_b64_tr_b16 v[226:227], v238 offset:30208\n"
"s_waitcnt lgkmcnt(4)\n"
"v_mfma_f32_32x32x16_bf16 v[176:191], v[212:215], v[136:139], v[176:191]\n"
"ds_read_b64_tr_b16 v[208:209], v238 offset:30720\n"
"ds_read_b64_tr_b16 v[210:211], v238 offset:31232\n"
"buffer_load_dwordx4 v229, s[40:43], s60 offen lds\n"
"v_mfma_f32_32x32x16_bf16 v[176:191], v[216:219], v[140:143], v[176:191]\n"
"ds_read_b64_tr_b16 v[212:213], v238 offset:31744\n"
"ds_read_b64_tr_b16 v[214:215], v238 offset:32256\n"
"s_waitcnt lgkmcnt(4)\n"
"v_mfma_f32_32x32x16_bf16 v[176:191], v[220:223], v[144:147], v[176:191]\n"
"ds_read_b64_tr_b16 v[216:217], v238 offset:32768\n"
"ds_read_b64_tr_b16 v[218:219], v238 offset:33280\n"
"buffer_load_dwordx4 v230, s[40:43], s60 offen offset:1024 lds\n"
"v_mfma_f32_32x32x16_bf16 v[176:191], v[224:227], v[148:151], v[176:191]\n"
"ds_read_b64_tr_b16 v[220:221], v238 offset:33792\n"
"ds_read_b64_tr_b16 v[222:223], v238 offset:34304\n"
"s_waitcnt lgkmcnt(4)\n"
"v_mfma_f32_32x32x16_bf16 v[176:191], v[208:211], v[152:155], v[176:191]\n"
"ds_read_b64_tr_b16 v[224:225], v238 offset:34816\n"
"ds_read_b64_tr_b16 v[226:227], v238 offset:35328\n"
"buffer_load_dwordx4 v231, s[40:43], s60 offen offset:2048 lds\n"
"v_mfma_f32_32x32x16_bf16 v[176:191], v[212:215], v[156:159], v[176:191]\n"
"s_waitcnt vmcnt(15)\n"
"ds_read_b64_tr_b16 v[208:209], v238 offset:35840\n"
"ds_read_b64_tr_b16 v[210:211], v238 offset:36352\n"
"s_mul_i32 s58, s52, 18432\n"
"s_add_u32 m0, s58, 6144\n"
"s_mul_i32 s59, s71, 147456\n"
"s_add_u32 s59, s59, s58\n"
"s_add_u32 s61, s59, 22528\n"
"s_waitcnt lgkmcnt(4)\n"
"v_mfma_f32_32x32x16_bf16 v[176:191], v[216:219], v[160:163], v[176:191]\n"
"s_barrier\n"
"ds_read_b64_tr_b16 v[212:213], v242\n"
"ds_read_b64_tr_b16 v[214:215], v242 offset:3072\n"
"buffer_load_dwordx4 v235, s[48:51], s61 offen sc1 lds\n"
"v_mfma_f32_32x32x16_bf16 v[176:191], v[220:223], v[164:167], v[176:191]\n"
"ds_read_b64_tr_b16 v[216:217], v242 offset:6144\n"
"ds_read_b64_tr_b16 v[218:219], v242 offset:9216\n"
"buffer_load_dwordx4 v235, s[48:51], s61 offen offset:1024 sc1 lds\n"
"s_waitcnt lgkmcnt(4)\n"
"v_mfma_f32_32x32x16_bf16 v[176:191], v[224:227], v[168:171], v[176:191]\n"
"ds_read_b64_tr_b16 v[220:221], v243 offset:64\n"
"ds_read_b64_tr_b16 v[222:223], v243 offset:3136\n"
"s_add_u32 m0, s55, 86016\n"
"v_mfma_f32_32x32x16_bf16 v[176:191], v[208:211], v[172:175], v[176:191]\n"
"ds_read_b64_tr_b16 v[224:225], v243 offset:6208\n"
"ds_read_b64_tr_b16 v[226:227], v243 offset:9280\n"
"s_waitcnt lgkmcnt(4)\n"
"v_mfma_f32_32x32x16_bf16 a[0:15], v[212:215], v[200:203], a[0:15]\n"
"ds_read_b64_tr_b16 v[208:209], v242 offset:128\n"
"ds_read_b64_tr_b16 v[210:211], v242 offset:3200\n"
"s_add_u32 s60, s19, 1769472\n"
"v_mfma_f32_32x32x16_bf16 a[0:15], v[216:219], v[204:207], a[0:15]\n"
"ds_read_b64_tr_b16 v[212:213], v242 offset:6272\n"
"ds_read_b64_tr_b16 v[214:215], v242 offset:9344\n"
"s_waitcnt lgkmcnt(4)\n"
"v_mfma_f32_32x32x16_bf16 a[16:31], v[220:223], v[200:203], a[16:31]\n"
"ds_read_b64_tr_b16 v[216:217], v243 offset:192\n"
"ds_read_b64_tr_b16 v[218:219], v243 offset:3264\n"
"buffer_load_dwordx4 v229, s[40:43], s60 offen lds\n"
"v_mfma_f32_32x32x16_bf16 a[16:31], v[224:227], v[204:207], a[16:31]\n"
"ds_read_b64_tr_b16 v[220:221], v243 offset:6336\n"
"ds_read_b64_tr_b16 v[222:223], v243 offset:9408\n"
"v_max_f32 v176, 0, v176\n"
"v_max_f32_e64 v177, 0, v177\n"
"v_max_f32 v178, 0, v178\n"
"s_waitcnt lgkmcnt(4)\n"
"v_mfma_f32_32x32x16_bf16 a[32:47], v[208:211], v[200:203], a[32:47]\n"
"ds_read_b64_tr_b16 v[224:225], v242 offset:256\n"
"ds_read_b64_tr_b16 v[226:227], v242 offset:3328\n"
"buffer_load_dwordx4 v230, s[40:43], s60 offen offset:1024 lds\n"
"v_max_f32 v179, 0, v179\n"
"v_mul_f32 v176, v176, v176\n"
"v_mfma_f32_32x32x16_bf16 a[32:47], v[212:215], v[204:207], a[32:47]\n"
"ds_read_b64_tr_b16 v[208:209], v242 offset:6400\n"
"ds_read_b64_tr_b16 v[210:211], v242 offset:9472\n"
"v_mul_f32 v177, v177, v177\n"
"v_max_f32 v180, 0, v180\n"
"v_max_f32 v181, 0, v181\n"
"s_waitcnt lgkmcnt(4)\n"
"v_mfma_f32_32x32x16_bf16 a[48:63], v[216:219], v[200:203], a[48:63]\n"
"ds_read_b64_tr_b16 v[212:213], v243 offset:320\n"
"ds_read_b64_tr_b16 v[214:215], v243 offset:3392\n"
"buffer_load_dwordx4 v231, s[40:43], s60 offen offset:2048 lds\n"
"v_mul_f32 v178, v178, v178\n"
"s_waitcnt vmcnt(15)\n"
"v_mul_f32_e64 v179, v179, v179\n"
"v_mfma_f32_32x32x16_bf16 a[48:63], v[220:223], v[204:207], a[48:63]\n"
"ds_read_b64_tr_b16 v[216:217], v243 offset:6464\n"
"ds_read_b64_tr_b16 v[218:219], v243 offset:9536\n"
"s_mul_i32 s58, s52, 18432\n"
"s_add_u32 m0, s58, 8192\n"
"s_mul_i32 s59, s71, 147456\n"
"s_add_u32 s59, s59, s58\n"
"s_add_u32 s61, s59, 24576\n"
"s_waitcnt lgkmcnt(4)\n"
"v_mfma_f32_32x32x16_bf16 a[64:79], v[224:227], v[200:203], a[64:79]\n"
"s_barrier\n"
"ds_read_b64_tr_b16 v[220:221], v242 offset:12288\n"
"ds_read_b64_tr_b16 v[222:223], v242 offset:15360\n"
"buffer_load_dwordx4 v235, s[48:51], s61 offen sc1 lds\n"
"v_cvt_pk_bf16_f32 v192, v176, v177\n"
"v_mfma_f32_32x32x16_bf16 a[64:79], v[208:211], v[204:207], a[64:79]\n"
"ds_read_b64_tr_b16 v[224:225], v242 offset:18432\n"
"ds_read_b64_tr_b16 v[226:227], v242 offset:21504\n"
"buffer_load_dwordx4 v235, s[48:51], s61 offen offset:1024 sc1 lds\n"
"v_max_f32 v182, 0, v182\n"
"v_max_f32 v183, 0, v183\n"
"s_waitcnt lgkmcnt(4)\n"
"v_mfma_f32_32x32x16_bf16 a[80:95], v[212:215], v[200:203], a[80:95]\n"
"ds_read_b64_tr_b16 v[208:209], v243 offset:12352\n"
"ds_read_b64_tr_b16 v[210:211], v243 offset:15424\n"
"s_add_u32 m0, s55, 98304\n"
"v_mul_f32 v180, v180, v180\n"
"v_mul_f32 v181, v181, v181\n"
"v_mfma_f32_32x32x16_bf16 a[80:95], v[216:219], v[204:207], a[80:95]\n"
"ds_read_b64_tr_b16 v[212:213], v243 offset:18496\n"
"ds_read_b64_tr_b16 v[214:215], v243 offset:21568\n"
"v_cvt_pk_bf16_f32 v193, v178, v179\n"
"v_max_f32_e64 v184, 0, v184\n"
"v_max_f32 v185, 0, v185\n"
"s_waitcnt lgkmcnt(4)\n"
"v_mfma_f32_32x32x16_bf16 a[96:111], v[220:223], v[200:203], a[96:111]\n"
"ds_read_b64_tr_b16 v[216:217], v242 offset:12416\n"
"ds_read_b64_tr_b16 v[218:219], v242 offset:15488\n"
"s_add_u32 s60, s19, 3538944\n"
"v_mul_f32 v182, v182, v182\n"
"v_mul_f32 v183, v183, v183\n"
"v_mfma_f32_32x32x16_bf16 a[96:111], v[224:227], v[204:207], a[96:111]\n"
"ds_read_b64_tr_b16 v[220:221], v242 offset:18560\n"
"ds_read_b64_tr_b16 v[222:223], v242 offset:21632\n"
"v_cvt_pk_bf16_f32 v194, v180, v181\n"
"v_max_f32_e64 v186, 0, v186\n"
"v_max_f32 v187, 0, v187\n"
"s_waitcnt lgkmcnt(4)\n"
"v_mfma_f32_32x32x16_bf16 a[112:127], v[208:211], v[200:203], a[112:127]\n"
"ds_read_b64_tr_b16 v[224:225], v243 offset:12480\n"
"ds_read_b64_tr_b16 v[226:227], v243 offset:15552\n"
"buffer_load_dwordx4 v229, s[40:43], s60 offen lds\n"
"v_mul_f32 v184, v184, v184\n"
"v_mul_f32 v185, v185, v185\n"
"v_mfma_f32_32x32x16_bf16 a[112:127], v[212:215], v[204:207], a[112:127]\n"
"ds_read_b64_tr_b16 v[208:209], v243 offset:18624\n"
"ds_read_b64_tr_b16 v[210:211], v243 offset:21696\n"
"v_cvt_pk_bf16_f32 v195, v182, v183\n"
"v_max_f32_e64 v188, 0, v188\n"
"v_max_f32 v189, 0, v189\n"
"s_waitcnt lgkmcnt(4)\n"
"v_mfma_f32_32x32x16_bf16 a[128:143], v[216:219], v[200:203], a[128:143]\n"
"ds_read_b64_tr_b16 v[212:213], v242 offset:12544\n"
"ds_read_b64_tr_b16 v[214:215], v242 offset:15616\n"
"buffer_load_dwordx4 v230, s[40:43], s60 offen offset:1024 lds\n"
"v_mul_f32 v186, v186, v186\n"
"v_mul_f32 v187, v187, v187\n"
"v_mfma_f32_32x32x16_bf16 a[128:143], v[220:223], v[204:207], a[128:143]\n"
"ds_read_b64_tr_b16 v[216:217], v242 offset:18688\n"
"ds_read_b64_tr_b16 v[218:219], v242 offset:21760\n"
"v_cvt_pk_bf16_f32 v196, v184, v185\n"
"v_max_f32_e64 v190, 0, v190\n"
"v_max_f32 v191, 0, v191\n"
"s_waitcnt lgkmcnt(4)\n"
"v_mfma_f32_32x32x16_bf16 a[144:159], v[224:227], v[200:203], a[144:159]\n"
"ds_read_b64_tr_b16 v[220:221], v243 offset:12608\n"
"ds_read_b64_tr_b16 v[222:223], v243 offset:15680\n"
"buffer_load_dwordx4 v231, s[40:43], s60 offen offset:2048 lds\n"
"v_mul_f32 v188, v188, v188\n"
"s_waitcnt vmcnt(15)\n"
"v_mul_f32_e64 v189, v189, v189\n"
"v_mfma_f32_32x32x16_bf16 a[144:159], v[208:211], v[204:207], a[144:159]\n"
"ds_read_b64_tr_b16 v[224:225], v243 offset:18752\n"
"ds_read_b64_tr_b16 v[226:227], v243 offset:21824\n"
"s_mul_i32 s58, s52, 18432\n"
"s_add_u32 m0, s58, 10240\n"
"s_mul_i32 s59, s71, 147456\n"
"s_add_u32 s59, s59, s58\n"
"s_add_u32 s61, s59, 26624\n"
"s_waitcnt lgkmcnt(4)\n"
"v_mfma_f32_32x32x16_bf16 a[160:175], v[212:215], v[200:203], a[160:175]\n"
"s_barrier\n"
"ds_read_b64_tr_b16 v[208:209], v242 offset:24576\n"
"ds_read_b64_tr_b16 v[210:211], v242 offset:27648\n"
"buffer_load_dwordx4 v235, s[48:51], s61 offen sc1 lds\n"
"v_cvt_pk_bf16_f32 v197, v186, v187\n"
"v_mfma_f32_32x32x16_bf16 a[160:175], v[216:219], v[204:207], a[160:175]\n"
"ds_read_b64_tr_b16 v[212:213], v242 offset:30720\n"
"ds_read_b64_tr_b16 v[214:215], v242 offset:33792\n"
"buffer_load_dwordx4 v235, s[48:51], s61 offen offset:1024 sc1 lds\n"
"v_mul_f32 v190, v190, v190\n"
"v_mul_f32 v191, v191, v191\n"
"s_waitcnt lgkmcnt(4)\n"
"v_mfma_f32_32x32x16_bf16 a[176:191], v[220:223], v[200:203], a[176:191]\n"
"ds_read_b64_tr_b16 v[216:217], v243 offset:24640\n"
"ds_read_b64_tr_b16 v[218:219], v243 offset:27712\n"
"s_add_u32 m0, s55, 110592\n"
"v_cvt_pk_bf16_f32 v198, v188, v189\n"
"v_cvt_pk_bf16_f32 v199, v190, v191\n"
"v_mfma_f32_32x32x16_bf16 a[176:191], v[224:227], v[204:207], a[176:191]\n"
"ds_read_b64_tr_b16 v[220:221], v243 offset:30784\n"
"ds_read_b64_tr_b16 v[222:223], v243 offset:33856\n"
"ds_read_b128 v[176:179], v248\n"
"ds_read_b128 v[180:183], v248 offset:32\n"
"ds_read_b128 v[184:187], v248 offset:64\n"
"s_waitcnt lgkmcnt(7)\n"
"v_mfma_f32_32x32x16_bf16 a[192:207], v[208:211], v[200:203], a[192:207]\n"
"ds_read_b64_tr_b16 v[224:225], v242 offset:24704\n"
"ds_read_b64_tr_b16 v[226:227], v242 offset:27776\n"
"s_add_u32 s17, s17, 1\n"
"s_cmp_eq_u32 s17, 9\n"
"s_cselect_b32 s17, 0, s17\n"
"s_mul_i32 s20, s17, 36864\n"
"s_add_u32 s60, s20, 0\n"
"ds_read_b128 v[188:191], v248 offset:96\n"
"s_add_u32 s18, s18, 1\n"
"s_cmp_eq_u32 s18, 9\n"
"s_mov_b32 s21, 128\n"
"s_cselect_b32 s21, -1024, s21\n"
"s_cselect_b32 s18, 0, s18\n"
"v_add_u32_e64 v248, s21, v248\n"
"v_mfma_f32_32x32x16_bf16 a[192:207], v[212:215], v[204:207], a[192:207]\n"
"ds_read_b64_tr_b16 v[208:209], v242 offset:30848\n"
"ds_read_b64_tr_b16 v[210:211], v242 offset:33920\n"
"s_waitcnt lgkmcnt(7)\n"
"v_mul_f32 v176, v249, v176\n"
"v_mul_f32 v177, v249, v177\n"
"v_mul_f32 v178, v249, v178\n"
"v_mfma_f32_32x32x16_bf16 a[208:223], v[216:219], v[200:203], a[208:223]\n"
"ds_read_b64_tr_b16 v[212:213], v243 offset:24768\n"
"ds_read_b64_tr_b16 v[214:215], v243 offset:27840\n"
"buffer_load_dwordx4 v232, s[44:47], s60 offen lds\n"
"v_mul_f32_e64 v179, v249, v179\n"
"s_waitcnt lgkmcnt(8)\n"
"v_mul_f32 v180, v249, v180\n"
"v_mfma_f32_32x32x16_bf16 a[208:223], v[220:223], v[204:207], a[208:223]\n"
"ds_read_b64_tr_b16 v[216:217], v243 offset:30912\n"
"ds_read_b64_tr_b16 v[218:219], v243 offset:33984\n"
"v_mul_f32 v181, v249, v181\n"
"v_mul_f32 v182, v249, v182\n"
"v_mul_f32 v183, v249, v183\n"
"s_waitcnt lgkmcnt(4)\n"
"v_mfma_f32_32x32x16_bf16 a[224:239], v[224:227], v[200:203], a[224:239]\n"
"ds_read_b64_tr_b16 v[220:221], v242 offset:24832\n"
"ds_read_b64_tr_b16 v[222:223], v242 offset:27904\n"
"buffer_load_dwordx4 v233, s[44:47], s60 offen offset:1024 lds\n"
"v_mul_f32 v184, v249, v184\n"
"v_mul_f32 v185, v249, v185\n"
"v_mfma_f32_32x32x16_bf16 a[224:239], v[208:211], v[204:207], a[224:239]\n"
"ds_read_b64_tr_b16 v[224:225], v242 offset:30976\n"
"ds_read_b64_tr_b16 v[226:227], v242 offset:34048\n"
"v_mul_f32 v186, v249, v186\n"
"v_mul_f32 v187, v249, v187\n"
"v_mul_f32 v188, v249, v188\n"
"s_waitcnt lgkmcnt(4)\n"
"v_mfma_f32_32x32x16_bf16 a[240:255], v[212:215], v[200:203], a[240:255]\n"
"ds_read_b64_tr_b16 v[208:209], v243 offset:24896\n"
"ds_read_b64_tr_b16 v[210:211], v243 offset:27968\n"
"buffer_load_dwordx4 v234, s[44:47], s60 offen offset:2048 lds\n"
"v_mul_f32 v189, v249, v189\n"
"s_waitcnt vmcnt(15)\n"
"v_mul_f32_e64 v190, v249, v190\n"
"v_mfma_f32_32x32x16_bf16 a[240:255], v[216:219], v[204:207], a[240:255]\n"
"ds_read_b64_tr_b16 v[212:213], v243 offset:31040\n"
"ds_read_b64_tr_b16 v[214:215], v243 offset:34112\n"
"s_mul_i32 s58, s52, 18432\n"
"s_add_u32 m0, s58, 12288\n"
"s_mul_i32 s59, s71, 147456\n"
"s_add_u32 s59, s59, s58\n"
"s_add_u32 s61, s59, 28672\n"
"s_waitcnt lgkmcnt(4)\n"
"v_mfma_f32_32x32x16_bf16 v[0:15], v[220:223], v[200:203], v[0:15]\n"
"s_barrier\n"
"ds_read_b64_tr_b16 v[216:217], v238\n"
"ds_read_b64_tr_b16 v[218:219], v238 offset:512\n"
"buffer_load_dwordx4 v235, s[48:51], s61 offen sc1 lds\n"
"v_mul_f32 v191, v249, v191\n"
"v_mfma_f32_32x32x16_bf16 v[0:15], v[224:227], v[204:207], v[0:15]\n"
"ds_read_b64_tr_b16 v[220:221], v238 offset:1024\n"
"ds_read_b64_tr_b16 v[222:223], v238 offset:1536\n"
"buffer_load_dwordx4 v235, s[48:51], s61 offen offset:1024 sc1 lds\n"
"s_waitcnt lgkmcnt(4)\n"
"v_mfma_f32_32x32x16_bf16 v[16:31], v[208:211], v[200:203], v[16:31]\n"
"ds_read_b64_tr_b16 v[224:225], v238 offset:2048\n"
"ds_read_b64_tr_b16 v[226:227], v238 offset:2560\n"
"s_add_u32 m0, s55, 122880\n"
"v_mfma_f32_32x32x16_bf16 v[16:31], v[212:215], v[204:207], v[16:31]\n"
"ds_read_b64_tr_b16 v[208:209], v238 offset:3072\n"
"ds_read_b64_tr_b16 v[210:211], v238 offset:3584\n"
"s_waitcnt lgkmcnt(4)\n"
"v_mfma_f32_32x32x16_bf16 v[176:191], v[216:219], v[32:35], v[176:191]\n"
"ds_read_b64_tr_b16 v[212:213], v238 offset:4096\n"
"ds_read_b64_tr_b16 v[214:215], v238 offset:4608\n"
"s_add_u32 s60, s20, 384\n"
"v_mfma_f32_32x32x16_bf16 v[176:191], v[220:223], v[36:39], v[176:191]\n"
"ds_read_b64_tr_b16 v[216:217], v238 offset:5120\n"
"ds_read_b64_tr_b16 v[218:219], v238 offset:5632\n"
"s_waitcnt lgkmcnt(4)\n"
"v_mfma_f32_32x32x16_bf16 v[176:191], v[224:227], v[40:43], v[176:191]\n"
"ds_read_b64_tr_b16 v[220:221], v238 offset:6144\n"
"ds_read_b64_tr_b16 v[222:223], v238 offset:6656\n"
"buffer_load_dwordx4 v232, s[44:47], s60 offen lds\n"
"v_mfma_f32_32x32x16_bf16 v[176:191], v[208:211], v[44:47], v[176:191]\n"
"ds_read_b64_tr_b16 v[224:225], v238 offset:7168\n"
"ds_read_b64_tr_b16 v[226:227], v238 offset:7680\n"
"s_waitcnt lgkmcnt(4)\n"
"v_mfma_f32_32x32x16_bf16 v[176:191], v[212:215], v[48:51], v[176:191]\n"
"ds_read_b64_tr_b16 v[208:209], v238 offset:8192\n"
"ds_read_b64_tr_b16 v[210:211], v238 offset:8704\n"
"buffer_load_dwordx4 v233, s[44:47], s60 offen offset:1024 lds\n"
"v_mfma_f32_32x32x16_bf16 v[176:191], v[216:219], v[52:55], v[176:191]\n"
"ds_read_b64_tr_b16 v[212:213], v238 offset:9216\n"
"ds_read_b64_tr_b16 v[214:215], v238 offset:9728\n"
"s_waitcnt lgkmcnt(4)\n"
"v_mfma_f32_32x32x16_bf16 v[176:191], v[220:223], v[56:59], v[176:191]\n"
"ds_read_b64_tr_b16 v[216:217], v238 offset:10240\n"
"ds_read_b64_tr_b16 v[218:219], v238 offset:10752\n"
"buffer_load_dwordx4 v234, s[44:47], s60 offen offset:2048 lds\n"
"v_mfma_f32_32x32x16_bf16 v[176:191], v[224:227], v[60:63], v[176:191]\n"
"s_waitcnt vmcnt(15)\n"
"ds_read_b64_tr_b16 v[220:221], v238 offset:11264\n"
"ds_read_b64_tr_b16 v[222:223], v238 offset:11776\n"
"s_mul_i32 s58, s52, 18432\n"
"s_add_u32 m0, s58, 14336\n"
"s_mul_i32 s59, s71, 147456\n"
"s_add_u32 s59, s59, s58\n"
"s_add_u32 s61, s59, 30720\n"
"s_waitcnt lgkmcnt(4)\n"
"v_mfma_f32_32x32x16_bf16 v[176:191], v[208:211], v[64:67], v[176:191]\n"
"s_barrier\n"
"ds_read_b64_tr_b16 v[224:225], v238 offset:12288\n"
"ds_read_b64_tr_b16 v[226:227], v238 offset:12800\n"
"buffer_load_dwordx4 v235, s[48:51], s61 offen sc1 lds\n"
"v_mfma_f32_32x32x16_bf16 v[176:191], v[212:215], v[68:71], v[176:191]\n"
"ds_read_b64_tr_b16 v[208:209], v238 offset:13312\n"
"ds_read_b64_tr_b16 v[210:211], v238 offset:13824\n"
"buffer_load_dwordx4 v235, s[48:51], s61 offen offset:1024 sc1 lds\n"
"s_waitcnt lgkmcnt(4)\n"
"v_mfma_f32_32x32x16_bf16 v[176:191], v[216:219], v[72:75], v[176:191]\n"
"ds_read_b64_tr_b16 v[212:213], v238 offset:14336\n"
"ds_read_b64_tr_b16 v[214:215], v238 offset:14848\n"
"s_add_u32 m0, s55, 135168\n"
"v_mfma_f32_32x32x16_bf16 v[176:191], v[220:223], v[76:79], v[176:191]\n"
"ds_read_b64_tr_b16 v[216:217], v238 offset:15360\n"
"ds_read_b64_tr_b16 v[218:219], v238 offset:15872\n"
"s_waitcnt lgkmcnt(4)\n"
"v_mfma_f32_32x32x16_bf16 v[176:191], v[224:227], v[80:83], v[176:191]\n"
"ds_read_b64_tr_b16 v[220:221], v238 offset:16384\n"
"ds_read_b64_tr_b16 v[222:223], v238 offset:16896\n"
"s_add_u32 s60, s20, 768\n"
"v_mfma_f32_32x32x16_bf16 v[176:191], v[208:211], v[84:87], v[176:191]\n"
"ds_read_b64_tr_b16 v[224:225], v238 offset:17408\n"
"ds_read_b64_tr_b16 v[226:227], v238 offset:17920\n"
"s_waitcnt lgkmcnt(4)\n"
"v_mfma_f32_32x32x16_bf16 v[176:191], v[212:215], v[88:91], v[176:191]\n"
"ds_read_b64_tr_b16 v[208:209], v238 offset:18432\n"
"ds_read_b64_tr_b16 v[210:211], v238 offset:18944\n"
"buffer_load_dwordx4 v232, s[44:47], s60 offen lds\n"
"v_mfma_f32_32x32x16_bf16 v[176:191], v[216:219], v[92:95], v[176:191]\n"
"ds_read_b64_tr_b16 v[212:213], v238 offset:19456\n"
"ds_read_b64_tr_b16 v[214:215], v238 offset:19968\n"
"s_waitcnt lgkmcnt(4)\n"
"v_mfma_f32_32x32x16_bf16 v[176:191], v[220:223], v[96:99], v[176:191]\n"
"ds_read_b64_tr_b16 v[216:217], v238 offset:20480\n"
"ds_read_b64_tr_b16 v[218:219], v238 offset:20992\n"
"buffer_load_dwordx4 v233, s[44:47], s60 offen offset:1024 lds\n"
"v_mfma_f32_32x32x16_bf16 v[176:191], v[224:227], v[100:103], v[176:191]\n"
"ds_read_b64_tr_b16 v[220:221], v238 offset:21504\n"
"ds_read_b64_tr_b16 v[222:223], v238 offset:22016\n"
"s_waitcnt lgkmcnt(4)\n"
"v_mfma_f32_32x32x16_bf16 v[176:191], v[208:211], v[104:107], v[176:191]\n"
"ds_read_b64_tr_b16 v[224:225], v238 offset:22528\n"
"ds_read_b64_tr_b16 v[226:227], v238 offset:23040\n"
"buffer_load_dwordx4 v234, s[44:47], s60 offen offset:2048 lds\n"
"v_mfma_f32_32x32x16_bf16 v[176:191], v[212:215], v[108:111], v[176:191]\n"
"s_waitcnt vmcnt(15)\n"
"ds_read_b64_tr_b16 v[208:209], v238 offset:23552\n"
"ds_read_b64_tr_b16 v[210:211], v238 offset:24064\n"
"s_mul_i32 s58, s52, 18432\n"
"s_add_u32 m0, s58, 16384\n"
"s_mul_i32 s59, s71, 147456\n"
"s_add_u32 s59, s59, s58\n"
"s_add_u32 s61, s59, 32768\n"
"s_waitcnt lgkmcnt(4)\n"
"v_mfma_f32_32x32x16_bf16 v[176:191], v[216:219], v[112:115], v[176:191]\n"
"s_barrier\n"
"ds_read_b64_tr_b16 v[212:213], v238 offset:24576\n"
"ds_read_b64_tr_b16 v[214:215], v238 offset:25088\n"
"buffer_load_dwordx4 v235, s[48:51], s61 offen sc1 lds\n"
"v_mfma_f32_32x32x16_bf16 v[176:191], v[220:223], v[116:119], v[176:191]\n"
"ds_read_b64_tr_b16 v[216:217], v238 offset:25600\n"
"ds_read_b64_tr_b16 v[218:219], v238 offset:26112\n"
"buffer_load_dwordx4 v235, s[48:51], s61 offen offset:1024 sc1 lds\n"
"s_waitcnt lgkmcnt(4)\n"
"v_mfma_f32_32x32x16_bf16 v[176:191], v[224:227], v[120:123], v[176:191]\n"
"ds_read_b64_tr_b16 v[220:221], v238 offset:26624\n"
"ds_read_b64_tr_b16 v[222:223], v238 offset:27136\n"
"s_add_u32 m0, s55, 73728\n"
"v_mfma_f32_32x32x16_bf16 v[176:191], v[208:211], v[124:127], v[176:191]\n"
"ds_read_b64_tr_b16 v[224:225], v238 offset:27648\n"
"ds_read_b64_tr_b16 v[226:227], v238 offset:28160\n"
"s_waitcnt lgkmcnt(4)\n"
"v_mfma_f32_32x32x16_bf16 v[176:191], v[212:215], v[128:131], v[176:191]\n"
"ds_read_b64_tr_b16 v[208:209], v238 offset:28672\n"
"ds_read_b64_tr_b16 v[210:211], v238 offset:29184\n"
"s_add_u32 s16, s16, 1\n"
"s_cmp_eq_u32 s16, 9\n"
"s_cselect_b32 s16, 0, s16\n"
"s_mul_i32 s19, s16, 64\n"
"s_add_u32 s60, s19, 0\n"
"v_mfma_f32_32x32x16_bf16 v[176:191], v[216:219], v[132:135], v[176:191]\n"
"ds_read_b64_tr_b16 v[212:213], v238 offset:29696\n"
"ds_read_b64_tr_b16 v[214:215], v238 offset:30208\n"
"s_waitcnt lgkmcnt(4)\n"
"v_mfma_f32_32x32x16_bf16 v[176:191], v[220:223], v[136:139], v[176:191]\n"
"ds_read_b64_tr_b16 v[216:217], v238 offset:30720\n"
"ds_read_b64_tr_b16 v[218:219], v238 offset:31232\n"
"buffer_load_dwordx4 v229, s[40:43], s60 offen lds\n"
"v_mfma_f32_32x32x16_bf16 v[176:191], v[224:227], v[140:143], v[176:191]\n"
"ds_read_b64_tr_b16 v[220:221], v238 offset:31744\n"
"ds_read_b64_tr_b16 v[222:223], v238 offset:32256\n"
"s_waitcnt lgkmcnt(4)\n"
"v_mfma_f32_32x32x16_bf16 v[176:191], v[208:211], v[144:147], v[176:191]\n"
"ds_read_b64_tr_b16 v[224:225], v238 offset:32768\n"
"ds_read_b64_tr_b16 v[226:227], v238 offset:33280\n"
"buffer_load_dwordx4 v230, s[40:43], s60 offen offset:1024 lds\n"
"v_mfma_f32_32x32x16_bf16 v[176:191], v[212:215], v[148:151], v[176:191]\n"
"ds_read_b64_tr_b16 v[208:209], v238 offset:33792\n"
"ds_read_b64_tr_b16 v[210:211], v238 offset:34304\n"
"s_waitcnt lgkmcnt(4)\n"
"v_mfma_f32_32x32x16_bf16 v[176:191], v[216:219], v[152:155], v[176:191]\n"
"ds_read_b64_tr_b16 v[212:213], v238 offset:34816\n"
"ds_read_b64_tr_b16 v[214:215], v238 offset:35328\n"
"buffer_load_dwordx4 v231, s[40:43], s60 offen offset:2048 lds\n"
"v_mfma_f32_32x32x16_bf16 v[176:191], v[220:223], v[156:159], v[176:191]\n"
"s_waitcnt vmcnt(15)\n"
"ds_read_b64_tr_b16 v[216:217], v238 offset:35840\n"
"ds_read_b64_tr_b16 v[218:219], v238 offset:36352\n"
"s_mul_i32 s58, s52, 2048\n"
"s_add_u32 m0, s58, 147456\n"
"s_mul_i32 s59, s71, 8192\n"
"s_add_u32 s59, s59, s58\n"
"s_add_u32 s61, s59, 4734976\n"
"s_waitcnt lgkmcnt(4)\n"
"v_mfma_f32_32x32x16_bf16 v[176:191], v[224:227], v[160:163], v[176:191]\n"
"s_barrier\n"
"ds_read_b64_tr_b16 v[220:221], v242\n"
"ds_read_b64_tr_b16 v[222:223], v242 offset:3072\n"
"buffer_load_dwordx4 v235, s[48:51], s61 offen sc1 lds\n"
"v_mfma_f32_32x32x16_bf16 v[176:191], v[208:211], v[164:167], v[176:191]\n"
"ds_read_b64_tr_b16 v[224:225], v242 offset:6144\n"
"ds_read_b64_tr_b16 v[226:227], v242 offset:9216\n"
"buffer_load_dwordx4 v235, s[48:51], s61 offen offset:1024 sc1 lds\n"
"s_waitcnt lgkmcnt(4)\n"
"v_mfma_f32_32x32x16_bf16 v[176:191], v[212:215], v[168:171], v[176:191]\n"
"ds_read_b64_tr_b16 v[208:209], v243 offset:64\n"
"ds_read_b64_tr_b16 v[210:211], v243 offset:3136\n"
"s_add_u32 m0, s55, 86016\n"
"v_mfma_f32_32x32x16_bf16 v[176:191], v[216:219], v[172:175], v[176:191]\n"
"ds_read_b64_tr_b16 v[212:213], v243 offset:6208\n"
"ds_read_b64_tr_b16 v[214:215], v243 offset:9280\n"
"s_waitcnt lgkmcnt(4)\n"
"v_mfma_f32_32x32x16_bf16 a[0:15], v[220:223], v[192:195], a[0:15]\n"
"ds_read_b64_tr_b16 v[216:217], v242 offset:128\n"
"ds_read_b64_tr_b16 v[218:219], v242 offset:3200\n"
"s_add_u32 s60, s19, 1769472\n"
"v_mfma_f32_32x32x16_bf16 a[0:15], v[224:227], v[196:199], a[0:15]\n"
"ds_read_b64_tr_b16 v[220:221], v242 offset:6272\n"
"ds_read_b64_tr_b16 v[222:223], v242 offset:9344\n"
"s_waitcnt lgkmcnt(4)\n"
"v_mfma_f32_32x32x16_bf16 a[16:31], v[208:211], v[192:195], a[16:31]\n"
"ds_read_b64_tr_b16 v[224:225], v243 offset:192\n"
"ds_read_b64_tr_b16 v[226:227], v243 offset:3264\n"
"buffer_load_dwordx4 v229, s[40:43], s60 offen lds\n"
"v_mfma_f32_32x32x16_bf16 a[16:31], v[212:215], v[196:199], a[16:31]\n"
"ds_read_b64_tr_b16 v[208:209], v243 offset:6336\n"
"ds_read_b64_tr_b16 v[210:211], v243 offset:9408\n"
"v_max_f32 v176, 0, v176\n"
"v_max_f32_e64 v177, 0, v177\n"
"v_max_f32 v178, 0, v178\n"
"s_waitcnt lgkmcnt(4)\n"
"v_mfma_f32_32x32x16_bf16 a[32:47], v[216:219], v[192:195], a[32:47]\n"
"ds_read_b64_tr_b16 v[212:213], v242 offset:256\n"
"ds_read_b64_tr_b16 v[214:215], v242 offset:3328\n"
"buffer_load_dwordx4 v230, s[40:43], s60 offen offset:1024 lds\n"
"v_max_f32 v179, 0, v179\n"
"v_mul_f32 v176, v176, v176\n"
"v_mfma_f32_32x32x16_bf16 a[32:47], v[220:223], v[196:199], a[32:47]\n"
"ds_read_b64_tr_b16 v[216:217], v242 offset:6400\n"
"ds_read_b64_tr_b16 v[218:219], v242 offset:9472\n"
"v_mul_f32 v177, v177, v177\n"
"v_max_f32 v180, 0, v180\n"
"v_max_f32 v181, 0, v181\n"
"s_waitcnt lgkmcnt(4)\n"
"v_mfma_f32_32x32x16_bf16 a[48:63], v[224:227], v[192:195], a[48:63]\n"
"ds_read_b64_tr_b16 v[220:221], v243 offset:320\n"
"ds_read_b64_tr_b16 v[222:223], v243 offset:3392\n"
"buffer_load_dwordx4 v231, s[40:43], s60 offen offset:2048 lds\n"
"v_mul_f32 v178, v178, v178\n"
"s_waitcnt vmcnt(15)\n"
"v_mul_f32_e64 v179, v179, v179\n"
"v_mfma_f32_32x32x16_bf16 a[48:63], v[208:211], v[196:199], a[48:63]\n"
"ds_read_b64_tr_b16 v[224:225], v243 offset:6464\n"
"ds_read_b64_tr_b16 v[226:227], v243 offset:9536\n"
"v_cvt_pk_bf16_f32 v200, v176, v177\n"
"v_max_f32 v182, 0, v182\n"
"s_waitcnt lgkmcnt(4)\n"
"v_mfma_f32_32x32x16_bf16 a[64:79], v[212:215], v[192:195], a[64:79]\n"
"s_barrier\n"
"ds_read_b64_tr_b16 v[208:209], v242 offset:12288\n"
"ds_read_b64_tr_b16 v[210:211], v242 offset:15360\n"
"v_max_f32 v183, 0, v183\n"
"v_mul_f32_e64 v180, v180, v180\n"
"v_mfma_f32_32x32x16_bf16 a[64:79], v[216:219], v[196:199], a[64:79]\n"
"ds_read_b64_tr_b16 v[212:213], v242 offset:18432\n"
"ds_read_b64_tr_b16 v[214:215], v242 offset:21504\n"
"v_mul_f32_e64 v181, v181, v181\n"
"v_cvt_pk_bf16_f32 v201, v178, v179\n"
"v_max_f32 v184, 0, v184\n"
"s_waitcnt lgkmcnt(4)\n"
"v_mfma_f32_32x32x16_bf16 a[80:95], v[220:223], v[192:195], a[80:95]\n"
"ds_read_b64_tr_b16 v[216:217], v243 offset:12352\n"
"ds_read_b64_tr_b16 v[218:219], v243 offset:15424\n"
"s_add_u32 m0, s55, 98304\n"
"v_max_f32 v185, 0, v185\n"
"v_mul_f32 v182, v182, v182\n"
"v_mfma_f32_32x32x16_bf16 a[80:95], v[224:227], v[196:199], a[80:95]\n"
"ds_read_b64_tr_b16 v[220:221], v243 offset:18496\n"
"ds_read_b64_tr_b16 v[222:223], v243 offset:21568\n"
"v_mul_f32_e64 v183, v183, v183\n"
"v_cvt_pk_bf16_f32 v202, v180, v181\n"
"v_max_f32 v186, 0, v186\n"
"s_waitcnt lgkmcnt(4)\n"
"v_mfma_f32_32x32x16_bf16 a[96:111], v[208:211], v[192:195], a[96:111]\n"
"ds_read_b64_tr_b16 v[224:225], v242 offset:12416\n"
"ds_read_b64_tr_b16 v[226:227], v242 offset:15488\n"
"s_add_u32 s60, s19, 3538944\n"
"v_max_f32 v187, 0, v187\n"
"v_mul_f32 v184, v184, v184\n"
"v_mfma_f32_32x32x16_bf16 a[96:111], v[212:215], v[196:199], a[96:111]\n"
"ds_read_b64_tr_b16 v[208:209], v242 offset:18560\n"
"ds_read_b64_tr_b16 v[210:211], v242 offset:21632\n"
"v_mul_f32_e64 v185, v185, v185\n"
"v_cvt_pk_bf16_f32 v203, v182, v183\n"
"v_max_f32 v188, 0, v188\n"
"s_waitcnt lgkmcnt(4)\n"
"v_mfma_f32_32x32x16_bf16 a[112:127], v[216:219], v[192:195], a[112:127]\n"
"ds_read_b64_tr_b16 v[212:213], v243 offset:12480\n"
"ds_read_b64_tr_b16 v[214:215], v243 offset:15552\n"
"buffer_load_dwordx4 v229, s[40:43], s60 offen lds\n"
"v_max_f32 v189, 0, v189\n"
"v_mul_f32 v186, v186, v186\n"
"v_mfma_f32_32x32x16_bf16 a[112:127], v[220:223], v[196:199], a[112:127]\n"
"ds_read_b64_tr_b16 v[216:217], v243 offset:18624\n"
"ds_read_b64_tr_b16 v[218:219], v243 offset:21696\n"
"v_mul_f32_e64 v187, v187, v187\n"
"v_cvt_pk_bf16_f32 v204, v184, v185\n"
"v_max_f32 v190, 0, v190\n"
"s_waitcnt lgkmcnt(4)\n"
"v_mfma_f32_32x32x16_bf16 a[128:143], v[224:227], v[192:195], a[128:143]\n"
"ds_read_b64_tr_b16 v[220:221], v242 offset:12544\n"
"ds_read_b64_tr_b16 v[222:223], v242 offset:15616\n"
"buffer_load_dwordx4 v230, s[40:43], s60 offen offset:1024 lds\n"
"v_max_f32 v191, 0, v191\n"
"v_mul_f32 v188, v188, v188\n"
"v_mfma_f32_32x32x16_bf16 a[128:143], v[208:211], v[196:199], a[128:143]\n"
"ds_read_b64_tr_b16 v[224:225], v242 offset:18688\n"
"ds_read_b64_tr_b16 v[226:227], v242 offset:21760\n"
"v_mul_f32_e64 v189, v189, v189\n"
"v_cvt_pk_bf16_f32 v205, v186, v187\n"
"v_mul_f32 v190, v190, v190\n"
"s_waitcnt lgkmcnt(4)\n"
"v_mfma_f32_32x32x16_bf16 a[144:159], v[212:215], v[192:195], a[144:159]\n"
"ds_read_b64_tr_b16 v[208:209], v243 offset:12608\n"
"ds_read_b64_tr_b16 v[210:211], v243 offset:15680\n"
"buffer_load_dwordx4 v231, s[40:43], s60 offen offset:2048 lds\n"
"v_mul_f32_e64 v191, v191, v191\n"
"v_cvt_pk_bf16_f32 v206, v188, v189\n"
"v_mfma_f32_32x32x16_bf16 a[144:159], v[216:219], v[196:199], a[144:159]\n"
"s_waitcnt vmcnt(13)\n"
"ds_read_b64_tr_b16 v[212:213], v243 offset:18752\n"
"ds_read_b64_tr_b16 v[214:215], v243 offset:21824\n"
"v_cvt_pk_bf16_f32 v207, v190, v191\n"
"ds_read_b128 v[176:179], v248\n"
"s_waitcnt lgkmcnt(5)\n"
"v_mfma_f32_32x32x16_bf16 a[160:175], v[220:223], v[192:195], a[160:175]\n"
"s_barrier\n"
"ds_read_b64_tr_b16 v[216:217], v242 offset:24576\n"
"ds_read_b64_tr_b16 v[218:219], v242 offset:27648\n"
"ds_read_b128 v[180:183], v248 offset:32\n"
"ds_read_b128 v[184:187], v248 offset:64\n"
"v_mfma_f32_32x32x16_bf16 a[160:175], v[224:227], v[196:199], a[160:175]\n"
"ds_read_b64_tr_b16 v[220:221], v242 offset:30720\n"
"ds_read_b64_tr_b16 v[222:223], v242 offset:33792\n"
"ds_read_b128 v[188:191], v248 offset:96\n"
"s_add_u32 s18, s18, 1\n"
"s_cmp_eq_u32 s18, 9\n"
"s_mov_b32 s21, 128\n"
"s_cselect_b32 s21, -1024, s21\n"
"s_cselect_b32 s18, 0, s18\n"
"v_add_u32_e64 v248, s21, v248\n"
"s_waitcnt lgkmcnt(7)\n"
"v_mul_f32 v176, v249, v176\n"
"v_mfma_f32_32x32x16_bf16 a[176:191], v[208:211], v[192:195], a[176:191]\n"
"ds_read_b64_tr_b16 v[224:225], v243 offset:24640\n"
"ds_read_b64_tr_b16 v[226:227], v243 offset:27712\n"
"s_add_u32 m0, s55, 110592\n"
"v_mul_f32 v177, v249, v177\n"
"v_mul_f32 v178, v249, v178\n"
"v_mfma_f32_32x32x16_bf16 a[176:191], v[212:215], v[196:199], a[176:191]\n"
"ds_read_b64_tr_b16 v[208:209], v243 offset:30784\n"
"ds_read_b64_tr_b16 v[210:211], v243 offset:33856\n"
"v_mul_f32_e64 v179, v249, v179\n"
"s_waitcnt lgkmcnt(8)\n"
"v_mul_f32 v180, v249, v180\n"
"v_mul_f32 v181, v249, v181\n"
"s_waitcnt lgkmcnt(5)\n"
"v_mfma_f32_32x32x16_bf16 a[192:207], v[216:219], v[192:195], a[192:207]\n"
"ds_read_b64_tr_b16 v[212:213], v242 offset:24704\n"
"ds_read_b64_tr_b16 v[214:215], v242 offset:27776\n"
"s_add_u32 s17, s17, 1\n"
"s_cmp_eq_u32 s17, 9\n"
"s_cselect_b32 s17, 0, s17\n"
"s_mul_i32 s20, s17, 36864\n"
"s_add_u32 s60, s20, 0\n"
"v_mul_f32 v182, v249, v182\n"
"v_mul_f32 v183, v249, v183\n"
"v_mfma_f32_32x32x16_bf16 a[192:207], v[220:223], v[196:199], a[192:207]\n"
"ds_read_b64_tr_b16 v[216:217], v242 offset:30848\n"
"ds_read_b64_tr_b16 v[218:219], v242 offset:33920\n"
"v_mul_f32 v184, v249, v184\n"
"v_mul_f32 v185, v249, v185\n"
"v_mul_f32 v186, v249, v186\n"
"s_waitcnt lgkmcnt(4)\n"
"v_mfma_f32_32x32x16_bf16 a[208:223], v[224:227], v[192:195], a[208:223]\n"
"ds_read_b64_tr_b16 v[220:221], v243 offset:24768\n"
"ds_read_b64_tr_b16 v[222:223], v243 offset:27840\n"
"buffer_load_dwordx4 v232, s[44:47], s60 offen lds\n"
"v_mul_f32 v187, v249, v187\n"
"v_mul_f32 v188, v249, v188\n"
"v_mfma_f32_32x32x16_bf16 a[208:223], v[208:211], v[196:199], a[208:223]\n"
"ds_read_b64_tr_b16 v[224:225], v243 offset:30912\n"
"ds_read_b64_tr_b16 v[226:227], v243 offset:33984\n"
"v_mul_f32 v189, v249, v189\n"
"v_mul_f32 v190, v249, v190\n"
"v_mul_f32 v191, v249, v191\n"
"s_waitcnt lgkmcnt(4)\n"
"v_mfma_f32_32x32x16_bf16 a[224:239], v[212:215], v[192:195], a[224:239]\n"
"ds_read_b64_tr_b16 v[208:209], v242 offset:24832\n"
"ds_read_b64_tr_b16 v[210:211], v242 offset:27904\n"
"buffer_load_dwordx4 v233, s[44:47], s60 offen offset:1024 lds\n"
"v_mfma_f32_32x32x16_bf16 a[224:239], v[216:219], v[196:199], a[224:239]\n"
"ds_read_b64_tr_b16 v[212:213], v242 offset:30976\n"
"ds_read_b64_tr_b16 v[214:215], v242 offset:34048\n"
"s_waitcnt lgkmcnt(4)\n"
"v_mfma_f32_32x32x16_bf16 a[240:255], v[220:223], v[192:195], a[240:255]\n"
"ds_read_b64_tr_b16 v[216:217], v243 offset:24896\n"
"ds_read_b64_tr_b16 v[218:219], v243 offset:27968\n"
"buffer_load_dwordx4 v234, s[44:47], s60 offen offset:2048 lds\n"
"v_mfma_f32_32x32x16_bf16 a[240:255], v[224:227], v[196:199], a[240:255]\n"
"s_waitcnt vmcnt(11)\n"
"ds_read_b64_tr_b16 v[220:221], v243 offset:31040\n"
"ds_read_b64_tr_b16 v[222:223], v243 offset:34112\n"
"s_waitcnt lgkmcnt(4)\n"
"v_mfma_f32_32x32x16_bf16 v[0:15], v[208:211], v[192:195], v[0:15]\n"
"s_barrier\n"
"ds_read_b64_tr_b16 v[224:225], v238\n"
"ds_read_b64_tr_b16 v[226:227], v238 offset:512\n"
"v_mfma_f32_32x32x16_bf16 v[0:15], v[212:215], v[196:199], v[0:15]\n"
"ds_read_b64_tr_b16 v[208:209], v238 offset:1024\n"
"ds_read_b64_tr_b16 v[210:211], v238 offset:1536\n"
"s_waitcnt lgkmcnt(4)\n"
"v_mfma_f32_32x32x16_bf16 v[16:31], v[216:219], v[192:195], v[16:31]\n"
"ds_read_b64_tr_b16 v[212:213], v238 offset:2048\n"
"ds_read_b64_tr_b16 v[214:215], v238 offset:2560\n"
"s_add_u32 m0, s55, 122880\n"
"v_mfma_f32_32x32x16_bf16 v[16:31], v[220:223], v[196:199], v[16:31]\n"
"ds_read_b64_tr_b16 v[216:217], v238 offset:3072\n"
"ds_read_b64_tr_b16 v[218:219], v238 offset:3584\n"
"s_waitcnt lgkmcnt(4)\n"
"v_mfma_f32_32x32x16_bf16 v[176:191], v[224:227], v[32:35], v[176:191]\n"
"ds_read_b64_tr_b16 v[220:221], v238 offset:4096\n"
"ds_read_b64_tr_b16 v[222:223], v238 offset:4608\n"
"s_add_u32 s60, s20, 384\n"
"v_mfma_f32_32x32x16_bf16 v[176:191], v[208:211], v[36:39], v[176:191]\n"
"ds_read_b64_tr_b16 v[224:225], v238 offset:5120\n"
"ds_read_b64_tr_b16 v[226:227], v238 offset:5632\n"
"s_waitcnt lgkmcnt(4)\n"
"v_mfma_f32_32x32x16_bf16 v[176:191], v[212:215], v[40:43], v[176:191]\n"
"ds_read_b64_tr_b16 v[208:209], v238 offset:6144\n"
"ds_read_b64_tr_b16 v[210:211], v238 offset:6656\n"
"buffer_load_dwordx4 v232, s[44:47], s60 offen lds\n"
"v_mfma_f32_32x32x16_bf16 v[176:191], v[216:219], v[44:47], v[176:191]\n"
"ds_read_b64_tr_b16 v[212:213], v238 offset:7168\n"
"ds_read_b64_tr_b16 v[214:215], v238 offset:7680\n"
"s_waitcnt lgkmcnt(4)\n"
"v_mfma_f32_32x32x16_bf16 v[176:191], v[220:223], v[48:51], v[176:191]\n"
"ds_read_b64_tr_b16 v[216:217], v238 offset:8192\n"
"ds_read_b64_tr_b16 v[218:219], v238 offset:8704\n"
"buffer_load_dwordx4 v233, s[44:47], s60 offen offset:1024 lds\n"
"v_mfma_f32_32x32x16_bf16 v[176:191], v[224:227], v[52:55], v[176:191]\n"
"ds_read_b64_tr_b16 v[220:221], v238 offset:9216\n"
"ds_read_b64_tr_b16 v[222:223], v238 offset:9728\n"
"s_waitcnt lgkmcnt(4)\n"
"v_mfma_f32_32x32x16_bf16 v[176:191], v[208:211], v[56:59], v[176:191]\n"
"ds_read_b64_tr_b16 v[224:225], v238 offset:10240\n"
"ds_read_b64_tr_b16 v[226:227], v238 offset:10752\n"
"buffer_load_dwordx4 v234, s[44:47], s60 offen offset:2048 lds\n"
"v_mfma_f32_32x32x16_bf16 v[176:191], v[212:215], v[60:63], v[176:191]\n"
"s_waitcnt vmcnt(9)\n"
"ds_read_b64_tr_b16 v[208:209], v238 offset:11264\n"
"ds_read_b64_tr_b16 v[210:211], v238 offset:11776\n"
"s_waitcnt lgkmcnt(4)\n"
"v_mfma_f32_32x32x16_bf16 v[176:191], v[216:219], v[64:67], v[176:191]\n"
"s_barrier\n"
"ds_read_b64_tr_b16 v[212:213], v238 offset:12288\n"
"ds_read_b64_tr_b16 v[214:215], v238 offset:12800\n"
"v_mfma_f32_32x32x16_bf16 v[176:191], v[220:223], v[68:71], v[176:191]\n"
"ds_read_b64_tr_b16 v[216:217], v238 offset:13312\n"
"ds_read_b64_tr_b16 v[218:219], v238 offset:13824\n"
"s_waitcnt lgkmcnt(4)\n"
"v_mfma_f32_32x32x16_bf16 v[176:191], v[224:227], v[72:75], v[176:191]\n"
"ds_read_b64_tr_b16 v[220:221], v238 offset:14336\n"
"ds_read_b64_tr_b16 v[222:223], v238 offset:14848\n"
"s_add_u32 m0, s55, 135168\n"
"v_mfma_f32_32x32x16_bf16 v[176:191], v[208:211], v[76:79], v[176:191]\n"
"ds_read_b64_tr_b16 v[224:225], v238 offset:15360\n"
"ds_read_b64_tr_b16 v[226:227], v238 offset:15872\n"
"s_waitcnt lgkmcnt(4)\n"
"v_mfma_f32_32x32x16_bf16 v[176:191], v[212:215], v[80:83], v[176:191]\n"
"ds_read_b64_tr_b16 v[208:209], v238 offset:16384\n"
"ds_read_b64_tr_b16 v[210:211], v238 offset:16896\n"
"s_add_u32 s60, s20, 768\n"
"v_mfma_f32_32x32x16_bf16 v[176:191], v[216:219], v[84:87], v[176:191]\n"
"ds_read_b64_tr_b16 v[212:213], v238 offset:17408\n"
"ds_read_b64_tr_b16 v[214:215], v238 offset:17920\n"
"s_waitcnt lgkmcnt(4)\n"
"v_mfma_f32_32x32x16_bf16 v[176:191], v[220:223], v[88:91], v[176:191]\n"
"ds_read_b64_tr_b16 v[216:217], v238 offset:18432\n"
"ds_read_b64_tr_b16 v[218:219], v238 offset:18944\n"
"buffer_load_dwordx4 v232, s[44:47], s60 offen lds\n"
"v_mfma_f32_32x32x16_bf16 v[176:191], v[224:227], v[92:95], v[176:191]\n"
"ds_read_b64_tr_b16 v[220:221], v238 offset:19456\n"
"ds_read_b64_tr_b16 v[222:223], v238 offset:19968\n"
"s_waitcnt lgkmcnt(4)\n"
"v_mfma_f32_32x32x16_bf16 v[176:191], v[208:211], v[96:99], v[176:191]\n"
"ds_read_b64_tr_b16 v[224:225], v238 offset:20480\n"
"ds_read_b64_tr_b16 v[226:227], v238 offset:20992\n"
"buffer_load_dwordx4 v233, s[44:47], s60 offen offset:1024 lds\n"
"v_mfma_f32_32x32x16_bf16 v[176:191], v[212:215], v[100:103], v[176:191]\n"
"ds_read_b64_tr_b16 v[208:209], v238 offset:21504\n"
"ds_read_b64_tr_b16 v[210:211], v238 offset:22016\n"
"s_waitcnt lgkmcnt(4)\n"
"v_mfma_f32_32x32x16_bf16 v[176:191], v[216:219], v[104:107], v[176:191]\n"
"ds_read_b64_tr_b16 v[212:213], v238 offset:22528\n"
"ds_read_b64_tr_b16 v[214:215], v238 offset:23040\n"
"buffer_load_dwordx4 v234, s[44:47], s60 offen offset:2048 lds\n"
"v_mfma_f32_32x32x16_bf16 v[176:191], v[220:223], v[108:111], v[176:191]\n"
"s_waitcnt vmcnt(9)\n"
"ds_read_b64_tr_b16 v[216:217], v238 offset:23552\n"
"ds_read_b64_tr_b16 v[218:219], v238 offset:24064\n"
"s_waitcnt lgkmcnt(4)\n"
"v_mfma_f32_32x32x16_bf16 v[176:191], v[224:227], v[112:115], v[176:191]\n"
"s_barrier\n"
"ds_read_b64_tr_b16 v[220:221], v238 offset:24576\n"
"ds_read_b64_tr_b16 v[222:223], v238 offset:25088\n"
"v_mfma_f32_32x32x16_bf16 v[176:191], v[208:211], v[116:119], v[176:191]\n"
"ds_read_b64_tr_b16 v[224:225], v238 offset:25600\n"
"ds_read_b64_tr_b16 v[226:227], v238 offset:26112\n"
"s_waitcnt lgkmcnt(4)\n"
"v_mfma_f32_32x32x16_bf16 v[176:191], v[212:215], v[120:123], v[176:191]\n"
"ds_read_b64_tr_b16 v[208:209], v238 offset:26624\n"
"ds_read_b64_tr_b16 v[210:211], v238 offset:27136\n"
"s_add_u32 m0, s55, 73728\n"
"v_mfma_f32_32x32x16_bf16 v[176:191], v[216:219], v[124:127], v[176:191]\n"
"ds_read_b64_tr_b16 v[212:213], v238 offset:27648\n"
"ds_read_b64_tr_b16 v[214:215], v238 offset:28160\n"
"s_waitcnt lgkmcnt(4)\n"
"v_mfma_f32_32x32x16_bf16 v[176:191], v[220:223], v[128:131], v[176:191]\n"
"ds_read_b64_tr_b16 v[216:217], v238 offset:28672\n"
"ds_read_b64_tr_b16 v[218:219], v238 offset:29184\n"
"s_add_u32 s17, s17, 1\n"
"s_cmp_eq_u32 s17, 9\n"
"s_cselect_b32 s17, 0, s17\n"
"s_mul_i32 s20, s17, 36864\n"
"s_add_u32 s60, s20, 0\n"
"v_mfma_f32_32x32x16_bf16 v[176:191], v[224:227], v[132:135], v[176:191]\n"
"ds_read_b64_tr_b16 v[220:221], v238 offset:29696\n"
"ds_read_b64_tr_b16 v[222:223], v238 offset:30208\n"
"s_waitcnt lgkmcnt(4)\n"
"v_mfma_f32_32x32x16_bf16 v[176:191], v[208:211], v[136:139], v[176:191]\n"
"ds_read_b64_tr_b16 v[224:225], v238 offset:30720\n"
"ds_read_b64_tr_b16 v[226:227], v238 offset:31232\n"
"buffer_load_dwordx4 v232, s[44:47], s60 offen lds\n"
"v_mfma_f32_32x32x16_bf16 v[176:191], v[212:215], v[140:143], v[176:191]\n"
"ds_read_b64_tr_b16 v[208:209], v238 offset:31744\n"
"ds_read_b64_tr_b16 v[210:211], v238 offset:32256\n"
"s_waitcnt lgkmcnt(4)\n"
"v_mfma_f32_32x32x16_bf16 v[176:191], v[216:219], v[144:147], v[176:191]\n"
"ds_read_b64_tr_b16 v[212:213], v238 offset:32768\n"
"ds_read_b64_tr_b16 v[214:215], v238 offset:33280\n"
"buffer_load_dwordx4 v233, s[44:47], s60 offen offset:1024 lds\n"
"v_mfma_f32_32x32x16_bf16 v[176:191], v[220:223], v[148:151], v[176:191]\n"
"ds_read_b64_tr_b16 v[216:217], v238 offset:33792\n"
"ds_read_b64_tr_b16 v[218:219], v238 offset:34304\n"
"s_waitcnt lgkmcnt(4)\n"
"v_mfma_f32_32x32x16_bf16 v[176:191], v[224:227], v[152:155], v[176:191]\n"
"ds_read_b64_tr_b16 v[220:221], v238 offset:34816\n"
"ds_read_b64_tr_b16 v[222:223], v238 offset:35328\n"
"buffer_load_dwordx4 v234, s[44:47], s60 offen offset:2048 lds\n"
"v_mfma_f32_32x32x16_bf16 v[176:191], v[208:211], v[156:159], v[176:191]\n"
"s_waitcnt vmcnt(9)\n"
"ds_read_b64_tr_b16 v[224:225], v238 offset:35840\n"
"ds_read_b64_tr_b16 v[226:227], v238 offset:36352\n"
"s_waitcnt lgkmcnt(4)\n"
"v_mfma_f32_32x32x16_bf16 v[176:191], v[212:215], v[160:163], v[176:191]\n"
"s_barrier\n"
"ds_read_b64_tr_b16 v[208:209], v242\n"
"ds_read_b64_tr_b16 v[210:211], v242 offset:3072\n"
"v_mfma_f32_32x32x16_bf16 v[176:191], v[216:219], v[164:167], v[176:191]\n"
"ds_read_b64_tr_b16 v[212:213], v242 offset:6144\n"
"ds_read_b64_tr_b16 v[214:215], v242 offset:9216\n"
"s_waitcnt lgkmcnt(4)\n"
"v_mfma_f32_32x32x16_bf16 v[176:191], v[220:223], v[168:171], v[176:191]\n"
"ds_read_b64_tr_b16 v[216:217], v243 offset:64\n"
"ds_read_b64_tr_b16 v[218:219], v243 offset:3136\n"
"s_add_u32 m0, s55, 86016\n"
"v_mfma_f32_32x32x16_bf16 v[176:191], v[224:227], v[172:175], v[176:191]\n"
"ds_read_b64_tr_b16 v[220:221], v243 offset:6208\n"
"ds_read_b64_tr_b16 v[222:223], v243 offset:9280\n"
"s_waitcnt lgkmcnt(4)\n"
"v_mfma_f32_32x32x16_bf16 a[0:15], v[208:211], v[200:203], a[0:15]\n"
"ds_read_b64_tr_b16 v[224:225], v242 offset:128\n"
"ds_read_b64_tr_b16 v[226:227], v242 offset:3200\n"
"s_add_u32 s60, s20, 384\n"
"v_mfma_f32_32x32x16_bf16 a[0:15], v[212:215], v[204:207], a[0:15]\n"
"ds_read_b64_tr_b16 v[208:209], v242 offset:6272\n"
"ds_read_b64_tr_b16 v[210:211], v242 offset:9344\n"
"s_waitcnt lgkmcnt(4)\n"
"v_mfma_f32_32x32x16_bf16 a[16:31], v[216:219], v[200:203], a[16:31]\n"
"ds_read_b64_tr_b16 v[212:213], v243 offset:192\n"
"ds_read_b64_tr_b16 v[214:215], v243 offset:3264\n"
"buffer_load_dwordx4 v232, s[44:47], s60 offen lds\n"
"v_mfma_f32_32x32x16_bf16 a[16:31], v[220:223], v[204:207], a[16:31]\n"
"ds_read_b64_tr_b16 v[216:217], v243 offset:6336\n"
"ds_read_b64_tr_b16 v[218:219], v243 offset:9408\n"
"v_max_f32 v176, 0, v176\n"
"v_max_f32_e64 v177, 0, v177\n"
"v_max_f32 v178, 0, v178\n"
"s_waitcnt lgkmcnt(4)\n"
"v_mfma_f32_32x32x16_bf16 a[32:47], v[224:227], v[200:203], a[32:47]\n"
"ds_read_b64_tr_b16 v[220:221], v242 offset:256\n"
"ds_read_b64_tr_b16 v[222:223], v242 offset:3328\n"
"buffer_load_dwordx4 v233, s[44:47], s60 offen offset:1024 lds\n"
"v_max_f32 v179, 0, v179\n"
"v_mul_f32 v176, v176, v176\n"
"v_mfma_f32_32x32x16_bf16 a[32:47], v[208:211], v[204:207], a[32:47]\n"
"ds_read_b64_tr_b16 v[224:225], v242 offset:6400\n"
"ds_read_b64_tr_b16 v[226:227], v242 offset:9472\n"
"v_mul_f32 v177, v177, v177\n"
"v_max_f32 v180, 0, v180\n"
"v_max_f32 v181, 0, v181\n"
"s_waitcnt lgkmcnt(4)\n"
"v_mfma_f32_32x32x16_bf16 a[48:63], v[212:215], v[200:203], a[48:63]\n"
"ds_read_b64_tr_b16 v[208:209], v243 offset:320\n"
"ds_read_b64_tr_b16 v[210:211], v243 offset:3392\n"
"buffer_load_dwordx4 v234, s[44:47], s60 offen offset:2048 lds\n"
"v_mul_f32 v178, v178, v178\n"
"s_waitcnt vmcnt(9)\n"
"v_mul_f32_e64 v179, v179, v179\n"
"v_mfma_f32_32x32x16_bf16 a[48:63], v[216:219], v[204:207], a[48:63]\n"
"ds_read_b64_tr_b16 v[212:213], v243 offset:6464\n"
"ds_read_b64_tr_b16 v[214:215], v243 offset:9536\n"
"v_cvt_pk_bf16_f32 v192, v176, v177\n"
"v_max_f32 v182, 0, v182\n"
"s_waitcnt lgkmcnt(4)\n"
"v_mfma_f32_32x32x16_bf16 a[64:79], v[220:223], v[200:203], a[64:79]\n"
"s_barrier\n"
"ds_read_b64_tr_b16 v[216:217], v242 offset:12288\n"
"ds_read_b64_tr_b16 v[218:219], v242 offset:15360\n"
"v_max_f32 v183, 0, v183\n"
"v_mul_f32_e64 v180, v180, v180\n"
"v_mfma_f32_32x32x16_bf16 a[64:79], v[224:227], v[204:207], a[64:79]\n"
"ds_read_b64_tr_b16 v[220:221], v242 offset:18432\n"
"ds_read_b64_tr_b16 v[222:223], v242 offset:21504\n"
"v_mul_f32_e64 v181, v181, v181\n"
"v_cvt_pk_bf16_f32 v193, v178, v179\n"
"v_max_f32 v184, 0, v184\n"
"s_waitcnt lgkmcnt(4)\n"
"v_mfma_f32_32x32x16_bf16 a[80:95], v[208:211], v[200:203], a[80:95]\n"
"ds_read_b64_tr_b16 v[224:225], v243 offset:12352\n"
"ds_read_b64_tr_b16 v[226:227], v243 offset:15424\n"
"s_add_u32 m0, s55, 98304\n"
"v_max_f32 v185, 0, v185\n"
"v_mul_f32 v182, v182, v182\n"
"v_mfma_f32_32x32x16_bf16 a[80:95], v[212:215], v[204:207], a[80:95]\n"
"ds_read_b64_tr_b16 v[208:209], v243 offset:18496\n"
"ds_read_b64_tr_b16 v[210:211], v243 offset:21568\n"
"v_mul_f32_e64 v183, v183, v183\n"
"v_cvt_pk_bf16_f32 v194, v180, v181\n"
"v_max_f32 v186, 0, v186\n"
"s_waitcnt lgkmcnt(4)\n"
"v_mfma_f32_32x32x16_bf16 a[96:111], v[216:219], v[200:203], a[96:111]\n"
"ds_read_b64_tr_b16 v[212:213], v242 offset:12416\n"
"ds_read_b64_tr_b16 v[214:215], v242 offset:15488\n"
"s_add_u32 s60, s20, 768\n"
"v_max_f32 v187, 0, v187\n"
"v_mul_f32 v184, v184, v184\n"
"v_mfma_f32_32x32x16_bf16 a[96:111], v[220:223], v[204:207], a[96:111]\n"
"ds_read_b64_tr_b16 v[216:217], v242 offset:18560\n"
"ds_read_b64_tr_b16 v[218:219], v242 offset:21632\n"
"v_mul_f32_e64 v185, v185, v185\n"
"v_cvt_pk_bf16_f32 v195, v182, v183\n"
"v_max_f32 v188, 0, v188\n"
"s_waitcnt lgkmcnt(4)\n"
"v_mfma_f32_32x32x16_bf16 a[112:127], v[224:227], v[200:203], a[112:127]\n"
"ds_read_b64_tr_b16 v[220:221], v243 offset:12480\n"
"ds_read_b64_tr_b16 v[222:223], v243 offset:15552\n"
"buffer_load_dwordx4 v232, s[44:47], s60 offen lds\n"
"v_max_f32 v189, 0, v189\n"
"v_mul_f32 v186, v186, v186\n"
"v_mfma_f32_32x32x16_bf16 a[112:127], v[208:211], v[204:207], a[112:127]\n"
"ds_read_b64_tr_b16 v[224:225], v243 offset:18624\n"
"ds_read_b64_tr_b16 v[226:227], v243 offset:21696\n"
"v_mul_f32_e64 v187, v187, v187\n"
"v_cvt_pk_bf16_f32 v196, v184, v185\n"
"v_max_f32 v190, 0, v190\n"
"s_waitcnt lgkmcnt(4)\n"
"v_mfma_f32_32x32x16_bf16 a[128:143], v[212:215], v[200:203], a[128:143]\n"
"ds_read_b64_tr_b16 v[208:209], v242 offset:12544\n"
"ds_read_b64_tr_b16 v[210:211], v242 offset:15616\n"
"buffer_load_dwordx4 v233, s[44:47], s60 offen offset:1024 lds\n"
"v_max_f32 v191, 0, v191\n"
"v_mul_f32 v188, v188, v188\n"
"v_mfma_f32_32x32x16_bf16 a[128:143], v[216:219], v[204:207], a[128:143]\n"
"ds_read_b64_tr_b16 v[212:213], v242 offset:18688\n"
"ds_read_b64_tr_b16 v[214:215], v242 offset:21760\n"
"v_mul_f32_e64 v189, v189, v189\n"
"v_cvt_pk_bf16_f32 v197, v186, v187\n"
"v_mul_f32 v190, v190, v190\n"
"s_waitcnt lgkmcnt(4)\n"
"v_mfma_f32_32x32x16_bf16 a[144:159], v[220:223], v[200:203], a[144:159]\n"
"ds_read_b64_tr_b16 v[216:217], v243 offset:12608\n"
"ds_read_b64_tr_b16 v[218:219], v243 offset:15680\n"
"buffer_load_dwordx4 v234, s[44:47], s60 offen offset:2048 lds\n"
"v_mul_f32_e64 v191, v191, v191\n"
"v_cvt_pk_bf16_f32 v198, v188, v189\n"
"v_mfma_f32_32x32x16_bf16 a[144:159], v[224:227], v[204:207], a[144:159]\n"
"s_waitcnt vmcnt(9)\n"
"ds_read_b64_tr_b16 v[220:221], v243 offset:18752\n"
"ds_read_b64_tr_b16 v[222:223], v243 offset:21824\n"
"v_cvt_pk_bf16_f32 v199, v190, v191\n"
"s_waitcnt lgkmcnt(4)\n"
"v_mfma_f32_32x32x16_bf16 a[160:175], v[208:211], v[200:203], a[160:175]\n"
"s_barrier\n"
"ds_read_b64_tr_b16 v[224:225], v242 offset:24576\n"
"ds_read_b64_tr_b16 v[226:227], v242 offset:27648\n"
"v_mfma_f32_32x32x16_bf16 a[160:175], v[212:215], v[204:207], a[160:175]\n"
"ds_read_b64_tr_b16 v[208:209], v242 offset:30720\n"
"ds_read_b64_tr_b16 v[210:211], v242 offset:33792\n"
"s_waitcnt lgkmcnt(4)\n"
"v_mfma_f32_32x32x16_bf16 a[176:191], v[216:219], v[200:203], a[176:191]\n"
"ds_read_b64_tr_b16 v[212:213], v243 offset:24640\n"
"ds_read_b64_tr_b16 v[214:215], v243 offset:27712\n"
"v_mfma_f32_32x32x16_bf16 a[176:191], v[220:223], v[204:207], a[176:191]\n"
"ds_read_b64_tr_b16 v[216:217], v243 offset:30784\n"
"ds_read_b64_tr_b16 v[218:219], v243 offset:33856\n"
"s_waitcnt lgkmcnt(4)\n"
"v_mfma_f32_32x32x16_bf16 a[192:207], v[224:227], v[200:203], a[192:207]\n"
"ds_read_b64_tr_b16 v[220:221], v242 offset:24704\n"
"ds_read_b64_tr_b16 v[222:223], v242 offset:27776\n"
"v_mfma_f32_32x32x16_bf16 a[192:207], v[208:211], v[204:207], a[192:207]\n"
"ds_read_b64_tr_b16 v[224:225], v242 offset:30848\n"
"ds_read_b64_tr_b16 v[226:227], v242 offset:33920\n"
"s_waitcnt lgkmcnt(4)\n"
"v_mfma_f32_32x32x16_bf16 a[208:223], v[212:215], v[200:203], a[208:223]\n"
"ds_read_b64_tr_b16 v[208:209], v243 offset:24768\n"
"ds_read_b64_tr_b16 v[210:211], v243 offset:27840\n"
"v_mfma_f32_32x32x16_bf16 a[208:223], v[216:219], v[204:207], a[208:223]\n"
"ds_read_b64_tr_b16 v[212:213], v243 offset:30912\n"
"ds_read_b64_tr_b16 v[214:215], v243 offset:33984\n"
"s_waitcnt lgkmcnt(4)\n"
"v_mfma_f32_32x32x16_bf16 a[224:239], v[220:223], v[200:203], a[224:239]\n"
"ds_read_b64_tr_b16 v[216:217], v242 offset:24832\n"
"ds_read_b64_tr_b16 v[218:219], v242 offset:27904\n"
"v_mfma_f32_32x32x16_bf16 a[224:239], v[224:227], v[204:207], a[224:239]\n"
"ds_read_b64_tr_b16 v[220:221], v242 offset:30976\n"
"ds_read_b64_tr_b16 v[222:223], v242 offset:34048\n"
"s_waitcnt lgkmcnt(4)\n"
"v_mfma_f32_32x32x16_bf16 a[240:255], v[208:211], v[200:203], a[240:255]\n"
"ds_read_b64_tr_b16 v[224:225], v243 offset:24896\n"
"ds_read_b64_tr_b16 v[226:227], v243 offset:27968\n"
"v_mfma_f32_32x32x16_bf16 a[240:255], v[212:215], v[204:207], a[240:255]\n"
"s_waitcnt vmcnt(6)\n"
"ds_read_b64_tr_b16 v[208:209], v243 offset:31040\n"
"ds_read_b64_tr_b16 v[210:211], v243 offset:34112\n"
"s_waitcnt lgkmcnt(4)\n"
"v_mfma_f32_32x32x16_bf16 v[0:15], v[216:219], v[200:203], v[0:15]\n"
"s_barrier\n"
"ds_read_b64_tr_b16 v[212:213], v240\n"
"ds_read_b64_tr_b16 v[214:215], v240 offset:3072\n"
"v_mfma_f32_32x32x16_bf16 v[0:15], v[220:223], v[204:207], v[0:15]\n"
"ds_read_b64_tr_b16 v[216:217], v240 offset:6144\n"
"ds_read_b64_tr_b16 v[218:219], v240 offset:9216\n"
"s_waitcnt lgkmcnt(4)\n"
"v_mfma_f32_32x32x16_bf16 v[16:31], v[224:227], v[200:203], v[16:31]\n"
"ds_read_b64_tr_b16 v[220:221], v241 offset:64\n"
"ds_read_b64_tr_b16 v[222:223], v241 offset:3136\n"
"v_mfma_f32_32x32x16_bf16 v[16:31], v[208:211], v[204:207], v[16:31]\n"
"ds_read_b64_tr_b16 v[224:225], v241 offset:6208\n"
"ds_read_b64_tr_b16 v[226:227], v241 offset:9280\n"
"s_waitcnt lgkmcnt(4)\n"
"v_mfma_f32_32x32x16_bf16 a[0:15], v[212:215], v[192:195], a[0:15]\n"
"ds_read_b64_tr_b16 v[208:209], v240 offset:128\n"
"ds_read_b64_tr_b16 v[210:211], v240 offset:3200\n"
"v_mfma_f32_32x32x16_bf16 a[0:15], v[216:219], v[196:199], a[0:15]\n"
"ds_read_b64_tr_b16 v[212:213], v240 offset:6272\n"
"ds_read_b64_tr_b16 v[214:215], v240 offset:9344\n"
"s_lshl_b32 s56, s71, 6\n"
"s_lshl_b32 s57, s72, 3\n"
"s_add_u32 s56, s56, s57\n"
"s_lshl_b32 s57, s52, 1\n"
"s_add_u32 s56, s56, s57\n"
"s_lshl_b32 s57, s56, 10\n"
"s_add_u32 s61, s57, 4997120\n"
"s_waitcnt lgkmcnt(4)\n"
"v_mfma_f32_32x32x16_bf16 a[16:31], v[220:223], v[192:195], a[16:31]\n"
"ds_read_b64_tr_b16 v[216:217], v241 offset:192\n"
"ds_read_b64_tr_b16 v[218:219], v241 offset:3264\n"
"buffer_load_dwordx4 v[80:83], v235, s[48:51], s61 offen sc1\n"
"buffer_load_dwordx4 v[84:87], v235, s[48:51], s61 offen offset:1024 sc1\n"
"s_lshl_b32 s57, s56, 7\n"
"s_add_u32 s61, s57, 7094272\n"
"v_mfma_f32_32x32x16_bf16 a[16:31], v[224:227], v[196:199], a[16:31]\n"
"ds_read_b64_tr_b16 v[220:221], v241 offset:6336\n"
"ds_read_b64_tr_b16 v[222:223], v241 offset:9408\n"
"v_and_b32 v171, 31, v228\n"
"v_lshlrev_b32_e64 v171, 2, v171\n"
"buffer_load_dword v172, v171, s[48:51], s61 offen sc1\n"
"buffer_load_dword v173, v171, s[48:51], s61 offen offset:128 sc1\n"
"s_waitcnt lgkmcnt(4)\n"
"v_mfma_f32_32x32x16_bf16 a[32:47], v[208:211], v[192:195], a[32:47]\n"
"ds_read_b64_tr_b16 v[224:225], v240 offset:256\n"
"ds_read_b64_tr_b16 v[226:227], v240 offset:3328\n"
"v_and_b32 v174, 31, v228\n"
"v_lshrrev_b32 v175, 5, v228\n"
"v_lshlrev_b32 v168, 4, v228\n"
"v_mfma_f32_32x32x16_bf16 a[32:47], v[212:215], v[196:199], a[32:47]\n"
"ds_read_b64_tr_b16 v[208:209], v240 offset:6400\n"
"ds_read_b64_tr_b16 v[210:211], v240 offset:9472\n"
"v_lshlrev_b32_e64 v169, 4, v174\n"
"v_add_u32 v169, 147456, v169\n"
"v_min_u32 v170, 19, v174\n"
"s_waitcnt lgkmcnt(4)\n"
"v_mfma_f32_32x32x16_bf16 a[48:63], v[216:219], v[192:195], a[48:63]\n"
"ds_read_b64_tr_b16 v[212:213], v241 offset:320\n"
"ds_read_b64_tr_b16 v[214:215], v241 offset:3392\n"
"v_lshlrev_b32_e64 v170, 4, v170\n"
"v_mul_u32_u24 v175, 320, v175\n"
"v_add_u32_e64 v170, v170, v175\n"
"v_mfma_f32_32x32x16_bf16 a[48:63], v[220:223], v[196:199], a[48:63]\n"
"s_waitcnt vmcnt(7)\n"
"ds_read_b64_tr_b16 v[216:217], v241 offset:6464\n"
"ds_read_b64_tr_b16 v[218:219], v241 offset:9536\n"
"v_add_u32 v170, 1024, v170\n"
"v_mov_b32 v88, 0\n"
"s_waitcnt lgkmcnt(4)\n"
"v_mfma_f32_32x32x16_bf16 a[64:79], v[224:227], v[192:195], a[64:79]\n"
"s_barrier\n"
"ds_read_b64_tr_b16 v[220:221], v240 offset:12288\n"
"ds_read_b64_tr_b16 v[222:223], v240 offset:15360\n"
"v_mov_b32 v89, 0\n"
"v_mov_b32 v90, 0\n"
"v_mfma_f32_32x32x16_bf16 a[64:79], v[208:211], v[196:199], a[64:79]\n"
"ds_read_b64_tr_b16 v[224:225], v240 offset:18432\n"
"ds_read_b64_tr_b16 v[226:227], v240 offset:21504\n"
"v_mov_b32 v91, 0\n"
"v_mov_b32 v92, 0\n"
"v_mov_b32 v93, 0\n"
"s_waitcnt lgkmcnt(4)\n"
"v_mfma_f32_32x32x16_bf16 a[80:95], v[212:215], v[192:195], a[80:95]\n"
"ds_read_b64_tr_b16 v[208:209], v241 offset:12352\n"
"ds_read_b64_tr_b16 v[210:211], v241 offset:15424\n"
"v_mov_b32_e64 v94, 0\n"
"v_mov_b32 v95, 0\n"
"v_mov_b32 v96, 0\n"
"v_mfma_f32_32x32x16_bf16 a[80:95], v[216:219], v[196:199], a[80:95]\n"
"ds_read_b64_tr_b16 v[212:213], v241 offset:18496\n"
"ds_read_b64_tr_b16 v[214:215], v241 offset:21568\n"
"v_mov_b32 v97, 0\n"
"v_mov_b32 v98, 0\n"
"v_mov_b32 v99, 0\n"
"s_waitcnt lgkmcnt(4)\n"
"v_mfma_f32_32x32x16_bf16 a[96:111], v[220:223], v[192:195], a[96:111]\n"
"ds_read_b64_tr_b16 v[216:217], v240 offset:12416\n"
"ds_read_b64_tr_b16 v[218:219], v240 offset:15488\n"
"v_mov_b32_e64 v100, 0\n"
"v_mov_b32 v101, 0\n"
"v_mov_b32 v102, 0\n"
"v_mfma_f32_32x32x16_bf16 a[96:111], v[224:227], v[196:199], a[96:111]\n"
"ds_read_b64_tr_b16 v[220:221], v240 offset:18560\n"
"ds_read_b64_tr_b16 v[222:223], v240 offset:21632\n"
"v_mov_b32 v103, 0\n"
"v_mov_b32 v104, 0\n"
"v_mov_b32 v105, 0\n"
"s_waitcnt lgkmcnt(4)\n"
"v_mfma_f32_32x32x16_bf16 a[112:127], v[208:211], v[192:195], a[112:127]\n"
"ds_read_b64_tr_b16 v[224:225], v241 offset:12480\n"
"ds_read_b64_tr_b16 v[226:227], v241 offset:15552\n"
"v_mov_b32_e64 v106, 0\n"
"v_mov_b32 v107, 0\n"
"v_mov_b32 v108, 0\n"
"v_mfma_f32_32x32x16_bf16 a[112:127], v[212:215], v[196:199], a[112:127]\n"
"ds_read_b64_tr_b16 v[208:209], v241 offset:18624\n"
"ds_read_b64_tr_b16 v[210:211], v241 offset:21696\n"
"v_mov_b32 v109, 0\n"
"v_mov_b32 v110, 0\n"
"v_mov_b32 v111, 0\n"
"s_waitcnt lgkmcnt(4)\n"
"v_mfma_f32_32x32x16_bf16 a[128:143], v[216:219], v[192:195], a[128:143]\n"
"ds_read_b64_tr_b16 v[212:213], v240 offset:12544\n"
"ds_read_b64_tr_b16 v[214:215], v240 offset:15616\n"
"v_mov_b32_e64 v112, 0\n"
"v_mov_b32 v113, 0\n"
"v_mov_b32 v114, 0\n"
"v_mfma_f32_32x32x16_bf16 a[128:143], v[220:223], v[196:199], a[128:143]\n"
"ds_read_b64_tr_b16 v[216:217], v240 offset:18688\n"
"ds_read_b64_tr_b16 v[218:219], v240 offset:21760\n"
"v_mov_b32 v115, 0\n"
"v_mov_b32 v116, 0\n"
"v_mov_b32 v117, 0\n"
"s_waitcnt lgkmcnt(4)\n"
"v_mfma_f32_32x32x16_bf16 a[144:159], v[224:227], v[192:195], a[144:159]\n"
"ds_read_b64_tr_b16 v[220:221], v241 offset:12608\n"
"ds_read_b64_tr_b16 v[222:223], v241 offset:15680\n"
"v_mov_b32_e64 v118, 0\n"
"v_mov_b32 v119, 0\n"
"s_waitcnt vmcnt(4)\n"
"v_mov_b32_e64 v136, 0\n"
"v_mfma_f32_32x32x16_bf16 a[144:159], v[208:211], v[196:199], a[144:159]\n"
"ds_read_b64_tr_b16 v[224:225], v241 offset:18752\n"
"ds_read_b64_tr_b16 v[226:227], v241 offset:21824\n"
"v_mov_b32_e64 v137, 0\n"
"v_mov_b32 v138, 0\n"
"s_waitcnt lgkmcnt(4)\n"
"v_mfma_f32_32x32x16_bf16 a[160:175], v[212:215], v[192:195], a[160:175]\n"
"s_barrier\n"
"ds_read_b64_tr_b16 v[208:209], v240 offset:24576\n"
"ds_read_b64_tr_b16 v[210:211], v240 offset:27648\n"
"v_mov_b32 v139, 0\n"
"v_mov_b32_e64 v140, 0\n"
"v_mfma_f32_32x32x16_bf16 a[160:175], v[216:219], v[196:199], a[160:175]\n"
"ds_read_b64_tr_b16 v[212:213], v240 offset:30720\n"
"ds_read_b64_tr_b16 v[214:215], v240 offset:33792\n"
"v_mov_b32 v141, 0\n"
"v_mov_b32 v142, 0\n"
"v_mov_b32 v143, 0\n"
"s_waitcnt lgkmcnt(4)\n"
"v_mfma_f32_32x32x16_bf16 a[176:191], v[220:223], v[192:195], a[176:191]\n"
"ds_read_b64_tr_b16 v[216:217], v241 offset:24640\n"
"ds_read_b64_tr_b16 v[218:219], v241 offset:27712\n"
"v_mov_b32_e64 v144, 0\n"
"v_mov_b32 v145, 0\n"
"v_mov_b32 v146, 0\n"
"v_mfma_f32_32x32x16_bf16 a[176:191], v[224:227], v[196:199], a[176:191]\n"
"ds_read_b64_tr_b16 v[220:221], v241 offset:30784\n"
"ds_read_b64_tr_b16 v[222:223], v241 offset:33856\n"
"v_mov_b32 v147, 0\n"
"v_mov_b32 v148, 0\n"
"v_mov_b32 v149, 0\n"
"s_waitcnt lgkmcnt(4)\n"
"v_mfma_f32_32x32x16_bf16 a[192:207], v[208:211], v[192:195], a[192:207]\n"
"ds_read_b64_tr_b16 v[224:225], v240 offset:24704\n"
"ds_read_b64_tr_b16 v[226:227], v240 offset:27776\n"
"v_mov_b32_e64 v150, 0\n"
"v_mov_b32 v151, 0\n"
"v_mov_b32 v152, 0\n"
"v_mfma_f32_32x32x16_bf16 a[192:207], v[212:215], v[196:199], a[192:207]\n"
"ds_read_b64_tr_b16 v[208:209], v240 offset:30848\n"
"ds_read_b64_tr_b16 v[210:211], v240 offset:33920\n"
"v_mov_b32 v153, 0\n"
"v_mov_b32 v154, 0\n"
"v_mov_b32 v155, 0\n"
"s_waitcnt lgkmcnt(4)\n"
"v_mfma_f32_32x32x16_bf16 a[208:223], v[216:219], v[192:195], a[208:223]\n"
"ds_read_b64_tr_b16 v[212:213], v241 offset:24768\n"
"ds_read_b64_tr_b16 v[214:215], v241 offset:27840\n"
"v_mov_b32_e64 v156, 0\n"
"v_mov_b32 v157, 0\n"
"v_mov_b32 v158, 0\n"
"v_mfma_f32_32x32x16_bf16 a[208:223], v[220:223], v[196:199], a[208:223]\n"
"ds_read_b64_tr_b16 v[216:217], v241 offset:30912\n"
"ds_read_b64_tr_b16 v[218:219], v241 offset:33984\n"
"v_mov_b32 v159, 0\n"
"v_mov_b32 v160, 0\n"
"v_mov_b32 v161, 0\n"
"s_waitcnt lgkmcnt(4)\n"
"v_mfma_f32_32x32x16_bf16 a[224:239], v[224:227], v[192:195], a[224:239]\n"
"ds_read_b64_tr_b16 v[220:221], v240 offset:24832\n"
"ds_read_b64_tr_b16 v[222:223], v240 offset:27904\n"
"v_mov_b32_e64 v162, 0\n"
"v_mov_b32 v163, 0\n"
"v_mov_b32 v164, 0\n"
"v_mfma_f32_32x32x16_bf16 a[224:239], v[208:211], v[196:199], a[224:239]\n"
"ds_read_b64_tr_b16 v[224:225], v240 offset:30976\n"
"ds_read_b64_tr_b16 v[226:227], v240 offset:34048\n"
"v_mov_b32 v165, 0\n"
"v_mov_b32 v166, 0\n"
"v_mov_b32 v167, 0\n"
"s_waitcnt lgkmcnt(4)\n"
"v_mfma_f32_32x32x16_bf16 a[240:255], v[212:215], v[192:195], a[240:255]\n"
"ds_read_b64_tr_b16 v[208:209], v241 offset:24896\n"
"ds_read_b64_tr_b16 v[210:211], v241 offset:27968\n"
"v_mov_b32_e64 v72, 0\n"
"v_mov_b32 v73, 0\n"
"v_mov_b32 v74, 0\n"
"v_mfma_f32_32x32x16_bf16 a[240:255], v[216:219], v[196:199], a[240:255]\n"
"ds_read_b64_tr_b16 v[212:213], v241 offset:31040\n"
"ds_read_b64_tr_b16 v[214:215], v241 offset:34112\n"
"v_mov_b32 v75, 0\n"
"v_mov_b32 v76, 0\n"
"v_mov_b32 v77, 0\n"
"s_waitcnt lgkmcnt(4)\n"
"v_mfma_f32_32x32x16_bf16 v[0:15], v[220:223], v[192:195], v[0:15]\n"
"v_mov_b32_e64 v78, 0\n"
"v_mov_b32 v79, 0\n"
"v_mov_b32 v128, 0\n"
"v_mfma_f32_32x32x16_bf16 v[0:15], v[224:227], v[196:199], v[0:15]\n"
"v_mov_b32 v129, 0\n"
"v_mov_b32 v130, 0\n"
"v_mov_b32 v131, 0\n"
"s_waitcnt lgkmcnt(0)\n"
"v_mfma_f32_32x32x16_bf16 v[16:31], v[208:211], v[192:195], v[16:31]\n"
"v_mov_b32_e64 v176, 0\n"
"v_mov_b32 v177, 0\n"
"v_mov_b32 v178, 0\n"
"v_mfma_f32_32x32x16_bf16 v[16:31], v[212:215], v[196:199], v[16:31]\n"
"v_mov_b32 v179, 0\n"
"s_barrier\n"
"v_cmp_gt_u32 vcc, 32, v228\n"
"s_nop 1\n"
"s_waitcnt vmcnt(0)\n"
"v_cndmask_b32 v88, 0, v172\n"
"v_cndmask_b32 v104, 0, v173\n"
"v_cndmask_b32 v93, 0, v172\n"
"v_cndmask_b32 v109, 0, v173\n"
"v_cndmask_b32 v98, 0, v172\n"
"v_cndmask_b32 v114, 0, v173\n"
"v_cndmask_b32 v103, 0, v172\n"
"v_cndmask_b32 v119, 0, v173\n"
"s_barrier\n"
"ds_read_b128 v[120:123], v168\n"
"ds_read_b128 v[124:127], v169\n"
"s_waitcnt lgkmcnt(1)\n"
"v_mfma_f32_32x32x16_bf16 v[32:47], v[120:123], v[80:83], 0\n"
"s_waitcnt lgkmcnt(0)\n"
"v_mfma_f32_32x32x16_bf16 v[32:47], v[124:127], v[88:91], v[32:47]\n"
"s_nop 10\n"
"s_mul_i32 s69, s52, 18432\n"
"s_mul_i32 s70, s71, 147456\n"
"s_add_u32 s70, s70, s69\n"
"s_lshl_b32 s56, s53, 4\n"
"s_add_u32 s56, s56, s54\n"
"s_lshl_b32 s56, s56, 2\n"
"s_add_u32 s56, s56, s52\n"
"s_mul_i32 s56, s56, 36864\n"
"s_add_u32 s67, s56, 9715712\n"
"v_mfma_f32_32x32x16_bf16 v[152:167], v[128:131], v[72:75], v[152:167]\n"
"ds_read_b128 v[128:131], v170\n"
"ds_read_b128 v[132:135], v170 offset:640\n"
"s_add_u32 m0, s69, 73728\n"
"s_add_u32 s61, s70, 90112\n"
"buffer_load_dwordx4 v235, s[48:51], s61 offen sc1 lds\n"
"buffer_load_dwordx4 v235, s[48:51], s61 offen offset:1024 sc1 lds\n"
"v_exp_f32 v32, v32\n"
"v_exp_f32 v33, v33\n"
"v_exp_f32 v34, v34\n"
"v_exp_f32 v35, v35\n"
"v_cvt_pk_bf16_f32 v64, v32, v33\n"
"v_cvt_pk_bf16_f32 v65, v34, v35\n"
"v_mfma_f32_32x32x16_bf16 v[48:63], v[120:123], v[84:87], 0\n"
"s_add_u32 m0, s69, 75776\n"
"s_add_u32 s61, s70, 92160\n"
"buffer_load_dwordx4 v235, s[48:51], s61 offen sc1 lds\n"
"v_exp_f32 v36, v36\n"
"v_exp_f32 v37, v37\n"
"v_exp_f32 v38, v38\n"
"v_exp_f32 v39, v39\n"
"v_cvt_pk_bf16_f32 v66, v36, v37\n"
"v_cvt_pk_bf16_f32 v67, v38, v39\n"
"v_mfma_f32_32x32x16_bf16 v[48:63], v[124:127], v[104:107], v[48:63]\n"
"ds_read_b128 v[120:123], v168 offset:2304\n"
"buffer_load_dwordx4 v235, s[48:51], s61 offen offset:1024 sc1 lds\n"
"s_add_u32 m0, s69, 77824\n"
"s_add_u32 s61, s70, 94208\n"
"v_exp_f32 v40, v40\n"
"v_exp_f32 v41, v41\n"
"v_exp_f32 v42, v42\n"
"v_exp_f32 v43, v43\n"
"v_cvt_pk_bf16_f32 v68, v40, v41\n"
"v_cvt_pk_bf16_f32 v69, v42, v43\n"
"v_mfma_f32_32x32x16_bf16 v[152:167], v[176:179], v[76:79], v[152:167]\n"
"buffer_load_dwordx4 v235, s[48:51], s61 offen sc1 lds\n"
"buffer_load_dwordx4 v235, s[48:51], s61 offen offset:1024 sc1 lds\n"
"s_add_u32 m0, s69, 79872\n"
"v_exp_f32 v44, v44\n"
"v_exp_f32 v45, v45\n"
"v_exp_f32 v46, v46\n"
"s_waitcnt lgkmcnt(0)\n"
"v_exp_f32_e64 v47, v47\n"
"v_cvt_pk_bf16_f32 v70, v44, v45\n"
"v_cvt_pk_bf16_f32 v71, v46, v47\n"
"v_mfma_f32_32x32x16_bf16 v[136:151], v[128:131], v[64:67], v[136:151]\n"
"s_add_u32 s61, s70, 96256\n"
"buffer_load_dwordx4 v235, s[48:51], s61 offen sc1 lds\n"
"buffer_load_dwordx4 v235, s[48:51], s61 offen offset:1024 sc1 lds\n"
"v_exp_f32 v48, v48\n"
"v_exp_f32 v49, v49\n"
"v_exp_f32 v50, v50\n"
"v_exp_f32 v51, v51\n"
"v_cvt_pk_bf16_f32 v72, v48, v49\n"
"v_cvt_pk_bf16_f32 v73, v50, v51\n"
"v_mfma_f32_32x32x16_bf16 v[32:47], v[120:123], v[80:83], 0\n"
"s_add_u32 m0, s69, 81920\n"
"s_add_u32 s61, s70, 98304\n"
"buffer_load_dwordx4 v235, s[48:51], s61 offen sc1 lds\n"
"v_exp_f32 v52, v52\n"
"v_exp_f32 v53, v53\n"
"v_exp_f32 v54, v54\n"
"v_exp_f32 v55, v55\n"
"v_cvt_pk_bf16_f32 v74, v52, v53\n"
"v_cvt_pk_bf16_f32 v75, v54, v55\n"
"v_mfma_f32_32x32x16_bf16 v[32:47], v[124:127], v[92:95], v[32:47]\n"
"buffer_load_dwordx4 v235, s[48:51], s61 offen offset:1024 sc1 lds\n"
"s_add_u32 m0, s69, 83968\n"
"s_add_u32 s61, s70, 100352\n"
"buffer_load_dwordx4 v235, s[48:51], s61 offen sc1 lds\n"
"v_exp_f32 v56, v56\n"
"v_exp_f32 v57, v57\n"
"v_exp_f32 v58, v58\n"
"v_exp_f32 v59, v59\n"
"v_cvt_pk_bf16_f32 v76, v56, v57\n"
"v_cvt_pk_bf16_f32 v77, v58, v59\n"
"v_mfma_f32_32x32x16_bf16 v[136:151], v[132:135], v[68:71], v[136:151]\n"
"buffer_load_dwordx4 v235, s[48:51], s61 offen offset:1024 sc1 lds\n"
"s_add_u32 m0, s69, 86016\n"
"s_add_u32 s61, s70, 102400\n"
"v_exp_f32 v60, v60\n"
"v_exp_f32 v61, v61\n"
"v_exp_f32 v62, v62\n"
"v_exp_f32 v63, v63\n"
"v_cvt_pk_bf16_f32 v78, v60, v61\n"
"v_cvt_pk_bf16_f32 v79, v62, v63\n"
"v_mfma_f32_32x32x16_bf16 v[152:167], v[128:131], v[72:75], v[152:167]\n"
"ds_read_b128 v[128:131], v170 offset:2304\n"
"ds_read_b128 v[176:179], v170 offset:2944\n"
"buffer_load_dwordx4 v235, s[48:51], s61 offen sc1 lds\n"
"buffer_load_dwordx4 v235, s[48:51], s61 offen offset:1024 sc1 lds\n"
"s_add_u32 m0, s69, 88064\n"
"v_exp_f32 v32, v32\n"
"v_exp_f32 v33, v33\n"
"v_exp_f32 v34, v34\n"
"v_exp_f32 v35, v35\n"
"v_cvt_pk_bf16_f32 v64, v32, v33\n"
"v_cvt_pk_bf16_f32 v65, v34, v35\n"
"v_mfma_f32_32x32x16_bf16 v[48:63], v[120:123], v[84:87], 0\n"
"s_add_u32 s61, s70, 104448\n"
"buffer_load_dwordx4 v235, s[48:51], s61 offen sc1 lds\n"
"buffer_load_dwordx4 v235, s[48:51], s61 offen offset:1024 sc1 lds\n"
"v_exp_f32 v36, v36\n"
"v_exp_f32 v37, v37\n"
"v_exp_f32 v38, v38\n"
"v_exp_f32 v39, v39\n"
"v_cvt_pk_bf16_f32 v66, v36, v37\n"
"v_cvt_pk_bf16_f32 v67, v38, v39\n"
"v_mfma_f32_32x32x16_bf16 v[48:63], v[124:127], v[108:111], v[48:63]\n"
"ds_read_b128 v[120:123], v168 offset:4608\n"
"s_add_u32 m0, s69, 90112\n"
"s_add_u32 s61, s70, 106496\n"
"buffer_load_dwordx4 v235, s[48:51], s61 offen sc1 lds\n"
"v_exp_f32 v40, v40\n"
"v_exp_f32 v41, v41\n"
"v_exp_f32 v42, v42\n"
"v_exp_f32 v43, v43\n"
"v_cvt_pk_bf16_f32 v68, v40, v41\n"
"v_cvt_pk_bf16_f32 v69, v42, v43\n"
"v_mfma_f32_32x32x16_bf16 v[152:167], v[132:135], v[76:79], v[152:167]\n"
"buffer_load_dwordx4 v235, s[48:51], s61 offen offset:1024 sc1 lds\n"
"v_accvgpr_read_b32 v180, a0\n"
"v_accvgpr_read_b32 v181, a1\n"
"v_exp_f32 v44, v44\n"
"v_exp_f32 v45, v45\n"
"v_exp_f32 v46, v46\n"
"s_waitcnt lgkmcnt(0)\n"
"v_exp_f32_e64 v47, v47\n"
"v_cvt_pk_bf16_f32 v70, v44, v45\n"
"v_cvt_pk_bf16_f32 v71, v46, v47\n"
"v_mfma_f32_32x32x16_bf16 v[136:151], v[128:131], v[64:67], v[136:151]\n"
"v_accvgpr_read_b32 v182, a2\n"
"v_accvgpr_read_b32 v183, a3\n"
"v_accvgpr_read_b32 v184, a4\n"
"v_exp_f32 v48, v48\n"
"v_exp_f32 v49, v49\n"
"v_exp_f32 v50, v50\n"
"v_exp_f32 v51, v51\n"
"v_cvt_pk_bf16_f32 v72, v48, v49\n"
"v_cvt_pk_bf16_f32 v73, v50, v51\n"
"v_mfma_f32_32x32x16_bf16 v[32:47], v[120:123], v[80:83], 0\n"
"v_accvgpr_read_b32 v185, a5\n"
"v_accvgpr_read_b32 v186, a6\n"
"v_accvgpr_read_b32 v187, a7\n"
"v_cvt_pk_bf16_f32 v196, v180, v181\n"
"v_exp_f32 v52, v52\n"
"v_exp_f32 v53, v53\n"
"v_exp_f32 v54, v54\n"
"v_exp_f32 v55, v55\n"
"v_cvt_pk_bf16_f32 v74, v52, v53\n"
"v_cvt_pk_bf16_f32 v75, v54, v55\n"
"v_mfma_f32_32x32x16_bf16 v[32:47], v[124:127], v[96:99], v[32:47]\n"
"v_cvt_pk_bf16_f32 v197, v182, v183\n"
"v_cvt_pk_bf16_f32 v198, v184, v185\n"
"v_cvt_pk_bf16_f32 v199, v186, v187\n"
"v_exp_f32 v56, v56\n"
"v_exp_f32 v57, v57\n"
"v_exp_f32 v58, v58\n"
"v_exp_f32 v59, v59\n"
"v_cvt_pk_bf16_f32 v76, v56, v57\n"
"v_cvt_pk_bf16_f32 v77, v58, v59\n"
"v_mfma_f32_32x32x16_bf16 v[136:151], v[176:179], v[68:71], v[136:151]\n"
"s_add_u32 s60, s67, 0\n"
"buffer_store_dwordx4 v[196:199], v235, s[48:51], s60 offen sc1\n"
"v_accvgpr_read_b32 v188, a8\n"
"v_exp_f32 v60, v60\n"
"v_exp_f32_e64 v61, v61\n"
"v_exp_f32 v62, v62\n"
"v_exp_f32 v63, v63\n"
"v_cvt_pk_bf16_f32 v78, v60, v61\n"
"v_cvt_pk_bf16_f32 v79, v62, v63\n"
"v_mfma_f32_32x32x16_bf16 v[152:167], v[128:131], v[72:75], v[152:167]\n"
"ds_read_b128 v[128:131], v170 offset:4608\n"
"ds_read_b128 v[132:135], v170 offset:5248\n"
"v_accvgpr_read_b32 v189, a9\n"
"v_accvgpr_read_b32 v190, a10\n"
"v_accvgpr_read_b32 v191, a11\n"
"v_exp_f32 v32, v32\n"
"v_exp_f32 v33, v33\n"
"v_exp_f32 v34, v34\n"
"v_exp_f32 v35, v35\n"
"v_cvt_pk_bf16_f32 v64, v32, v33\n"
"v_cvt_pk_bf16_f32 v65, v34, v35\n"
"v_mfma_f32_32x32x16_bf16 v[48:63], v[120:123], v[84:87], 0\n"
"v_accvgpr_read_b32 v192, a12\n"
"v_accvgpr_read_b32 v193, a13\n"
"v_accvgpr_read_b32 v194, a14\n"
"v_exp_f32 v36, v36\n"
"v_exp_f32 v37, v37\n"
"v_exp_f32 v38, v38\n"
"v_exp_f32 v39, v39\n"
"v_cvt_pk_bf16_f32 v66, v36, v37\n"
"v_cvt_pk_bf16_f32 v67, v38, v39\n"
"v_mfma_f32_32x32x16_bf16 v[48:63], v[124:127], v[112:115], v[48:63]\n"
"ds_read_b128 v[120:123], v168 offset:6912\n"
"v_accvgpr_read_b32 v195, a15\n"
"v_cvt_pk_bf16_f32 v200, v188, v189\n"
"v_cvt_pk_bf16_f32 v201, v190, v191\n"
"v_exp_f32 v40, v40\n"
"v_exp_f32 v41, v41\n"
"v_exp_f32 v42, v42\n"
"v_exp_f32 v43, v43\n"
"v_cvt_pk_bf16_f32 v68, v40, v41\n"
"v_cvt_pk_bf16_f32 v69, v42, v43\n"
"v_mfma_f32_32x32x16_bf16 v[152:167], v[176:179], v[76:79], v[152:167]\n"
"v_cvt_pk_bf16_f32 v202, v192, v193\n"
"v_cvt_pk_bf16_f32 v203, v194, v195\n"
"buffer_store_dwordx4 v[200:203], v235, s[48:51], s60 offen offset:1024 sc1\n"
"v_exp_f32 v44, v44\n"
"v_exp_f32 v45, v45\n"
"v_exp_f32 v46, v46\n"
"s_waitcnt lgkmcnt(0)\n"
"v_exp_f32_e64 v47, v47\n"
"v_cvt_pk_bf16_f32 v70, v44, v45\n"
"v_cvt_pk_bf16_f32 v71, v46, v47\n"
"v_mfma_f32_32x32x16_bf16 v[136:151], v[128:131], v[64:67], v[136:151]\n"
"v_accvgpr_read_b32 v180, a16\n"
"v_accvgpr_read_b32 v181, a17\n"
"v_accvgpr_read_b32 v182, a18\n"
"v_accvgpr_read_b32 v183, a19\n"
"v_exp_f32 v48, v48\n"
"v_exp_f32 v49, v49\n"
"v_exp_f32 v50, v50\n"
"v_exp_f32 v51, v51\n"
"v_cvt_pk_bf16_f32 v72, v48, v49\n"
"v_cvt_pk_bf16_f32 v73, v50, v51\n"
"v_mfma_f32_32x32x16_bf16 v[32:47], v[120:123], v[80:83], 0\n"
"v_accvgpr_read_b32 v184, a20\n"
"v_accvgpr_read_b32 v185, a21\n"
"v_accvgpr_read_b32 v186, a22\n"
"v_exp_f32 v52, v52\n"
"v_exp_f32 v53, v53\n"
"v_exp_f32 v54, v54\n"
"v_exp_f32 v55, v55\n"
"v_cvt_pk_bf16_f32 v74, v52, v53\n"
"v_cvt_pk_bf16_f32 v75, v54, v55\n"
"v_mfma_f32_32x32x16_bf16 v[32:47], v[124:127], v[100:103], v[32:47]\n"
"v_accvgpr_read_b32 v187, a23\n"
"v_cvt_pk_bf16_f32 v204, v180, v181\n"
"v_cvt_pk_bf16_f32 v205, v182, v183\n"
"v_exp_f32 v56, v56\n"
"v_exp_f32 v57, v57\n"
"v_exp_f32 v58, v58\n"
"v_exp_f32 v59, v59\n"
"v_cvt_pk_bf16_f32 v76, v56, v57\n"
"v_cvt_pk_bf16_f32 v77, v58, v59\n"
"v_mfma_f32_32x32x16_bf16 v[136:151], v[132:135], v[68:71], v[136:151]\n"
"v_cvt_pk_bf16_f32 v206, v184, v185\n"
"v_cvt_pk_bf16_f32 v207, v186, v187\n"
"s_add_u32 s60, s67, 2048\n"
"v_exp_f32 v60, v60\n"
"v_exp_f32 v61, v61\n"
"v_exp_f32 v62, v62\n"
"v_exp_f32 v63, v63\n"
"v_cvt_pk_bf16_f32 v78, v60, v61\n"
"v_cvt_pk_bf16_f32 v79, v62, v63\n"
"v_mfma_f32_32x32x16_bf16 v[152:167], v[128:131], v[72:75], v[152:167]\n"
"ds_read_b128 v[128:131], v170 offset:6912\n"
"ds_read_b128 v[176:179], v170 offset:7552\n"
"buffer_store_dwordx4 v[204:207], v235, s[48:51], s60 offen sc1\n"
"v_accvgpr_read_b32 v188, a24\n"
"v_accvgpr_read_b32 v189, a25\n"
"v_exp_f32 v32, v32\n"
"v_exp_f32 v33, v33\n"
"v_exp_f32 v34, v34\n"
"v_exp_f32 v35, v35\n"
"v_cvt_pk_bf16_f32 v64, v32, v33\n"
"v_cvt_pk_bf16_f32 v65, v34, v35\n"
"v_mfma_f32_32x32x16_bf16 v[48:63], v[120:123], v[84:87], 0\n"
"v_accvgpr_read_b32 v190, a26\n"
"v_accvgpr_read_b32 v191, a27\n"
"v_accvgpr_read_b32 v192, a28\n"
"v_exp_f32 v36, v36\n"
"v_exp_f32 v37, v37\n"
"v_exp_f32 v38, v38\n"
"v_exp_f32 v39, v39\n"
"v_cvt_pk_bf16_f32 v66, v36, v37\n"
"v_cvt_pk_bf16_f32 v67, v38, v39\n"
"v_mfma_f32_32x32x16_bf16 v[48:63], v[124:127], v[116:119], v[48:63]\n"
"ds_read_b128 v[120:123], v168 offset:9216\n"
"ds_read_b128 v[124:127], v169 offset:512\n"
"v_accvgpr_read_b32 v193, a29\n"
"v_accvgpr_read_b32 v194, a30\n"
"v_accvgpr_read_b32 v195, a31\n"
"v_cvt_pk_bf16_f32 v208, v188, v189\n"
"v_exp_f32 v40, v40\n"
"v_exp_f32 v41, v41\n"
"v_exp_f32 v42, v42\n"
"v_exp_f32 v43, v43\n"
"v_cvt_pk_bf16_f32 v68, v40, v41\n"
"v_cvt_pk_bf16_f32 v69, v42, v43\n"
"v_mfma_f32_32x32x16_bf16 v[152:167], v[132:135], v[76:79], v[152:167]\n"
"v_cvt_pk_bf16_f32 v209, v190, v191\n"
"v_cvt_pk_bf16_f32 v210, v192, v193\n"
"v_cvt_pk_bf16_f32 v211, v194, v195\n"
"v_exp_f32 v44, v44\n"
"v_exp_f32 v45, v45\n"
"v_exp_f32 v46, v46\n"
"s_waitcnt lgkmcnt(0)\n"
"v_exp_f32_e64 v47, v47\n"
"v_cvt_pk_bf16_f32 v70, v44, v45\n"
"v_cvt_pk_bf16_f32 v71, v46, v47\n"
"v_mfma_f32_32x32x16_bf16 v[136:151], v[128:131], v[64:67], v[136:151]\n"
"buffer_store_dwordx4 v[208:211], v235, s[48:51], s60 offen offset:1024 sc1\n"
"v_accvgpr_read_b32 v180, a32\n"
"v_accvgpr_read_b32 v181, a33\n"
"v_exp_f32 v48, v48\n"
"v_exp_f32 v49, v49\n"
"v_exp_f32 v50, v50\n"
"v_exp_f32 v51, v51\n"
"v_cvt_pk_bf16_f32 v72, v48, v49\n"
"v_cvt_pk_bf16_f32 v73, v50, v51\n"
"v_mfma_f32_32x32x16_bf16 v[32:47], v[120:123], v[80:83], 0\n"
"v_accvgpr_read_b32 v182, a34\n"
"v_accvgpr_read_b32 v183, a35\n"
"v_accvgpr_read_b32 v184, a36\n"
"v_exp_f32 v52, v52\n"
"v_exp_f32 v53, v53\n"
"v_exp_f32 v54, v54\n"
"v_exp_f32 v55, v55\n"
"v_cvt_pk_bf16_f32 v74, v52, v53\n"
"v_cvt_pk_bf16_f32 v75, v54, v55\n"
"v_mfma_f32_32x32x16_bf16 v[32:47], v[124:127], v[88:91], v[32:47]\n"
"v_accvgpr_read_b32 v185, a37\n"
"v_accvgpr_read_b32 v186, a38\n"
"v_accvgpr_read_b32 v187, a39\n"
"v_exp_f32 v56, v56\n"
"v_exp_f32 v57, v57\n"
"v_exp_f32 v58, v58\n"
"v_exp_f32 v59, v59\n"
"v_cvt_pk_bf16_f32 v76, v56, v57\n"
"v_cvt_pk_bf16_f32 v77, v58, v59\n"
"v_mfma_f32_32x32x16_bf16 v[136:151], v[176:179], v[68:71], v[136:151]\n"
"v_cvt_pk_bf16_f32 v196, v180, v181\n"
"v_cvt_pk_bf16_f32 v197, v182, v183\n"
"v_cvt_pk_bf16_f32 v198, v184, v185\n"
"v_exp_f32 v60, v60\n"
"v_exp_f32 v61, v61\n"
"v_exp_f32 v62, v62\n"
"v_exp_f32 v63, v63\n"
"v_cvt_pk_bf16_f32 v78, v60, v61\n"
"v_cvt_pk_bf16_f32 v79, v62, v63\n"
"v_mfma_f32_32x32x16_bf16 v[152:167], v[128:131], v[72:75], v[152:167]\n"
"ds_read_b128 v[128:131], v170 offset:9216\n"
"ds_read_b128 v[132:135], v170 offset:9856\n"
"v_cvt_pk_bf16_f32 v199, v186, v187\n"
"s_add_u32 s60, s67, 4096\n"
"buffer_store_dwordx4 v[196:199], v235, s[48:51], s60 offen sc1\n"
"v_exp_f32 v32, v32\n"
"v_exp_f32 v33, v33\n"
"v_exp_f32 v34, v34\n"
"v_exp_f32 v35, v35\n"
"v_cvt_pk_bf16_f32 v64, v32, v33\n"
"v_cvt_pk_bf16_f32 v65, v34, v35\n"
"v_mfma_f32_32x32x16_bf16 v[48:63], v[120:123], v[84:87], 0\n"
"v_accvgpr_read_b32 v188, a40\n"
"v_accvgpr_read_b32 v189, a41\n"
"v_accvgpr_read_b32 v190, a42\n"
"v_accvgpr_read_b32 v191, a43\n"
"v_exp_f32 v36, v36\n"
"v_exp_f32 v37, v37\n"
"v_exp_f32 v38, v38\n"
"v_exp_f32 v39, v39\n"
"v_cvt_pk_bf16_f32 v66, v36, v37\n"
"v_cvt_pk_bf16_f32 v67, v38, v39\n"
"v_mfma_f32_32x32x16_bf16 v[48:63], v[124:127], v[104:107], v[48:63]\n"
"ds_read_b128 v[120:123], v168 offset:11520\n"
"v_accvgpr_read_b32 v192, a44\n"
"v_accvgpr_read_b32 v193, a45\n"
"v_accvgpr_read_b32 v194, a46\n"
"v_exp_f32 v40, v40\n"
"v_exp_f32 v41, v41\n"
"v_exp_f32 v42, v42\n"
"v_exp_f32 v43, v43\n"
"v_cvt_pk_bf16_f32 v68, v40, v41\n"
"v_cvt_pk_bf16_f32 v69, v42, v43\n"
"v_mfma_f32_32x32x16_bf16 v[152:167], v[176:179], v[76:79], v[152:167]\n"
"v_accvgpr_read_b32 v195, a47\n"
"v_cvt_pk_bf16_f32 v200, v188, v189\n"
"v_cvt_pk_bf16_f32 v201, v190, v191\n"
"v_exp_f32 v44, v44\n"
"v_exp_f32 v45, v45\n"
"v_exp_f32 v46, v46\n"
"s_waitcnt lgkmcnt(0)\n"
"v_exp_f32_e64 v47, v47\n"
"v_cvt_pk_bf16_f32 v70, v44, v45\n"
"v_cvt_pk_bf16_f32 v71, v46, v47\n"
"v_mfma_f32_32x32x16_bf16 v[136:151], v[128:131], v[64:67], v[136:151]\n"
"v_cvt_pk_bf16_f32 v202, v192, v193\n"
"v_cvt_pk_bf16_f32 v203, v194, v195\n"
"buffer_store_dwordx4 v[200:203], v235, s[48:51], s60 offen offset:1024 sc1\n"
"v_exp_f32 v48, v48\n"
"v_exp_f32 v49, v49\n"
"v_exp_f32 v50, v50\n"
"v_exp_f32 v51, v51\n"
"v_cvt_pk_bf16_f32 v72, v48, v49\n"
"v_cvt_pk_bf16_f32 v73, v50, v51\n"
"v_mfma_f32_32x32x16_bf16 v[32:47], v[120:123], v[80:83], 0\n"
"v_accvgpr_read_b32 v180, a48\n"
"v_accvgpr_read_b32 v181, a49\n"
"v_accvgpr_read_b32 v182, a50\n"
"v_exp_f32 v52, v52\n"
"v_exp_f32 v53, v53\n"
"v_exp_f32 v54, v54\n"
"v_exp_f32 v55, v55\n"
"v_cvt_pk_bf16_f32 v74, v52, v53\n"
"v_cvt_pk_bf16_f32 v75, v54, v55\n"
"v_mfma_f32_32x32x16_bf16 v[32:47], v[124:127], v[92:95], v[32:47]\n"
"v_accvgpr_read_b32 v183, a51\n"
"v_accvgpr_read_b32 v184, a52\n"
"v_accvgpr_read_b32 v185, a53\n"
"v_exp_f32 v56, v56\n"
"v_exp_f32 v57, v57\n"
"v_exp_f32 v58, v58\n"
"v_exp_f32 v59, v59\n"
"v_cvt_pk_bf16_f32 v76, v56, v57\n"
"v_cvt_pk_bf16_f32 v77, v58, v59\n"
"v_mfma_f32_32x32x16_bf16 v[136:151], v[132:135], v[68:71], v[136:151]\n"
"v_accvgpr_read_b32 v186, a54\n"
"v_accvgpr_read_b32 v187, a55\n"
"v_cvt_pk_bf16_f32 v204, v180, v181\n"
"v_exp_f32 v60, v60\n"
"v_exp_f32 v61, v61\n"
"v_exp_f32 v62, v62\n"
"v_exp_f32 v63, v63\n"
"v_cvt_pk_bf16_f32 v78, v60, v61\n"
"v_cvt_pk_bf16_f32 v79, v62, v63\n"
"v_mfma_f32_32x32x16_bf16 v[152:167], v[128:131], v[72:75], v[152:167]\n"
"ds_read_b128 v[128:131], v170 offset:11520\n"
"ds_read_b128 v[176:179], v170 offset:12160\n"
"v_cvt_pk_bf16_f32 v205, v182, v183\n"
"v_cvt_pk_bf16_f32 v206, v184, v185\n"
"v_cvt_pk_bf16_f32 v207, v186, v187\n"
"s_add_u32 s60, s67, 6144\n"
"v_exp_f32 v32, v32\n"
"v_exp_f32 v33, v33\n"
"v_exp_f32 v34, v34\n"
"v_exp_f32 v35, v35\n"
"v_cvt_pk_bf16_f32 v64, v32, v33\n"
"v_cvt_pk_bf16_f32 v65, v34, v35\n"
"v_mfma_f32_32x32x16_bf16 v[48:63], v[120:123], v[84:87], 0\n"
"buffer_store_dwordx4 v[204:207], v235, s[48:51], s60 offen sc1\n"
"v_accvgpr_read_b32 v188, a56\n"
"v_accvgpr_read_b32 v189, a57\n"
"v_exp_f32 v36, v36\n"
"v_exp_f32 v37, v37\n"
"v_exp_f32 v38, v38\n"
"v_exp_f32 v39, v39\n"
"v_cvt_pk_bf16_f32 v66, v36, v37\n"
"v_cvt_pk_bf16_f32 v67, v38, v39\n"
"v_mfma_f32_32x32x16_bf16 v[48:63], v[124:127], v[108:111], v[48:63]\n"
"ds_read_b128 v[120:123], v168 offset:13824\n"
"v_accvgpr_read_b32 v190, a58\n"
"v_accvgpr_read_b32 v191, a59\n"
"v_accvgpr_read_b32 v192, a60\n"
"v_exp_f32 v40, v40\n"
"v_exp_f32 v41, v41\n"
"v_exp_f32 v42, v42\n"
"v_exp_f32 v43, v43\n"
"v_cvt_pk_bf16_f32 v68, v40, v41\n"
"v_cvt_pk_bf16_f32 v69, v42, v43\n"
"v_mfma_f32_32x32x16_bf16 v[152:167], v[132:135], v[76:79], v[152:167]\n"
"v_accvgpr_read_b32 v193, a61\n"
"v_accvgpr_read_b32 v194, a62\n"
"v_accvgpr_read_b32 v195, a63\n"
"v_exp_f32 v44, v44\n"
"v_exp_f32 v45, v45\n"
"v_exp_f32 v46, v46\n"
"s_waitcnt lgkmcnt(0)\n"
"v_exp_f32_e64 v47, v47\n"
"v_cvt_pk_bf16_f32 v70, v44, v45\n"
"v_cvt_pk_bf16_f32 v71, v46, v47\n"
"v_mfma_f32_32x32x16_bf16 v[136:151], v[128:131], v[64:67], v[136:151]\n"
"v_cvt_pk_bf16_f32 v208, v188, v189\n"
"v_cvt_pk_bf16_f32 v209, v190, v191\n"
"v_cvt_pk_bf16_f32 v210, v192, v193\n"
"v_exp_f32 v48, v48\n"
"v_exp_f32 v49, v49\n"
"v_exp_f32 v50, v50\n"
"v_exp_f32 v51, v51\n"
"v_cvt_pk_bf16_f32 v72, v48, v49\n"
"v_cvt_pk_bf16_f32 v73, v50, v51\n"
"v_mfma_f32_32x32x16_bf16 v[32:47], v[120:123], v[80:83], 0\n"
"v_cvt_pk_bf16_f32 v211, v194, v195\n"
"buffer_store_dwordx4 v[208:211], v235, s[48:51], s60 offen offset:1024 sc1\n"
"v_accvgpr_read_b32 v180, a64\n"
"v_exp_f32 v52, v52\n"
"v_exp_f32 v53, v53\n"
"v_exp_f32 v54, v54\n"
"v_exp_f32 v55, v55\n"
"v_cvt_pk_bf16_f32 v74, v52, v53\n"
"v_cvt_pk_bf16_f32 v75, v54, v55\n"
"v_mfma_f32_32x32x16_bf16 v[32:47], v[124:127], v[96:99], v[32:47]\n"
"v_accvgpr_read_b32 v181, a65\n"
"v_accvgpr_read_b32 v182, a66\n"
"v_accvgpr_read_b32 v183, a67\n"
"v_accvgpr_read_b32 v184, a68\n"
"v_exp_f32 v56, v56\n"
"v_exp_f32 v57, v57\n"
"v_exp_f32 v58, v58\n"
"v_exp_f32 v59, v59\n"
"v_cvt_pk_bf16_f32 v76, v56, v57\n"
"v_cvt_pk_bf16_f32 v77, v58, v59\n"
"v_mfma_f32_32x32x16_bf16 v[136:151], v[176:179], v[68:71], v[136:151]\n"
"v_accvgpr_read_b32 v185, a69\n"
"v_accvgpr_read_b32 v186, a70\n"
"v_accvgpr_read_b32 v187, a71\n"
"v_exp_f32 v60, v60\n"
"v_exp_f32 v61, v61\n"
"v_exp_f32 v62, v62\n"
"v_exp_f32 v63, v63\n"
"v_cvt_pk_bf16_f32 v78, v60, v61\n"
"v_cvt_pk_bf16_f32 v79, v62, v63\n"
"v_mfma_f32_32x32x16_bf16 v[152:167], v[128:131], v[72:75], v[152:167]\n"
"ds_read_b128 v[128:131], v170 offset:13824\n"
"ds_read_b128 v[132:135], v170 offset:14464\n"
"v_cvt_pk_bf16_f32 v196, v180, v181\n"
"v_cvt_pk_bf16_f32 v197, v182, v183\n"
"v_cvt_pk_bf16_f32 v198, v184, v185\n"
"v_exp_f32 v32, v32\n"
"v_exp_f32 v33, v33\n"
"v_exp_f32 v34, v34\n"
"v_exp_f32 v35, v35\n"
"v_cvt_pk_bf16_f32 v64, v32, v33\n"
"v_cvt_pk_bf16_f32 v65, v34, v35\n"
"v_mfma_f32_32x32x16_bf16 v[48:63], v[120:123], v[84:87], 0\n"
"v_cvt_pk_bf16_f32 v199, v186, v187\n"
"s_add_u32 s60, s67, 8192\n"
"buffer_store_dwordx4 v[196:199], v235, s[48:51], s60 offen sc1\n"
"v_exp_f32 v36, v36\n"
"v_exp_f32 v37, v37\n"
"v_exp_f32 v38, v38\n"
"v_exp_f32 v39, v39\n"
"v_cvt_pk_bf16_f32 v66, v36, v37\n"
"v_cvt_pk_bf16_f32 v67, v38, v39\n"
"v_mfma_f32_32x32x16_bf16 v[48:63], v[124:127], v[112:115], v[48:63]\n"
"ds_read_b128 v[120:123], v168 offset:16128\n"
"v_accvgpr_read_b32 v188, a72\n"
"v_accvgpr_read_b32 v189, a73\n"
"v_accvgpr_read_b32 v190, a74\n"
"v_exp_f32 v40, v40\n"
"v_exp_f32 v41, v41\n"
"v_exp_f32 v42, v42\n"
"v_exp_f32 v43, v43\n"
"v_cvt_pk_bf16_f32 v68, v40, v41\n"
"v_cvt_pk_bf16_f32 v69, v42, v43\n"
"v_mfma_f32_32x32x16_bf16 v[152:167], v[176:179], v[76:79], v[152:167]\n"
"v_accvgpr_read_b32 v191, a75\n"
"v_accvgpr_read_b32 v192, a76\n"
"v_accvgpr_read_b32 v193, a77\n"
"v_exp_f32 v44, v44\n"
"v_exp_f32 v45, v45\n"
"v_exp_f32 v46, v46\n"
"s_waitcnt lgkmcnt(0)\n"
"v_exp_f32_e64 v47, v47\n"
"v_cvt_pk_bf16_f32 v70, v44, v45\n"
"v_cvt_pk_bf16_f32 v71, v46, v47\n"
"v_mfma_f32_32x32x16_bf16 v[136:151], v[128:131], v[64:67], v[136:151]\n"
"v_accvgpr_read_b32 v194, a78\n"
"v_accvgpr_read_b32 v195, a79\n"
"v_cvt_pk_bf16_f32 v200, v188, v189\n"
"v_exp_f32 v48, v48\n"
"v_exp_f32 v49, v49\n"
"v_exp_f32 v50, v50\n"
"v_exp_f32 v51, v51\n"
"v_cvt_pk_bf16_f32 v72, v48, v49\n"
"v_cvt_pk_bf16_f32 v73, v50, v51\n"
"v_mfma_f32_32x32x16_bf16 v[32:47], v[120:123], v[80:83], 0\n"
"v_cvt_pk_bf16_f32 v201, v190, v191\n"
"v_cvt_pk_bf16_f32 v202, v192, v193\n"
"v_cvt_pk_bf16_f32 v203, v194, v195\n"
"buffer_store_dwordx4 v[200:203], v235, s[48:51], s60 offen offset:1024 sc1\n"
"v_exp_f32 v52, v52\n"
"v_exp_f32 v53, v53\n"
"v_exp_f32 v54, v54\n"
"v_exp_f32 v55, v55\n"
"v_cvt_pk_bf16_f32 v74, v52, v53\n"
"v_cvt_pk_bf16_f32 v75, v54, v55\n"
"v_mfma_f32_32x32x16_bf16 v[32:47], v[124:127], v[100:103], v[32:47]\n"
"v_accvgpr_read_b32 v180, a80\n"
"v_accvgpr_read_b32 v181, a81\n"
"v_accvgpr_read_b32 v182, a82\n"
"v_exp_f32 v56, v56\n"
"v_exp_f32 v57, v57\n"
"v_exp_f32 v58, v58\n"
"v_exp_f32 v59, v59\n"
"v_cvt_pk_bf16_f32 v76, v56, v57\n"
"v_cvt_pk_bf16_f32 v77, v58, v59\n"
"v_mfma_f32_32x32x16_bf16 v[136:151], v[132:135], v[68:71], v[136:151]\n"
"v_accvgpr_read_b32 v183, a83\n"
"v_accvgpr_read_b32 v184, a84\n"
"v_accvgpr_read_b32 v185, a85\n"
"v_exp_f32 v60, v60\n"
"v_exp_f32 v61, v61\n"
"v_exp_f32 v62, v62\n"
"v_exp_f32 v63, v63\n"
"v_cvt_pk_bf16_f32 v78, v60, v61\n"
"v_cvt_pk_bf16_f32 v79, v62, v63\n"
"v_mfma_f32_32x32x16_bf16 v[152:167], v[128:131], v[72:75], v[152:167]\n"
"ds_read_b128 v[128:131], v170 offset:16128\n"
"ds_read_b128 v[176:179], v170 offset:16768\n"
"v_accvgpr_read_b32 v186, a86\n"
"v_accvgpr_read_b32 v187, a87\n"
"v_cvt_pk_bf16_f32 v204, v180, v181\n"
"v_exp_f32 v32, v32\n"
"v_exp_f32 v33, v33\n"
"v_exp_f32 v34, v34\n"
"v_exp_f32 v35, v35\n"
"v_cvt_pk_bf16_f32 v64, v32, v33\n"
"v_cvt_pk_bf16_f32 v65, v34, v35\n"
"v_mfma_f32_32x32x16_bf16 v[48:63], v[120:123], v[84:87], 0\n"
"v_cvt_pk_bf16_f32 v205, v182, v183\n"
"v_cvt_pk_bf16_f32 v206, v184, v185\n"
"v_cvt_pk_bf16_f32 v207, v186, v187\n"
"v_exp_f32 v36, v36\n"
"v_exp_f32 v37, v37\n"
"v_exp_f32 v38, v38\n"
"v_exp_f32 v39, v39\n"
"v_cvt_pk_bf16_f32 v66, v36, v37\n"
"v_cvt_pk_bf16_f32 v67, v38, v39\n"
"v_mfma_f32_32x32x16_bf16 v[48:63], v[124:127], v[116:119], v[48:63]\n"
"ds_read_b128 v[120:123], v168 offset:18432\n"
"ds_read_b128 v[124:127], v169 offset:1024\n"
"s_add_u32 s60, s67, 10240\n"
"buffer_store_dwordx4 v[204:207], v235, s[48:51], s60 offen sc1\n"
"v_accvgpr_read_b32 v188, a88\n"
"v_exp_f32 v40, v40\n"
"v_exp_f32 v41, v41\n"
"v_exp_f32 v42, v42\n"
"v_exp_f32 v43, v43\n"
"v_cvt_pk_bf16_f32 v68, v40, v41\n"
"v_cvt_pk_bf16_f32 v69, v42, v43\n"
"v_mfma_f32_32x32x16_bf16 v[152:167], v[132:135], v[76:79], v[152:167]\n"
"v_accvgpr_read_b32 v189, a89\n"
"v_accvgpr_read_b32 v190, a90\n"
"v_accvgpr_read_b32 v191, a91\n"
"v_exp_f32 v44, v44\n"
"v_exp_f32 v45, v45\n"
"v_exp_f32 v46, v46\n"
"s_waitcnt lgkmcnt(0)\n"
"v_exp_f32_e64 v47, v47\n"
"v_cvt_pk_bf16_f32 v70, v44, v45\n"
"v_cvt_pk_bf16_f32 v71, v46, v47\n"
"v_mfma_f32_32x32x16_bf16 v[136:151], v[128:131], v[64:67], v[136:151]\n"
"v_accvgpr_read_b32 v192, a92\n"
"v_accvgpr_read_b32 v193, a93\n"
"v_accvgpr_read_b32 v194, a94\n"
"v_accvgpr_read_b32 v195, a95\n"
"v_exp_f32 v48, v48\n"
"v_exp_f32 v49, v49\n"
"v_exp_f32 v50, v50\n"
"v_exp_f32 v51, v51\n"
"v_cvt_pk_bf16_f32 v72, v48, v49\n"
"v_cvt_pk_bf16_f32 v73, v50, v51\n"
"v_mfma_f32_32x32x16_bf16 v[32:47], v[120:123], v[80:83], 0\n"
"v_cvt_pk_bf16_f32 v208, v188, v189\n"
"v_cvt_pk_bf16_f32 v209, v190, v191\n"
"v_cvt_pk_bf16_f32 v210, v192, v193\n"
"v_exp_f32 v52, v52\n"
"v_exp_f32 v53, v53\n"
"v_exp_f32 v54, v54\n"
"v_exp_f32 v55, v55\n"
"v_cvt_pk_bf16_f32 v74, v52, v53\n"
"v_cvt_pk_bf16_f32 v75, v54, v55\n"
"v_mfma_f32_32x32x16_bf16 v[32:47], v[124:127], v[88:91], v[32:47]\n"
"v_cvt_pk_bf16_f32 v211, v194, v195\n"
"buffer_store_dwordx4 v[208:211], v235, s[48:51], s60 offen offset:1024 sc1\n"
"v_accvgpr_read_b32 v180, a96\n"
"v_exp_f32 v56, v56\n"
"v_exp_f32 v57, v57\n"
"v_exp_f32 v58, v58\n"
"v_exp_f32 v59, v59\n"
"v_cvt_pk_bf16_f32 v76, v56, v57\n"
"v_cvt_pk_bf16_f32 v77, v58, v59\n"
"v_mfma_f32_32x32x16_bf16 v[136:151], v[176:179], v[68:71], v[136:151]\n"
"v_accvgpr_read_b32 v181, a97\n"
"v_accvgpr_read_b32 v182, a98\n"
"v_accvgpr_read_b32 v183, a99\n"
"v_exp_f32 v60, v60\n"
"v_exp_f32 v61, v61\n"
"v_exp_f32 v62, v62\n"
"v_exp_f32 v63, v63\n"
"v_cvt_pk_bf16_f32 v78, v60, v61\n"
"v_cvt_pk_bf16_f32 v79, v62, v63\n"
"v_mfma_f32_32x32x16_bf16 v[152:167], v[128:131], v[72:75], v[152:167]\n"
"ds_read_b128 v[128:131], v170 offset:18432\n"
"ds_read_b128 v[132:135], v170 offset:19072\n"
"v_accvgpr_read_b32 v184, a100\n"
"v_accvgpr_read_b32 v185, a101\n"
"v_accvgpr_read_b32 v186, a102\n"
"v_exp_f32 v32, v32\n"
"v_exp_f32 v33, v33\n"
"v_exp_f32 v34, v34\n"
"v_exp_f32 v35, v35\n"
"v_cvt_pk_bf16_f32 v64, v32, v33\n"
"v_cvt_pk_bf16_f32 v65, v34, v35\n"
"v_mfma_f32_32x32x16_bf16 v[48:63], v[120:123], v[84:87], 0\n"
"v_accvgpr_read_b32 v187, a103\n"
"v_cvt_pk_bf16_f32 v196, v180, v181\n"
"v_cvt_pk_bf16_f32 v197, v182, v183\n"
"v_exp_f32 v36, v36\n"
"v_exp_f32 v37, v37\n"
"v_exp_f32 v38, v38\n"
"v_exp_f32 v39, v39\n"
"v_cvt_pk_bf16_f32 v66, v36, v37\n"
"v_cvt_pk_bf16_f32 v67, v38, v39\n"
"v_mfma_f32_32x32x16_bf16 v[48:63], v[124:127], v[104:107], v[48:63]\n"
"ds_read_b128 v[120:123], v168 offset:20736\n"
"v_cvt_pk_bf16_f32 v198, v184, v185\n"
"v_cvt_pk_bf16_f32 v199, v186, v187\n"
"s_add_u32 s60, s67, 12288\n"
"buffer_store_dwordx4 v[196:199], v235, s[48:51], s60 offen sc1\n"
"v_exp_f32 v40, v40\n"
"v_exp_f32 v41, v41\n"
"v_exp_f32 v42, v42\n"
"v_exp_f32 v43, v43\n"
"v_cvt_pk_bf16_f32 v68, v40, v41\n"
"v_cvt_pk_bf16_f32 v69, v42, v43\n"
"v_mfma_f32_32x32x16_bf16 v[152:167], v[176:179], v[76:79], v[152:167]\n"
"v_accvgpr_read_b32 v188, a104\n"
"v_accvgpr_read_b32 v189, a105\n"
"v_accvgpr_read_b32 v190, a106\n"
"v_exp_f32 v44, v44\n"
"v_exp_f32 v45, v45\n"
"v_exp_f32 v46, v46\n"
"s_waitcnt lgkmcnt(0)\n"
"v_exp_f32_e64 v47, v47\n"
"v_cvt_pk_bf16_f32 v70, v44, v45\n"
"v_cvt_pk_bf16_f32 v71, v46, v47\n"
"v_mfma_f32_32x32x16_bf16 v[136:151], v[128:131], v[64:67], v[136:151]\n"
"v_accvgpr_read_b32 v191, a107\n"
"v_accvgpr_read_b32 v192, a108\n"
"v_accvgpr_read_b32 v193, a109\n"
"v_exp_f32 v48, v48\n"
"v_exp_f32 v49, v49\n"
"v_exp_f32 v50, v50\n"
"v_exp_f32 v51, v51\n"
"v_cvt_pk_bf16_f32 v72, v48, v49\n"
"v_cvt_pk_bf16_f32 v73, v50, v51\n"
"v_mfma_f32_32x32x16_bf16 v[32:47], v[120:123], v[80:83], 0\n"
"v_accvgpr_read_b32 v194, a110\n"
"v_accvgpr_read_b32 v195, a111\n"
"v_cvt_pk_bf16_f32 v200, v188, v189\n"
"v_exp_f32 v52, v52\n"
"v_exp_f32 v53, v53\n"
"v_exp_f32 v54, v54\n"
"v_exp_f32 v55, v55\n"
"v_cvt_pk_bf16_f32 v74, v52, v53\n"
"v_cvt_pk_bf16_f32 v75, v54, v55\n"
"v_mfma_f32_32x32x16_bf16 v[32:47], v[124:127], v[92:95], v[32:47]\n"
"v_cvt_pk_bf16_f32 v201, v190, v191\n"
"v_cvt_pk_bf16_f32 v202, v192, v193\n"
"v_cvt_pk_bf16_f32 v203, v194, v195\n"
"v_exp_f32 v56, v56\n"
"v_exp_f32 v57, v57\n"
"v_exp_f32 v58, v58\n"
"v_exp_f32 v59, v59\n"
"v_cvt_pk_bf16_f32 v76, v56, v57\n"
"v_cvt_pk_bf16_f32 v77, v58, v59\n"
"v_mfma_f32_32x32x16_bf16 v[136:151], v[132:135], v[68:71], v[136:151]\n"
"buffer_store_dwordx4 v[200:203], v235, s[48:51], s60 offen offset:1024 sc1\n"
"v_accvgpr_read_b32 v180, a112\n"
"v_accvgpr_read_b32 v181, a113\n"
"v_exp_f32 v60, v60\n"
"v_exp_f32 v61, v61\n"
"v_exp_f32 v62, v62\n"
"v_exp_f32 v63, v63\n"
"v_cvt_pk_bf16_f32 v78, v60, v61\n"
"v_cvt_pk_bf16_f32 v79, v62, v63\n"
"v_mfma_f32_32x32x16_bf16 v[152:167], v[128:131], v[72:75], v[152:167]\n"
"ds_read_b128 v[128:131], v170 offset:20736\n"
"ds_read_b128 v[176:179], v170 offset:21376\n"
"v_accvgpr_read_b32 v182, a114\n"
"v_accvgpr_read_b32 v183, a115\n"
"v_accvgpr_read_b32 v184, a116\n"
"v_exp_f32 v32, v32\n"
"v_exp_f32 v33, v33\n"
"v_exp_f32 v34, v34\n"
"v_exp_f32 v35, v35\n"
"v_cvt_pk_bf16_f32 v64, v32, v33\n"
"v_cvt_pk_bf16_f32 v65, v34, v35\n"
"v_mfma_f32_32x32x16_bf16 v[48:63], v[120:123], v[84:87], 0\n"
"v_accvgpr_read_b32 v185, a117\n"
"v_accvgpr_read_b32 v186, a118\n"
"v_accvgpr_read_b32 v187, a119\n"
"v_cvt_pk_bf16_f32 v204, v180, v181\n"
"v_exp_f32 v36, v36\n"
"v_exp_f32 v37, v37\n"
"v_exp_f32 v38, v38\n"
"v_exp_f32 v39, v39\n"
"v_cvt_pk_bf16_f32 v66, v36, v37\n"
"v_cvt_pk_bf16_f32 v67, v38, v39\n"
"v_mfma_f32_32x32x16_bf16 v[48:63], v[124:127], v[108:111], v[48:63]\n"
"ds_read_b128 v[120:123], v168 offset:23040\n"
"v_cvt_pk_bf16_f32 v205, v182, v183\n"
"v_cvt_pk_bf16_f32 v206, v184, v185\n"
"v_cvt_pk_bf16_f32 v207, v186, v187\n"
"v_exp_f32 v40, v40\n"
"v_exp_f32 v41, v41\n"
"v_exp_f32 v42, v42\n"
"v_exp_f32 v43, v43\n"
"v_cvt_pk_bf16_f32 v68, v40, v41\n"
"v_cvt_pk_bf16_f32 v69, v42, v43\n"
"v_mfma_f32_32x32x16_bf16 v[152:167], v[132:135], v[76:79], v[152:167]\n"
"s_add_u32 s60, s67, 14336\n"
"buffer_store_dwordx4 v[204:207], v235, s[48:51], s60 offen sc1\n"
"v_accvgpr_read_b32 v188, a120\n"
"v_exp_f32 v44, v44\n"
"v_exp_f32 v45, v45\n"
"v_exp_f32 v46, v46\n"
"s_waitcnt lgkmcnt(0)\n"
"v_exp_f32_e64 v47, v47\n"
"v_cvt_pk_bf16_f32 v70, v44, v45\n"
"v_cvt_pk_bf16_f32 v71, v46, v47\n"
"v_mfma_f32_32x32x16_bf16 v[136:151], v[128:131], v[64:67], v[136:151]\n"
"v_accvgpr_read_b32 v189, a121\n"
"v_accvgpr_read_b32 v190, a122\n"
"v_accvgpr_read_b32 v191, a123\n"
"v_exp_f32 v48, v48\n"
"v_exp_f32 v49, v49\n"
"v_exp_f32 v50, v50\n"
"v_exp_f32 v51, v51\n"
"v_cvt_pk_bf16_f32 v72, v48, v49\n"
"v_cvt_pk_bf16_f32 v73, v50, v51\n"
"v_mfma_f32_32x32x16_bf16 v[32:47], v[120:123], v[80:83], 0\n"
"v_accvgpr_read_b32 v192, a124\n"
"v_accvgpr_read_b32 v193, a125\n"
"v_accvgpr_read_b32 v194, a126\n"
"v_exp_f32 v52, v52\n"
"v_exp_f32 v53, v53\n"
"v_exp_f32 v54, v54\n"
"v_exp_f32 v55, v55\n"
"v_cvt_pk_bf16_f32 v74, v52, v53\n"
"v_cvt_pk_bf16_f32 v75, v54, v55\n"
"v_mfma_f32_32x32x16_bf16 v[32:47], v[124:127], v[96:99], v[32:47]\n"
"v_accvgpr_read_b32 v195, a127\n"
"v_cvt_pk_bf16_f32 v208, v188, v189\n"
"v_cvt_pk_bf16_f32 v209, v190, v191\n"
"v_exp_f32 v56, v56\n"
"v_exp_f32 v57, v57\n"
"v_exp_f32 v58, v58\n"
"v_exp_f32 v59, v59\n"
"v_cvt_pk_bf16_f32 v76, v56, v57\n"
"v_cvt_pk_bf16_f32 v77, v58, v59\n"
"v_mfma_f32_32x32x16_bf16 v[136:151], v[176:179], v[68:71], v[136:151]\n"
"v_cvt_pk_bf16_f32 v210, v192, v193\n"
"v_cvt_pk_bf16_f32 v211, v194, v195\n"
"buffer_store_dwordx4 v[208:211], v235, s[48:51], s60 offen offset:1024 sc1\n"
"v_exp_f32 v60, v60\n"
"v_exp_f32 v61, v61\n"
"v_exp_f32 v62, v62\n"
"v_exp_f32 v63, v63\n"
"v_cvt_pk_bf16_f32 v78, v60, v61\n"
"v_cvt_pk_bf16_f32 v79, v62, v63\n"
"v_mfma_f32_32x32x16_bf16 v[152:167], v[128:131], v[72:75], v[152:167]\n"
"ds_read_b128 v[128:131], v170 offset:23040\n"
"ds_read_b128 v[132:135], v170 offset:23680\n"
"v_accvgpr_read_b32 v180, a128\n"
"v_accvgpr_read_b32 v181, a129\n"
"v_accvgpr_read_b32 v182, a130\n"
"v_accvgpr_read_b32 v183, a131\n"
"v_exp_f32 v32, v32\n"
"v_exp_f32 v33, v33\n"
"v_exp_f32 v34, v34\n"
"v_exp_f32 v35, v35\n"
"v_cvt_pk_bf16_f32 v64, v32, v33\n"
"v_cvt_pk_bf16_f32 v65, v34, v35\n"
"v_mfma_f32_32x32x16_bf16 v[48:63], v[120:123], v[84:87], 0\n"
"v_accvgpr_read_b32 v184, a132\n"
"v_accvgpr_read_b32 v185, a133\n"
"v_accvgpr_read_b32 v186, a134\n"
"v_exp_f32 v36, v36\n"
"v_exp_f32 v37, v37\n"
"v_exp_f32 v38, v38\n"
"v_exp_f32 v39, v39\n"
"v_cvt_pk_bf16_f32 v66, v36, v37\n"
"v_cvt_pk_bf16_f32 v67, v38, v39\n"
"v_mfma_f32_32x32x16_bf16 v[48:63], v[124:127], v[112:115], v[48:63]\n"
"ds_read_b128 v[120:123], v168 offset:25344\n"
"v_accvgpr_read_b32 v187, a135\n"
"v_cvt_pk_bf16_f32 v196, v180, v181\n"
"v_cvt_pk_bf16_f32 v197, v182, v183\n"
"v_exp_f32 v40, v40\n"
"v_exp_f32 v41, v41\n"
"v_exp_f32 v42, v42\n"
"v_exp_f32 v43, v43\n"
"v_cvt_pk_bf16_f32 v68, v40, v41\n"
"v_cvt_pk_bf16_f32 v69, v42, v43\n"
"v_mfma_f32_32x32x16_bf16 v[152:167], v[176:179], v[76:79], v[152:167]\n"
"v_cvt_pk_bf16_f32 v198, v184, v185\n"
"v_cvt_pk_bf16_f32 v199, v186, v187\n"
"s_add_u32 s60, s67, 16384\n"
"v_exp_f32 v44, v44\n"
"v_exp_f32 v45, v45\n"
"v_exp_f32 v46, v46\n"
"s_waitcnt lgkmcnt(0)\n"
"v_exp_f32_e64 v47, v47\n"
"v_cvt_pk_bf16_f32 v70, v44, v45\n"
"v_cvt_pk_bf16_f32 v71, v46, v47\n"
"v_mfma_f32_32x32x16_bf16 v[136:151], v[128:131], v[64:67], v[136:151]\n"
"buffer_store_dwordx4 v[196:199], v235, s[48:51], s60 offen sc1\n"
"v_accvgpr_read_b32 v188, a136\n"
"v_accvgpr_read_b32 v189, a137\n"
"v_exp_f32 v48, v48\n"
"v_exp_f32 v49, v49\n"
"v_exp_f32 v50, v50\n"
"v_exp_f32 v51, v51\n"
"v_cvt_pk_bf16_f32 v72, v48, v49\n"
"v_cvt_pk_bf16_f32 v73, v50, v51\n"
"v_mfma_f32_32x32x16_bf16 v[32:47], v[120:123], v[80:83], 0\n"
"v_accvgpr_read_b32 v190, a138\n"
"v_accvgpr_read_b32 v191, a139\n"
"v_accvgpr_read_b32 v192, a140\n"
"v_exp_f32 v52, v52\n"
"v_exp_f32 v53, v53\n"
"v_exp_f32 v54, v54\n"
"v_exp_f32 v55, v55\n"
"v_cvt_pk_bf16_f32 v74, v52, v53\n"
"v_cvt_pk_bf16_f32 v75, v54, v55\n"
"v_mfma_f32_32x32x16_bf16 v[32:47], v[124:127], v[100:103], v[32:47]\n"
"v_accvgpr_read_b32 v193, a141\n"
"v_accvgpr_read_b32 v194, a142\n"
"v_accvgpr_read_b32 v195, a143\n"
"v_cvt_pk_bf16_f32 v200, v188, v189\n"
"v_exp_f32 v56, v56\n"
"v_exp_f32 v57, v57\n"
"v_exp_f32 v58, v58\n"
"v_exp_f32 v59, v59\n"
"v_cvt_pk_bf16_f32 v76, v56, v57\n"
"v_cvt_pk_bf16_f32 v77, v58, v59\n"
"v_mfma_f32_32x32x16_bf16 v[136:151], v[132:135], v[68:71], v[136:151]\n"
"v_cvt_pk_bf16_f32 v201, v190, v191\n"
"v_cvt_pk_bf16_f32 v202, v192, v193\n"
"v_cvt_pk_bf16_f32 v203, v194, v195\n"
"v_exp_f32 v60, v60\n"
"v_exp_f32 v61, v61\n"
"v_exp_f32 v62, v62\n"
"v_exp_f32 v63, v63\n"
"v_cvt_pk_bf16_f32 v78, v60, v61\n"
"v_cvt_pk_bf16_f32 v79, v62, v63\n"
"v_mfma_f32_32x32x16_bf16 v[152:167], v[128:131], v[72:75], v[152:167]\n"
"ds_read_b128 v[128:131], v170 offset:25344\n"
"ds_read_b128 v[176:179], v170 offset:25984\n"
"buffer_store_dwordx4 v[200:203], v235, s[48:51], s60 offen offset:1024 sc1\n"
"v_accvgpr_read_b32 v180, a144\n"
"v_accvgpr_read_b32 v181, a145\n"
"v_exp_f32 v32, v32\n"
"v_exp_f32 v33, v33\n"
"v_exp_f32 v34, v34\n"
"v_exp_f32 v35, v35\n"
"v_cvt_pk_bf16_f32 v64, v32, v33\n"
"v_cvt_pk_bf16_f32 v65, v34, v35\n"
"v_mfma_f32_32x32x16_bf16 v[48:63], v[120:123], v[84:87], 0\n"
"v_accvgpr_read_b32 v182, a146\n"
"v_accvgpr_read_b32 v183, a147\n"
"v_accvgpr_read_b32 v184, a148\n"
"v_exp_f32 v36, v36\n"
"v_exp_f32 v37, v37\n"
"v_exp_f32 v38, v38\n"
"v_exp_f32 v39, v39\n"
"v_cvt_pk_bf16_f32 v66, v36, v37\n"
"v_cvt_pk_bf16_f32 v67, v38, v39\n"
"v_mfma_f32_32x32x16_bf16 v[48:63], v[124:127], v[116:119], v[48:63]\n"
"ds_read_b128 v[120:123], v168 offset:27648\n"
"ds_read_b128 v[124:127], v169 offset:1536\n"
"v_accvgpr_read_b32 v185, a149\n"
"v_accvgpr_read_b32 v186, a150\n"
"v_accvgpr_read_b32 v187, a151\n"
"v_exp_f32 v40, v40\n"
"v_exp_f32 v41, v41\n"
"v_exp_f32 v42, v42\n"
"v_exp_f32 v43, v43\n"
"v_cvt_pk_bf16_f32 v68, v40, v41\n"
"v_cvt_pk_bf16_f32 v69, v42, v43\n"
"v_mfma_f32_32x32x16_bf16 v[152:167], v[132:135], v[76:79], v[152:167]\n"
"v_cvt_pk_bf16_f32 v204, v180, v181\n"
"v_cvt_pk_bf16_f32 v205, v182, v183\n"
"v_cvt_pk_bf16_f32 v206, v184, v185\n"
"v_exp_f32 v44, v44\n"
"v_exp_f32 v45, v45\n"
"v_exp_f32 v46, v46\n"
"s_waitcnt lgkmcnt(0)\n"
"v_exp_f32_e64 v47, v47\n"
"v_cvt_pk_bf16_f32 v70, v44, v45\n"
"v_cvt_pk_bf16_f32 v71, v46, v47\n"
"v_mfma_f32_32x32x16_bf16 v[136:151], v[128:131], v[64:67], v[136:151]\n"
"v_cvt_pk_bf16_f32 v207, v186, v187\n"
"s_add_u32 s60, s67, 18432\n"
"buffer_store_dwordx4 v[204:207], v235, s[48:51], s60 offen sc1\n"
"v_exp_f32 v48, v48\n"
"v_exp_f32 v49, v49\n"
"v_exp_f32 v50, v50\n"
"v_exp_f32 v51, v51\n"
"v_cvt_pk_bf16_f32 v72, v48, v49\n"
"v_cvt_pk_bf16_f32 v73, v50, v51\n"
"v_mfma_f32_32x32x16_bf16 v[32:47], v[120:123], v[80:83], 0\n"
"v_accvgpr_read_b32 v188, a152\n"
"v_accvgpr_read_b32 v189, a153\n"
"v_accvgpr_read_b32 v190, a154\n"
"v_accvgpr_read_b32 v191, a155\n"
"v_exp_f32 v52, v52\n"
"v_exp_f32 v53, v53\n"
"v_exp_f32 v54, v54\n"
"v_exp_f32 v55, v55\n"
"v_cvt_pk_bf16_f32 v74, v52, v53\n"
"v_cvt_pk_bf16_f32 v75, v54, v55\n"
"v_mfma_f32_32x32x16_bf16 v[32:47], v[124:127], v[88:91], v[32:47]\n"
"v_accvgpr_read_b32 v192, a156\n"
"v_accvgpr_read_b32 v193, a157\n"
"v_accvgpr_read_b32 v194, a158\n"
"v_exp_f32 v56, v56\n"
"v_exp_f32 v57, v57\n"
"v_exp_f32 v58, v58\n"
"v_exp_f32 v59, v59\n"
"v_cvt_pk_bf16_f32 v76, v56, v57\n"
"v_cvt_pk_bf16_f32 v77, v58, v59\n"
"v_mfma_f32_32x32x16_bf16 v[136:151], v[176:179], v[68:71], v[136:151]\n"
"v_accvgpr_read_b32 v195, a159\n"
"v_cvt_pk_bf16_f32 v208, v188, v189\n"
"v_cvt_pk_bf16_f32 v209, v190, v191\n"
"v_exp_f32 v60, v60\n"
"v_exp_f32 v61, v61\n"
"v_exp_f32 v62, v62\n"
"v_exp_f32 v63, v63\n"
"v_cvt_pk_bf16_f32 v78, v60, v61\n"
"v_cvt_pk_bf16_f32 v79, v62, v63\n"
"v_mfma_f32_32x32x16_bf16 v[152:167], v[128:131], v[72:75], v[152:167]\n"
"ds_read_b128 v[128:131], v170 offset:27648\n"
"ds_read_b128 v[132:135], v170 offset:28288\n"
"v_cvt_pk_bf16_f32 v210, v192, v193\n"
"v_cvt_pk_bf16_f32 v211, v194, v195\n"
"buffer_store_dwordx4 v[208:211], v235, s[48:51], s60 offen offset:1024 sc1\n"
"v_exp_f32 v32, v32\n"
"v_exp_f32 v33, v33\n"
"v_exp_f32 v34, v34\n"
"v_exp_f32 v35, v35\n"
"v_cvt_pk_bf16_f32 v64, v32, v33\n"
"v_cvt_pk_bf16_f32 v65, v34, v35\n"
"v_mfma_f32_32x32x16_bf16 v[48:63], v[120:123], v[84:87], 0\n"
"v_accvgpr_read_b32 v180, a160\n"
"v_accvgpr_read_b32 v181, a161\n"
"v_accvgpr_read_b32 v182, a162\n"
"v_exp_f32 v36, v36\n"
"v_exp_f32 v37, v37\n"
"v_exp_f32 v38, v38\n"
"v_exp_f32 v39, v39\n"
"v_cvt_pk_bf16_f32 v66, v36, v37\n"
"v_cvt_pk_bf16_f32 v67, v38, v39\n"
"v_mfma_f32_32x32x16_bf16 v[48:63], v[124:127], v[104:107], v[48:63]\n"
"ds_read_b128 v[120:123], v168 offset:29952\n"
"v_accvgpr_read_b32 v183, a163\n"
"v_accvgpr_read_b32 v184, a164\n"
"v_accvgpr_read_b32 v185, a165\n"
"v_exp_f32 v40, v40\n"
"v_exp_f32 v41, v41\n"
"v_exp_f32 v42, v42\n"
"v_exp_f32 v43, v43\n"
"v_cvt_pk_bf16_f32 v68, v40, v41\n"
"v_cvt_pk_bf16_f32 v69, v42, v43\n"
"v_mfma_f32_32x32x16_bf16 v[152:167], v[176:179], v[76:79], v[152:167]\n"
"v_accvgpr_read_b32 v186, a166\n"
"v_accvgpr_read_b32 v187, a167\n"
"v_cvt_pk_bf16_f32 v196, v180, v181\n"
"v_exp_f32 v44, v44\n"
"v_exp_f32 v45, v45\n"
"v_exp_f32 v46, v46\n"
"s_waitcnt lgkmcnt(0)\n"
"v_exp_f32_e64 v47, v47\n"
"v_cvt_pk_bf16_f32 v70, v44, v45\n"
"v_cvt_pk_bf16_f32 v71, v46, v47\n"
"v_mfma_f32_32x32x16_bf16 v[136:151], v[128:131], v[64:67], v[136:151]\n"
"v_cvt_pk_bf16_f32 v197, v182, v183\n"
"v_cvt_pk_bf16_f32 v198, v184, v185\n"
"v_cvt_pk_bf16_f32 v199, v186, v187\n"
"s_add_u32 s60, s67, 20480\n"
"v_exp_f32 v48, v48\n"
"v_exp_f32 v49, v49\n"
"v_exp_f32 v50, v50\n"
"v_exp_f32 v51, v51\n"
"v_cvt_pk_bf16_f32 v72, v48, v49\n"
"v_cvt_pk_bf16_f32 v73, v50, v51\n"
"v_mfma_f32_32x32x16_bf16 v[32:47], v[120:123], v[80:83], 0\n"
"buffer_store_dwordx4 v[196:199], v235, s[48:51], s60 offen sc1\n"
"v_accvgpr_read_b32 v188, a168\n"
"v_accvgpr_read_b32 v189, a169\n"
"v_exp_f32 v52, v52\n"
"v_exp_f32 v53, v53\n"
"v_exp_f32 v54, v54\n"
"v_exp_f32 v55, v55\n"
"v_cvt_pk_bf16_f32 v74, v52, v53\n"
"v_cvt_pk_bf16_f32 v75, v54, v55\n"
"v_mfma_f32_32x32x16_bf16 v[32:47], v[124:127], v[92:95], v[32:47]\n"
"v_accvgpr_read_b32 v190, a170\n"
"v_accvgpr_read_b32 v191, a171\n"
"v_accvgpr_read_b32 v192, a172\n"
"v_exp_f32 v56, v56\n"
"v_exp_f32 v57, v57\n"
"v_exp_f32 v58, v58\n"
"v_exp_f32 v59, v59\n"
"v_cvt_pk_bf16_f32 v76, v56, v57\n"
"v_cvt_pk_bf16_f32 v77, v58, v59\n"
"v_mfma_f32_32x32x16_bf16 v[136:151], v[132:135], v[68:71], v[136:151]\n"
"v_accvgpr_read_b32 v193, a173\n"
"v_accvgpr_read_b32 v194, a174\n"
"v_accvgpr_read_b32 v195, a175\n"
"v_exp_f32 v60, v60\n"
"v_exp_f32 v61, v61\n"
"v_exp_f32 v62, v62\n"
"v_exp_f32 v63, v63\n"
"v_cvt_pk_bf16_f32 v78, v60, v61\n"
"v_cvt_pk_bf16_f32 v79, v62, v63\n"
"v_mfma_f32_32x32x16_bf16 v[152:167], v[128:131], v[72:75], v[152:167]\n"
"ds_read_b128 v[128:131], v170 offset:29952\n"
"ds_read_b128 v[176:179], v170 offset:30592\n"
"v_cvt_pk_bf16_f32 v200, v188, v189\n"
"v_cvt_pk_bf16_f32 v201, v190, v191\n"
"v_cvt_pk_bf16_f32 v202, v192, v193\n"
"v_exp_f32 v32, v32\n"
"v_exp_f32 v33, v33\n"
"v_exp_f32 v34, v34\n"
"v_exp_f32 v35, v35\n"
"v_cvt_pk_bf16_f32 v64, v32, v33\n"
"v_cvt_pk_bf16_f32 v65, v34, v35\n"
"v_mfma_f32_32x32x16_bf16 v[48:63], v[120:123], v[84:87], 0\n"
"v_cvt_pk_bf16_f32 v203, v194, v195\n"
"buffer_store_dwordx4 v[200:203], v235, s[48:51], s60 offen offset:1024 sc1\n"
"v_accvgpr_read_b32 v180, a176\n"
"v_exp_f32 v36, v36\n"
"v_exp_f32 v37, v37\n"
"v_exp_f32 v38, v38\n"
"v_exp_f32 v39, v39\n"
"v_cvt_pk_bf16_f32 v66, v36, v37\n"
"v_cvt_pk_bf16_f32 v67, v38, v39\n"
"v_mfma_f32_32x32x16_bf16 v[48:63], v[124:127], v[108:111], v[48:63]\n"
"ds_read_b128 v[120:123], v168 offset:32256\n"
"v_accvgpr_read_b32 v181, a177\n"
"v_accvgpr_read_b32 v182, a178\n"
"v_accvgpr_read_b32 v183, a179\n"
"v_accvgpr_read_b32 v184, a180\n"
"v_exp_f32 v40, v40\n"
"v_exp_f32 v41, v41\n"
"v_exp_f32 v42, v42\n"
"v_exp_f32 v43, v43\n"
"v_cvt_pk_bf16_f32 v68, v40, v41\n"
"v_cvt_pk_bf16_f32 v69, v42, v43\n"
"v_mfma_f32_32x32x16_bf16 v[152:167], v[132:135], v[76:79], v[152:167]\n"
"v_accvgpr_read_b32 v185, a181\n"
"v_accvgpr_read_b32 v186, a182\n"
"v_accvgpr_read_b32 v187, a183\n"
"v_exp_f32 v44, v44\n"
"v_exp_f32 v45, v45\n"
"v_exp_f32 v46, v46\n"
"s_waitcnt lgkmcnt(0)\n"
"v_exp_f32_e64 v47, v47\n"
"v_cvt_pk_bf16_f32 v70, v44, v45\n"
"v_cvt_pk_bf16_f32 v71, v46, v47\n"
"v_mfma_f32_32x32x16_bf16 v[136:151], v[128:131], v[64:67], v[136:151]\n"
"v_cvt_pk_bf16_f32 v204, v180, v181\n"
"v_cvt_pk_bf16_f32 v205, v182, v183\n"
"v_cvt_pk_bf16_f32 v206, v184, v185\n"
"v_exp_f32 v48, v48\n"
"v_exp_f32 v49, v49\n"
"v_exp_f32 v50, v50\n"
"v_exp_f32 v51, v51\n"
"v_cvt_pk_bf16_f32 v72, v48, v49\n"
"v_cvt_pk_bf16_f32 v73, v50, v51\n"
"v_mfma_f32_32x32x16_bf16 v[32:47], v[120:123], v[80:83], 0\n"
"v_cvt_pk_bf16_f32 v207, v186, v187\n"
"s_add_u32 s60, s67, 22528\n"
"buffer_store_dwordx4 v[204:207], v235, s[48:51], s60 offen sc1\n"
"v_exp_f32 v52, v52\n"
"v_exp_f32 v53, v53\n"
"v_exp_f32 v54, v54\n"
"v_exp_f32 v55, v55\n"
"v_cvt_pk_bf16_f32 v74, v52, v53\n"
"v_cvt_pk_bf16_f32 v75, v54, v55\n"
"v_mfma_f32_32x32x16_bf16 v[32:47], v[124:127], v[96:99], v[32:47]\n"
"v_accvgpr_read_b32 v188, a184\n"
"v_accvgpr_read_b32 v189, a185\n"
"v_accvgpr_read_b32 v190, a186\n"
"v_exp_f32 v56, v56\n"
"v_exp_f32 v57, v57\n"
"v_exp_f32 v58, v58\n"
"v_exp_f32 v59, v59\n"
"v_cvt_pk_bf16_f32 v76, v56, v57\n"
"v_cvt_pk_bf16_f32 v77, v58, v59\n"
"v_mfma_f32_32x32x16_bf16 v[136:151], v[176:179], v[68:71], v[136:151]\n"
"v_accvgpr_read_b32 v191, a187\n"
"v_accvgpr_read_b32 v192, a188\n"
"v_accvgpr_read_b32 v193, a189\n"
"v_exp_f32 v60, v60\n"
"v_exp_f32 v61, v61\n"
"v_exp_f32 v62, v62\n"
"v_exp_f32 v63, v63\n"
"v_cvt_pk_bf16_f32 v78, v60, v61\n"
"v_cvt_pk_bf16_f32 v79, v62, v63\n"
"v_mfma_f32_32x32x16_bf16 v[152:167], v[128:131], v[72:75], v[152:167]\n"
"ds_read_b128 v[128:131], v170 offset:32256\n"
"ds_read_b128 v[132:135], v170 offset:32896\n"
"v_accvgpr_read_b32 v194, a190\n"
"v_accvgpr_read_b32 v195, a191\n"
"v_cvt_pk_bf16_f32 v208, v188, v189\n"
"v_exp_f32 v32, v32\n"
"v_exp_f32 v33, v33\n"
"v_exp_f32 v34, v34\n"
"v_exp_f32 v35, v35\n"
"v_cvt_pk_bf16_f32 v64, v32, v33\n"
"v_cvt_pk_bf16_f32 v65, v34, v35\n"
"v_mfma_f32_32x32x16_bf16 v[48:63], v[120:123], v[84:87], 0\n"
"v_cvt_pk_bf16_f32 v209, v190, v191\n"
"v_cvt_pk_bf16_f32 v210, v192, v193\n"
"v_cvt_pk_bf16_f32 v211, v194, v195\n"
"buffer_store_dwordx4 v[208:211], v235, s[48:51], s60 offen offset:1024 sc1\n"
"v_exp_f32 v36, v36\n"
"v_exp_f32 v37, v37\n"
"v_exp_f32 v38, v38\n"
"v_exp_f32 v39, v39\n"
"v_cvt_pk_bf16_f32 v66, v36, v37\n"
"v_cvt_pk_bf16_f32 v67, v38, v39\n"
"v_mfma_f32_32x32x16_bf16 v[48:63], v[124:127], v[112:115], v[48:63]\n"
"ds_read_b128 v[120:123], v168 offset:34560\n"
"v_accvgpr_read_b32 v180, a192\n"
"v_accvgpr_read_b32 v181, a193\n"
"v_accvgpr_read_b32 v182, a194\n"
"v_exp_f32 v40, v40\n"
"v_exp_f32 v41, v41\n"
"v_exp_f32 v42, v42\n"
"v_exp_f32 v43, v43\n"
"v_cvt_pk_bf16_f32 v68, v40, v41\n"
"v_cvt_pk_bf16_f32 v69, v42, v43\n"
"v_mfma_f32_32x32x16_bf16 v[152:167], v[176:179], v[76:79], v[152:167]\n"
"v_accvgpr_read_b32 v183, a195\n"
"v_accvgpr_read_b32 v184, a196\n"
"v_accvgpr_read_b32 v185, a197\n"
"v_exp_f32 v44, v44\n"
"v_exp_f32 v45, v45\n"
"v_exp_f32 v46, v46\n"
"s_waitcnt lgkmcnt(0)\n"
"v_exp_f32_e64 v47, v47\n"
"v_cvt_pk_bf16_f32 v70, v44, v45\n"
"v_cvt_pk_bf16_f32 v71, v46, v47\n"
"v_mfma_f32_32x32x16_bf16 v[136:151], v[128:131], v[64:67], v[136:151]\n"
"v_accvgpr_read_b32 v186, a198\n"
"v_accvgpr_read_b32 v187, a199\n"
"v_cvt_pk_bf16_f32 v196, v180, v181\n"
"v_exp_f32 v48, v48\n"
"v_exp_f32 v49, v49\n"
"v_exp_f32 v50, v50\n"
"v_exp_f32 v51, v51\n"
"v_cvt_pk_bf16_f32 v72, v48, v49\n"
"v_cvt_pk_bf16_f32 v73, v50, v51\n"
"v_mfma_f32_32x32x16_bf16 v[32:47], v[120:123], v[80:83], 0\n"
"v_cvt_pk_bf16_f32 v197, v182, v183\n"
"v_cvt_pk_bf16_f32 v198, v184, v185\n"
"v_cvt_pk_bf16_f32 v199, v186, v187\n"
"v_exp_f32 v52, v52\n"
"v_exp_f32 v53, v53\n"
"v_exp_f32 v54, v54\n"
"v_exp_f32 v55, v55\n"
"v_cvt_pk_bf16_f32 v74, v52, v53\n"
"v_cvt_pk_bf16_f32 v75, v54, v55\n"
"v_mfma_f32_32x32x16_bf16 v[32:47], v[124:127], v[100:103], v[32:47]\n"
"s_add_u32 s60, s67, 24576\n"
"buffer_store_dwordx4 v[196:199], v235, s[48:51], s60 offen sc1\n"
"v_accvgpr_read_b32 v188, a200\n"
"v_exp_f32 v56, v56\n"
"v_exp_f32 v57, v57\n"
"v_exp_f32 v58, v58\n"
"v_exp_f32 v59, v59\n"
"v_cvt_pk_bf16_f32 v76, v56, v57\n"
"v_cvt_pk_bf16_f32 v77, v58, v59\n"
"v_mfma_f32_32x32x16_bf16 v[136:151], v[132:135], v[68:71], v[136:151]\n"
"v_accvgpr_read_b32 v189, a201\n"
"v_accvgpr_read_b32 v190, a202\n"
"v_accvgpr_read_b32 v191, a203\n"
"v_exp_f32 v60, v60\n"
"v_exp_f32 v61, v61\n"
"v_exp_f32 v62, v62\n"
"v_exp_f32 v63, v63\n"
"v_cvt_pk_bf16_f32 v78, v60, v61\n"
"v_cvt_pk_bf16_f32 v79, v62, v63\n"
"v_mfma_f32_32x32x16_bf16 v[152:167], v[128:131], v[72:75], v[152:167]\n"
"ds_read_b128 v[128:131], v170 offset:34560\n"
"ds_read_b128 v[176:179], v170 offset:35200\n"
"v_accvgpr_read_b32 v192, a204\n"
"v_accvgpr_read_b32 v193, a205\n"
"v_accvgpr_read_b32 v194, a206\n"
"v_accvgpr_read_b32 v195, a207\n"
"v_exp_f32 v32, v32\n"
"v_exp_f32 v33, v33\n"
"v_exp_f32 v34, v34\n"
"v_exp_f32 v35, v35\n"
"v_cvt_pk_bf16_f32 v64, v32, v33\n"
"v_cvt_pk_bf16_f32 v65, v34, v35\n"
"v_mfma_f32_32x32x16_bf16 v[48:63], v[120:123], v[84:87], 0\n"
"v_cvt_pk_bf16_f32 v200, v188, v189\n"
"v_cvt_pk_bf16_f32 v201, v190, v191\n"
"v_cvt_pk_bf16_f32 v202, v192, v193\n"
"v_exp_f32 v36, v36\n"
"v_exp_f32 v37, v37\n"
"v_exp_f32 v38, v38\n"
"v_exp_f32 v39, v39\n"
"v_cvt_pk_bf16_f32 v66, v36, v37\n"
"v_cvt_pk_bf16_f32 v67, v38, v39\n"
"v_mfma_f32_32x32x16_bf16 v[48:63], v[124:127], v[116:119], v[48:63]\n"
"ds_read_b128 v[120:123], v168 offset:36864\n"
"ds_read_b128 v[124:127], v169 offset:2048\n"
"v_cvt_pk_bf16_f32 v203, v194, v195\n"
"buffer_store_dwordx4 v[200:203], v235, s[48:51], s60 offen offset:1024 sc1\n"
"v_accvgpr_read_b32 v180, a208\n"
"v_exp_f32 v40, v40\n"
"v_exp_f32 v41, v41\n"
"v_exp_f32 v42, v42\n"
"v_exp_f32 v43, v43\n"
"v_cvt_pk_bf16_f32 v68, v40, v41\n"
"v_cvt_pk_bf16_f32 v69, v42, v43\n"
"v_mfma_f32_32x32x16_bf16 v[152:167], v[132:135], v[76:79], v[152:167]\n"
"v_accvgpr_read_b32 v181, a209\n"
"v_accvgpr_read_b32 v182, a210\n"
"v_accvgpr_read_b32 v183, a211\n"
"v_exp_f32 v44, v44\n"
"v_exp_f32 v45, v45\n"
"v_exp_f32 v46, v46\n"
"s_waitcnt lgkmcnt(0)\n"
"v_exp_f32_e64 v47, v47\n"
"v_cvt_pk_bf16_f32 v70, v44, v45\n"
"v_cvt_pk_bf16_f32 v71, v46, v47\n"
"v_mfma_f32_32x32x16_bf16 v[136:151], v[128:131], v[64:67], v[136:151]\n"
"v_accvgpr_read_b32 v184, a212\n"
"v_accvgpr_read_b32 v185, a213\n"
"v_accvgpr_read_b32 v186, a214\n"
"v_exp_f32 v48, v48\n"
"v_exp_f32 v49, v49\n"
"v_exp_f32 v50, v50\n"
"v_exp_f32 v51, v51\n"
"v_cvt_pk_bf16_f32 v72, v48, v49\n"
"v_cvt_pk_bf16_f32 v73, v50, v51\n"
"v_mfma_f32_32x32x16_bf16 v[32:47], v[120:123], v[80:83], 0\n"
"v_accvgpr_read_b32 v187, a215\n"
"v_cvt_pk_bf16_f32 v204, v180, v181\n"
"v_cvt_pk_bf16_f32 v205, v182, v183\n"
"v_exp_f32 v52, v52\n"
"v_exp_f32 v53, v53\n"
"v_exp_f32 v54, v54\n"
"v_exp_f32 v55, v55\n"
"v_cvt_pk_bf16_f32 v74, v52, v53\n"
"v_cvt_pk_bf16_f32 v75, v54, v55\n"
"v_mfma_f32_32x32x16_bf16 v[32:47], v[124:127], v[88:91], v[32:47]\n"
"v_cvt_pk_bf16_f32 v206, v184, v185\n"
"v_cvt_pk_bf16_f32 v207, v186, v187\n"
"s_add_u32 s60, s67, 26624\n"
"buffer_store_dwordx4 v[204:207], v235, s[48:51], s60 offen sc1\n"
"v_exp_f32 v56, v56\n"
"v_exp_f32 v57, v57\n"
"v_exp_f32 v58, v58\n"
"v_exp_f32 v59, v59\n"
"v_cvt_pk_bf16_f32 v76, v56, v57\n"
"v_cvt_pk_bf16_f32 v77, v58, v59\n"
"v_mfma_f32_32x32x16_bf16 v[136:151], v[176:179], v[68:71], v[136:151]\n"
"v_accvgpr_read_b32 v188, a216\n"
"v_accvgpr_read_b32 v189, a217\n"
"v_accvgpr_read_b32 v190, a218\n"
"v_exp_f32 v60, v60\n"
"v_exp_f32 v61, v61\n"
"v_exp_f32 v62, v62\n"
"v_exp_f32 v63, v63\n"
"v_cvt_pk_bf16_f32 v78, v60, v61\n"
"v_cvt_pk_bf16_f32 v79, v62, v63\n"
"v_add_u32 v168, 36864, v168\n"
"v_add_u32 v170, 36864, v170\n"
"v_add_u32 v169, 2048, v169\n"
"v_mfma_f32_32x32x16_bf16 v[152:167], v[128:131], v[72:75], v[152:167]\n"
"ds_read_b128 v[128:131], v170\n"
"ds_read_b128 v[132:135], v170 offset:640\n"
"v_accvgpr_read_b32 v191, a219\n"
"v_accvgpr_read_b32 v192, a220\n"
"v_accvgpr_read_b32 v193, a221\n"
"v_exp_f32 v32, v32\n"
"v_exp_f32 v33, v33\n"
"v_exp_f32 v34, v34\n"
"v_exp_f32 v35, v35\n"
"v_cvt_pk_bf16_f32 v64, v32, v33\n"
"v_cvt_pk_bf16_f32 v65, v34, v35\n"
"v_mfma_f32_32x32x16_bf16 v[48:63], v[120:123], v[84:87], 0\n"
"v_accvgpr_read_b32 v194, a222\n"
"v_accvgpr_read_b32 v195, a223\n"
"v_cvt_pk_bf16_f32 v208, v188, v189\n"
"v_exp_f32 v36, v36\n"
"v_exp_f32 v37, v37\n"
"v_exp_f32 v38, v38\n"
"v_exp_f32 v39, v39\n"
"v_cvt_pk_bf16_f32 v66, v36, v37\n"
"v_cvt_pk_bf16_f32 v67, v38, v39\n"
"v_mfma_f32_32x32x16_bf16 v[48:63], v[124:127], v[104:107], v[48:63]\n"
"ds_read_b128 v[120:123], v168 offset:2304\n"
"v_cvt_pk_bf16_f32 v209, v190, v191\n"
"v_cvt_pk_bf16_f32 v210, v192, v193\n"
"v_cvt_pk_bf16_f32 v211, v194, v195\n"
"v_exp_f32 v40, v40\n"
"v_exp_f32 v41, v41\n"
"v_exp_f32 v42, v42\n"
"v_exp_f32 v43, v43\n"
"v_cvt_pk_bf16_f32 v68, v40, v41\n"
"v_cvt_pk_bf16_f32 v69, v42, v43\n"
"v_mfma_f32_32x32x16_bf16 v[152:167], v[176:179], v[76:79], v[152:167]\n"
"buffer_store_dwordx4 v[208:211], v235, s[48:51], s60 offen offset:1024 sc1\n"
"v_accvgpr_read_b32 v180, a224\n"
"v_accvgpr_read_b32 v181, a225\n"
"v_exp_f32 v44, v44\n"
"v_exp_f32 v45, v45\n"
"v_exp_f32 v46, v46\n"
"s_waitcnt lgkmcnt(0)\n"
"v_exp_f32_e64 v47, v47\n"
"v_cvt_pk_bf16_f32 v70, v44, v45\n"
"v_cvt_pk_bf16_f32 v71, v46, v47\n"
"v_mfma_f32_32x32x16_bf16 v[136:151], v[128:131], v[64:67], v[136:151]\n"
"v_accvgpr_read_b32 v182, a226\n"
"v_accvgpr_read_b32 v183, a227\n"
"v_accvgpr_read_b32 v184, a228\n"
"v_exp_f32 v48, v48\n"
"v_exp_f32 v49, v49\n"
"v_exp_f32 v50, v50\n"
"v_exp_f32 v51, v51\n"
"v_cvt_pk_bf16_f32 v72, v48, v49\n"
"v_cvt_pk_bf16_f32 v73, v50, v51\n"
"v_mfma_f32_32x32x16_bf16 v[32:47], v[120:123], v[80:83], 0\n"
"v_accvgpr_read_b32 v185, a229\n"
"v_accvgpr_read_b32 v186, a230\n"
"v_accvgpr_read_b32 v187, a231\n"
"v_cvt_pk_bf16_f32 v196, v180, v181\n"
"v_exp_f32 v52, v52\n"
"v_exp_f32 v53, v53\n"
"v_exp_f32 v54, v54\n"
"v_exp_f32 v55, v55\n"
"v_cvt_pk_bf16_f32 v74, v52, v53\n"
"v_cvt_pk_bf16_f32 v75, v54, v55\n"
"v_mfma_f32_32x32x16_bf16 v[32:47], v[124:127], v[92:95], v[32:47]\n"
"v_cvt_pk_bf16_f32 v197, v182, v183\n"
"v_cvt_pk_bf16_f32 v198, v184, v185\n"
"v_cvt_pk_bf16_f32 v199, v186, v187\n"
"v_exp_f32 v56, v56\n"
"v_exp_f32 v57, v57\n"
"v_exp_f32 v58, v58\n"
"v_exp_f32 v59, v59\n"
"v_cvt_pk_bf16_f32 v76, v56, v57\n"
"v_cvt_pk_bf16_f32 v77, v58, v59\n"
"v_mfma_f32_32x32x16_bf16 v[136:151], v[132:135], v[68:71], v[136:151]\n"
"s_add_u32 s60, s67, 28672\n"
"buffer_store_dwordx4 v[196:199], v235, s[48:51], s60 offen sc1\n"
"v_accvgpr_read_b32 v188, a232\n"
"v_exp_f32 v60, v60\n"
"v_exp_f32 v61, v61\n"
"v_exp_f32 v62, v62\n"
"v_exp_f32 v63, v63\n"
"v_cvt_pk_bf16_f32 v78, v60, v61\n"
"v_cvt_pk_bf16_f32 v79, v62, v63\n"
"v_mfma_f32_32x32x16_bf16 v[152:167], v[128:131], v[72:75], v[152:167]\n"
"ds_read_b128 v[128:131], v170 offset:2304\n"
"ds_read_b128 v[176:179], v170 offset:2944\n"
"v_accvgpr_read_b32 v189, a233\n"
"v_accvgpr_read_b32 v190, a234\n"
"v_accvgpr_read_b32 v191, a235\n"
"v_exp_f32 v32, v32\n"
"v_exp_f32 v33, v33\n"
"v_exp_f32 v34, v34\n"
"v_exp_f32 v35, v35\n"
"v_cvt_pk_bf16_f32 v64, v32, v33\n"
"v_cvt_pk_bf16_f32 v65, v34, v35\n"
"v_mfma_f32_32x32x16_bf16 v[48:63], v[120:123], v[84:87], 0\n"
"v_accvgpr_read_b32 v192, a236\n"
"v_accvgpr_read_b32 v193, a237\n"
"v_accvgpr_read_b32 v194, a238\n"
"v_exp_f32 v36, v36\n"
"v_exp_f32 v37, v37\n"
"v_exp_f32 v38, v38\n"
"v_exp_f32 v39, v39\n"
"v_cvt_pk_bf16_f32 v66, v36, v37\n"
"v_cvt_pk_bf16_f32 v67, v38, v39\n"
"v_mfma_f32_32x32x16_bf16 v[48:63], v[124:127], v[108:111], v[48:63]\n"
"ds_read_b128 v[120:123], v168 offset:4608\n"
"v_accvgpr_read_b32 v195, a239\n"
"v_cvt_pk_bf16_f32 v200, v188, v189\n"
"v_cvt_pk_bf16_f32 v201, v190, v191\n"
"v_exp_f32 v40, v40\n"
"v_exp_f32 v41, v41\n"
"v_exp_f32 v42, v42\n"
"v_exp_f32 v43, v43\n"
"v_cvt_pk_bf16_f32 v68, v40, v41\n"
"v_cvt_pk_bf16_f32 v69, v42, v43\n"
"v_mfma_f32_32x32x16_bf16 v[152:167], v[132:135], v[76:79], v[152:167]\n"
"v_cvt_pk_bf16_f32 v202, v192, v193\n"
"v_cvt_pk_bf16_f32 v203, v194, v195\n"
"buffer_store_dwordx4 v[200:203], v235, s[48:51], s60 offen offset:1024 sc1\n"
"v_exp_f32 v44, v44\n"
"v_exp_f32 v45, v45\n"
"v_exp_f32 v46, v46\n"
"s_waitcnt lgkmcnt(0)\n"
"v_exp_f32_e64 v47, v47\n"
"v_cvt_pk_bf16_f32 v70, v44, v45\n"
"v_cvt_pk_bf16_f32 v71, v46, v47\n"
"v_mfma_f32_32x32x16_bf16 v[136:151], v[128:131], v[64:67], v[136:151]\n"
"v_accvgpr_read_b32 v180, a240\n"
"v_accvgpr_read_b32 v181, a241\n"
"v_accvgpr_read_b32 v182, a242\n"
"v_accvgpr_read_b32 v183, a243\n"
"v_exp_f32 v48, v48\n"
"v_exp_f32 v49, v49\n"
"v_exp_f32 v50, v50\n"
"v_exp_f32 v51, v51\n"
"v_cvt_pk_bf16_f32 v72, v48, v49\n"
"v_cvt_pk_bf16_f32 v73, v50, v51\n"
"v_mfma_f32_32x32x16_bf16 v[32:47], v[120:123], v[80:83], 0\n"
"v_accvgpr_read_b32 v184, a244\n"
"v_accvgpr_read_b32 v185, a245\n"
"v_accvgpr_read_b32 v186, a246\n"
"v_exp_f32 v52, v52\n"
"v_exp_f32 v53, v53\n"
"v_exp_f32 v54, v54\n"
"v_exp_f32 v55, v55\n"
"v_cvt_pk_bf16_f32 v74, v52, v53\n"
"v_cvt_pk_bf16_f32 v75, v54, v55\n"
"v_mfma_f32_32x32x16_bf16 v[32:47], v[124:127], v[96:99], v[32:47]\n"
"v_accvgpr_read_b32 v187, a247\n"
"v_cvt_pk_bf16_f32 v204, v180, v181\n"
"v_cvt_pk_bf16_f32 v205, v182, v183\n"
"v_exp_f32 v56, v56\n"
"v_exp_f32 v57, v57\n"
"v_exp_f32 v58, v58\n"
"v_exp_f32 v59, v59\n"
"v_cvt_pk_bf16_f32 v76, v56, v57\n"
"v_cvt_pk_bf16_f32 v77, v58, v59\n"
"v_mfma_f32_32x32x16_bf16 v[136:151], v[176:179], v[68:71], v[136:151]\n"
"v_cvt_pk_bf16_f32 v206, v184, v185\n"
"v_cvt_pk_bf16_f32 v207, v186, v187\n"
"s_add_u32 s60, s67, 30720\n"
"v_exp_f32 v60, v60\n"
"v_exp_f32 v61, v61\n"
"v_exp_f32 v62, v62\n"
"v_exp_f32 v63, v63\n"
"v_cvt_pk_bf16_f32 v78, v60, v61\n"
"v_cvt_pk_bf16_f32 v79, v62, v63\n"
"v_mfma_f32_32x32x16_bf16 v[152:167], v[128:131], v[72:75], v[152:167]\n"
"ds_read_b128 v[128:131], v170 offset:4608\n"
"ds_read_b128 v[132:135], v170 offset:5248\n"
"buffer_store_dwordx4 v[204:207], v235, s[48:51], s60 offen sc1\n"
"v_accvgpr_read_b32 v188, a248\n"
"v_accvgpr_read_b32 v189, a249\n"
"v_exp_f32 v32, v32\n"
"v_exp_f32 v33, v33\n"
"v_exp_f32 v34, v34\n"
"v_exp_f32 v35, v35\n"
"v_cvt_pk_bf16_f32 v64, v32, v33\n"
"v_cvt_pk_bf16_f32 v65, v34, v35\n"
"v_mfma_f32_32x32x16_bf16 v[48:63], v[120:123], v[84:87], 0\n"
"v_accvgpr_read_b32 v190, a250\n"
"v_accvgpr_read_b32 v191, a251\n"
"v_accvgpr_read_b32 v192, a252\n"
"v_exp_f32 v36, v36\n"
"v_exp_f32 v37, v37\n"
"v_exp_f32 v38, v38\n"
"v_exp_f32 v39, v39\n"
"v_cvt_pk_bf16_f32 v66, v36, v37\n"
"v_cvt_pk_bf16_f32 v67, v38, v39\n"
"v_mfma_f32_32x32x16_bf16 v[48:63], v[124:127], v[112:115], v[48:63]\n"
"ds_read_b128 v[120:123], v168 offset:6912\n"
"v_accvgpr_read_b32 v193, a253\n"
"v_accvgpr_read_b32 v194, a254\n"
"v_accvgpr_read_b32 v195, a255\n"
"v_cvt_pk_bf16_f32 v208, v188, v189\n"
"v_exp_f32 v40, v40\n"
"v_exp_f32 v41, v41\n"
"v_exp_f32 v42, v42\n"
"v_exp_f32 v43, v43\n"
"v_cvt_pk_bf16_f32 v68, v40, v41\n"
"v_cvt_pk_bf16_f32 v69, v42, v43\n"
"v_mfma_f32_32x32x16_bf16 v[152:167], v[176:179], v[76:79], v[152:167]\n"
"v_cvt_pk_bf16_f32 v209, v190, v191\n"
"v_cvt_pk_bf16_f32 v210, v192, v193\n"
"v_cvt_pk_bf16_f32 v211, v194, v195\n"
"v_exp_f32 v44, v44\n"
"v_exp_f32 v45, v45\n"
"v_exp_f32 v46, v46\n"
"s_waitcnt lgkmcnt(0)\n"
"v_exp_f32_e64 v47, v47\n"
"v_cvt_pk_bf16_f32 v70, v44, v45\n"
"v_cvt_pk_bf16_f32 v71, v46, v47\n"
"v_mfma_f32_32x32x16_bf16 v[136:151], v[128:131], v[64:67], v[136:151]\n"
"buffer_store_dwordx4 v[208:211], v235, s[48:51], s60 offen offset:1024 sc1\n"
"v_cvt_pk_bf16_f32 v196, v0, v1\n"
"v_cvt_pk_bf16_f32 v197, v2, v3\n"
"v_exp_f32 v48, v48\n"
"v_exp_f32 v49, v49\n"
"v_exp_f32 v50, v50\n"
"v_exp_f32 v51, v51\n"
"v_cvt_pk_bf16_f32 v72, v48, v49\n"
"v_cvt_pk_bf16_f32 v73, v50, v51\n"
"v_mfma_f32_32x32x16_bf16 v[32:47], v[120:123], v[80:83], 0\n"
"v_cvt_pk_bf16_f32 v198, v4, v5\n"
"v_cvt_pk_bf16_f32 v199, v6, v7\n"
"s_add_u32 s60, s67, 32768\n"
"v_exp_f32 v52, v52\n"
"v_exp_f32 v53, v53\n"
"v_exp_f32 v54, v54\n"
"v_exp_f32 v55, v55\n"
"v_cvt_pk_bf16_f32 v74, v52, v53\n"
"v_cvt_pk_bf16_f32 v75, v54, v55\n"
"v_mfma_f32_32x32x16_bf16 v[32:47], v[124:127], v[100:103], v[32:47]\n"
"buffer_store_dwordx4 v[196:199], v235, s[48:51], s60 offen sc1\n"
"v_cvt_pk_bf16_f32 v200, v8, v9\n"
"v_cvt_pk_bf16_f32 v201, v10, v11\n"
"v_exp_f32 v56, v56\n"
"v_exp_f32 v57, v57\n"
"v_exp_f32 v58, v58\n"
"v_exp_f32 v59, v59\n"
"v_cvt_pk_bf16_f32 v76, v56, v57\n"
"v_cvt_pk_bf16_f32 v77, v58, v59\n"
"v_mfma_f32_32x32x16_bf16 v[136:151], v[132:135], v[68:71], v[136:151]\n"
"v_cvt_pk_bf16_f32 v202, v12, v13\n"
"v_cvt_pk_bf16_f32 v203, v14, v15\n"
"buffer_store_dwordx4 v[200:203], v235, s[48:51], s60 offen offset:1024 sc1\n"
"v_exp_f32 v60, v60\n"
"v_exp_f32 v61, v61\n"
"v_exp_f32 v62, v62\n"
"v_exp_f32 v63, v63\n"
"v_cvt_pk_bf16_f32 v78, v60, v61\n"
"v_cvt_pk_bf16_f32 v79, v62, v63\n"
"v_mfma_f32_32x32x16_bf16 v[152:167], v[128:131], v[72:75], v[152:167]\n"
"ds_read_b128 v[128:131], v170 offset:6912\n"
"ds_read_b128 v[176:179], v170 offset:7552\n"
"v_cvt_pk_bf16_f32 v204, v16, v17\n"
"v_cvt_pk_bf16_f32 v205, v18, v19\n"
"v_cvt_pk_bf16_f32 v206, v20, v21\n"
"v_exp_f32 v32, v32\n"
"v_exp_f32 v33, v33\n"
"v_exp_f32 v34, v34\n"
"v_exp_f32 v35, v35\n"
"v_cvt_pk_bf16_f32 v64, v32, v33\n"
"v_cvt_pk_bf16_f32 v65, v34, v35\n"
"v_mfma_f32_32x32x16_bf16 v[48:63], v[120:123], v[84:87], 0\n"
"v_cvt_pk_bf16_f32 v207, v22, v23\n"
"s_add_u32 s60, s67, 34816\n"
"buffer_store_dwordx4 v[204:207], v235, s[48:51], s60 offen sc1\n"
"v_cvt_pk_bf16_f32 v208, v24, v25\n"
"v_exp_f32 v36, v36\n"
"v_exp_f32 v37, v37\n"
"v_exp_f32 v38, v38\n"
"v_exp_f32 v39, v39\n"
"v_cvt_pk_bf16_f32 v66, v36, v37\n"
"v_cvt_pk_bf16_f32 v67, v38, v39\n"
"v_mfma_f32_32x32x16_bf16 v[48:63], v[124:127], v[116:119], v[48:63]\n"
"ds_read_b128 v[120:123], v168 offset:9216\n"
"ds_read_b128 v[124:127], v169 offset:512\n"
"v_cvt_pk_bf16_f32 v209, v26, v27\n"
"v_cvt_pk_bf16_f32 v210, v28, v29\n"
"v_cvt_pk_bf16_f32 v211, v30, v31\n"
"v_exp_f32 v40, v40\n"
"v_exp_f32 v41, v41\n"
"v_exp_f32 v42, v42\n"
"v_exp_f32 v43, v43\n"
"v_cvt_pk_bf16_f32 v68, v40, v41\n"
"v_cvt_pk_bf16_f32 v69, v42, v43\n"
"v_mfma_f32_32x32x16_bf16 v[152:167], v[132:135], v[76:79], v[152:167]\n"
"buffer_store_dwordx4 v[208:211], v235, s[48:51], s60 offen offset:1024 sc1\n"
"v_and_b32_e64 v212, 31, v228\n"
"v_lshrrev_b32 v213, 5, v228\n"
"v_lshrrev_b32 v214, 2, v212\n"
"v_and_b32 v214, 1, v214\n"
"v_cmp_eq_u32 vcc, v214, v213\n"
"v_exp_f32 v44, v44\n"
"v_exp_f32 v45, v45\n"
"v_exp_f32 v46, v46\n"
"s_waitcnt lgkmcnt(0)\n"
"v_exp_f32_e64 v47, v47\n"
"v_cvt_pk_bf16_f32 v70, v44, v45\n"
"v_cvt_pk_bf16_f32 v71, v46, v47\n"
"v_mfma_f32_32x32x16_bf16 v[136:151], v[128:131], v[64:67], v[136:151]\n"
"v_lshrrev_b32_e64 v215, 3, v212\n"
"v_and_b32 v215, 1, v215\n"
"v_and_b32 v216, 3, v212\n"
"v_lshl_add_u32 v215, v215, 2, v216\n"
"v_mov_b32 v216, 99\n"
"v_cndmask_b32 v215, v216, v215\n"
"v_cmp_gt_u32 vcc, 16, v212\n"
"v_mov_b32 v217, 0x3f80\n"
"v_mov_b32 v218, 0x3f800000\n"
"v_cndmask_b32 v215, v216, v215\n"
"v_exp_f32 v48, v48\n"
"v_exp_f32_e64 v49, v49\n"
"v_exp_f32 v50, v50\n"
"v_exp_f32 v51, v51\n"
"v_cvt_pk_bf16_f32 v72, v48, v49\n"
"v_cvt_pk_bf16_f32 v73, v50, v51\n"
"v_mfma_f32_32x32x16_bf16 v[32:47], v[120:123], v[80:83], 0\n"
"v_cmp_eq_u32 vcc, 0, v215\n"
"s_nop 1\n"
"v_cndmask_b32 v214, 0, v217\n"
"v_cmp_eq_u32 vcc, 1, v215\n"
"s_nop 1\n"
"v_cndmask_b32 v230, v214, v218\n"
"v_cmp_eq_u32 vcc, 2, v215\n"
"s_nop 1\n"
"v_cndmask_b32 v214, 0, v217\n"
"v_exp_f32 v52, v52\n"
"v_exp_f32_e64 v53, v53\n"
"v_exp_f32 v54, v54\n"
"v_exp_f32 v55, v55\n"
"v_cvt_pk_bf16_f32 v74, v52, v53\n"
"v_cvt_pk_bf16_f32 v75, v54, v55\n"
"v_mfma_f32_32x32x16_bf16 v[32:47], v[124:127], v[88:91], v[32:47]\n"
"v_cmp_eq_u32 vcc, 3, v215\n"
"s_nop 1\n"
"v_cndmask_b32 v231, v214, v218\n"
"v_cmp_eq_u32 vcc, 4, v215\n"
"s_nop 1\n"
"v_cndmask_b32 v214, 0, v217\n"
"v_cmp_eq_u32 vcc, 5, v215\n"
"s_nop 1\n"
"v_cndmask_b32 v232, v214, v218\n"
"v_exp_f32 v56, v56\n"
"v_exp_f32_e64 v57, v57\n"
"v_exp_f32 v58, v58\n"
"v_exp_f32 v59, v59\n"
"v_cvt_pk_bf16_f32 v76, v56, v57\n"
"v_cvt_pk_bf16_f32 v77, v58, v59\n"
"v_mfma_f32_32x32x16_bf16 v[136:151], v[176:179], v[68:71], v[136:151]\n"
"v_cmp_eq_u32 vcc, 6, v215\n"
"s_nop 1\n"
"v_cndmask_b32 v214, 0, v217\n"
"v_cmp_eq_u32 vcc, 7, v215\n"
"s_nop 1\n"
"v_cndmask_b32 v233, v214, v218\n"
"v_mov_b32_e64 v236, v235\n"
"v_add_u32 v237, 147456, v235\n"
"v_exp_f32 v60, v60\n"
"v_exp_f32 v61, v61\n"
"v_exp_f32 v62, v62\n"
"v_exp_f32 v63, v63\n"
"v_cvt_pk_bf16_f32 v78, v60, v61\n"
"v_cvt_pk_bf16_f32 v79, v62, v63\n"
"v_mfma_f32_32x32x16_bf16 v[152:167], v[128:131], v[72:75], v[152:167]\n"
"ds_read_b128 v[128:131], v170 offset:9216\n"
"ds_read_b128 v[132:135], v170 offset:9856\n"
"v_exp_f32 v32, v32\n"
"v_exp_f32 v33, v33\n"
"v_exp_f32 v34, v34\n"
"v_exp_f32 v35, v35\n"
"v_cvt_pk_bf16_f32 v64, v32, v33\n"
"v_cvt_pk_bf16_f32 v65, v34, v35\n"
"v_mfma_f32_32x32x16_bf16 v[48:63], v[120:123], v[84:87], 0\n"
"v_exp_f32 v36, v36\n"
"v_exp_f32 v37, v37\n"
"v_exp_f32 v38, v38\n"
"v_exp_f32 v39, v39\n"
"v_cvt_pk_bf16_f32 v66, v36, v37\n"
"v_cvt_pk_bf16_f32 v67, v38, v39\n"
"v_mfma_f32_32x32x16_bf16 v[48:63], v[124:127], v[104:107], v[48:63]\n"
"ds_read_b128 v[120:123], v168 offset:11520\n"
"v_exp_f32 v40, v40\n"
"v_exp_f32 v41, v41\n"
"v_exp_f32 v42, v42\n"
"v_exp_f32 v43, v43\n"
"v_cvt_pk_bf16_f32 v68, v40, v41\n"
"v_cvt_pk_bf16_f32 v69, v42, v43\n"
"v_mfma_f32_32x32x16_bf16 v[152:167], v[176:179], v[76:79], v[152:167]\n"
"v_exp_f32 v44, v44\n"
"v_exp_f32 v45, v45\n"
"v_exp_f32 v46, v46\n"
"s_waitcnt lgkmcnt(0)\n"
"v_exp_f32_e64 v47, v47\n"
"v_cvt_pk_bf16_f32 v70, v44, v45\n"
"v_cvt_pk_bf16_f32 v71, v46, v47\n"
"v_mfma_f32_32x32x16_bf16 v[136:151], v[128:131], v[64:67], v[136:151]\n"
"v_exp_f32 v48, v48\n"
"v_exp_f32 v49, v49\n"
"v_exp_f32 v50, v50\n"
"v_exp_f32 v51, v51\n"
"v_cvt_pk_bf16_f32 v72, v48, v49\n"
"v_cvt_pk_bf16_f32 v73, v50, v51\n"
"v_mfma_f32_32x32x16_bf16 v[32:47], v[120:123], v[80:83], 0\n"
"v_exp_f32 v52, v52\n"
"v_exp_f32 v53, v53\n"
"v_exp_f32 v54, v54\n"
"v_exp_f32 v55, v55\n"
"v_cvt_pk_bf16_f32 v74, v52, v53\n"
"v_cvt_pk_bf16_f32 v75, v54, v55\n"
"v_mfma_f32_32x32x16_bf16 v[32:47], v[124:127], v[92:95], v[32:47]\n"
"v_exp_f32 v56, v56\n"
"v_exp_f32 v57, v57\n"
"v_exp_f32 v58, v58\n"
"v_exp_f32 v59, v59\n"
"v_cvt_pk_bf16_f32 v76, v56, v57\n"
"v_cvt_pk_bf16_f32 v77, v58, v59\n"
"v_mfma_f32_32x32x16_bf16 v[136:151], v[132:135], v[68:71], v[136:151]\n"
"v_exp_f32 v60, v60\n"
"v_exp_f32 v61, v61\n"
"v_exp_f32 v62, v62\n"
"v_exp_f32 v63, v63\n"
"v_cvt_pk_bf16_f32 v78, v60, v61\n"
"v_cvt_pk_bf16_f32 v79, v62, v63\n"
"v_mfma_f32_32x32x16_bf16 v[152:167], v[128:131], v[72:75], v[152:167]\n"
"ds_read_b128 v[128:131], v170 offset:11520\n"
"ds_read_b128 v[176:179], v170 offset:12160\n"
"v_exp_f32 v32, v32\n"
"v_exp_f32 v33, v33\n"
"v_exp_f32 v34, v34\n"
"v_exp_f32 v35, v35\n"
"v_cvt_pk_bf16_f32 v64, v32, v33\n"
"v_cvt_pk_bf16_f32 v65, v34, v35\n"
"v_mfma_f32_32x32x16_bf16 v[48:63], v[120:123], v[84:87], 0\n"
"v_exp_f32 v36, v36\n"
"v_exp_f32 v37, v37\n"
"v_exp_f32 v38, v38\n"
"v_exp_f32 v39, v39\n"
"v_cvt_pk_bf16_f32 v66, v36, v37\n"
"v_cvt_pk_bf16_f32 v67, v38, v39\n"
"v_mfma_f32_32x32x16_bf16 v[48:63], v[124:127], v[108:111], v[48:63]\n"
"ds_read_b128 v[120:123], v168 offset:13824\n"
"v_exp_f32 v40, v40\n"
"v_exp_f32 v41, v41\n"
"v_exp_f32 v42, v42\n"
"v_exp_f32 v43, v43\n"
"v_cvt_pk_bf16_f32 v68, v40, v41\n"
"v_cvt_pk_bf16_f32 v69, v42, v43\n"
"v_mfma_f32_32x32x16_bf16 v[152:167], v[132:135], v[76:79], v[152:167]\n"
"v_exp_f32 v44, v44\n"
"v_exp_f32 v45, v45\n"
"v_exp_f32 v46, v46\n"
"s_waitcnt lgkmcnt(0)\n"
"v_exp_f32_e64 v47, v47\n"
"v_cvt_pk_bf16_f32 v70, v44, v45\n"
"v_cvt_pk_bf16_f32 v71, v46, v47\n"
"v_mfma_f32_32x32x16_bf16 v[136:151], v[128:131], v[64:67], v[136:151]\n"
"v_exp_f32 v48, v48\n"
"v_exp_f32 v49, v49\n"
"v_exp_f32 v50, v50\n"
"v_exp_f32 v51, v51\n"
"v_cvt_pk_bf16_f32 v72, v48, v49\n"
"v_cvt_pk_bf16_f32 v73, v50, v51\n"
"v_mfma_f32_32x32x16_bf16 v[32:47], v[120:123], v[80:83], 0\n"
"v_exp_f32 v52, v52\n"
"v_exp_f32 v53, v53\n"
"v_exp_f32 v54, v54\n"
"v_exp_f32 v55, v55\n"
"v_cvt_pk_bf16_f32 v74, v52, v53\n"
"v_cvt_pk_bf16_f32 v75, v54, v55\n"
"v_mfma_f32_32x32x16_bf16 v[32:47], v[124:127], v[96:99], v[32:47]\n"
"v_exp_f32 v56, v56\n"
"v_exp_f32 v57, v57\n"
"v_exp_f32 v58, v58\n"
"v_exp_f32 v59, v59\n"
"v_cvt_pk_bf16_f32 v76, v56, v57\n"
"v_cvt_pk_bf16_f32 v77, v58, v59\n"
"v_mfma_f32_32x32x16_bf16 v[136:151], v[176:179], v[68:71], v[136:151]\n"
"v_exp_f32 v60, v60\n"
"v_exp_f32 v61, v61\n"
"v_exp_f32 v62, v62\n"
"v_exp_f32 v63, v63\n"
"v_cvt_pk_bf16_f32 v78, v60, v61\n"
"v_cvt_pk_bf16_f32 v79, v62, v63\n"
"v_mfma_f32_32x32x16_bf16 v[152:167], v[128:131], v[72:75], v[152:167]\n"
"ds_read_b128 v[128:131], v170 offset:13824\n"
"ds_read_b128 v[132:135], v170 offset:14464\n"
"v_exp_f32 v32, v32\n"
"v_exp_f32 v33, v33\n"
"v_exp_f32 v34, v34\n"
"v_exp_f32 v35, v35\n"
"v_cvt_pk_bf16_f32 v64, v32, v33\n"
"v_cvt_pk_bf16_f32 v65, v34, v35\n"
"v_mfma_f32_32x32x16_bf16 v[48:63], v[120:123], v[84:87], 0\n"
"v_exp_f32 v36, v36\n"
"v_exp_f32 v37, v37\n"
"v_exp_f32 v38, v38\n"
"v_exp_f32 v39, v39\n"
"v_cvt_pk_bf16_f32 v66, v36, v37\n"
"v_cvt_pk_bf16_f32 v67, v38, v39\n"
"v_mfma_f32_32x32x16_bf16 v[48:63], v[124:127], v[112:115], v[48:63]\n"
"ds_read_b128 v[120:123], v168 offset:16128\n"
"v_exp_f32 v40, v40\n"
"v_exp_f32 v41, v41\n"
"v_exp_f32 v42, v42\n"
"v_exp_f32 v43, v43\n"
"v_cvt_pk_bf16_f32 v68, v40, v41\n"
"v_cvt_pk_bf16_f32 v69, v42, v43\n"
"v_mfma_f32_32x32x16_bf16 v[152:167], v[176:179], v[76:79], v[152:167]\n"
"v_exp_f32 v44, v44\n"
"v_exp_f32 v45, v45\n"
"v_exp_f32 v46, v46\n"
"s_waitcnt lgkmcnt(0)\n"
"v_exp_f32_e64 v47, v47\n"
"v_cvt_pk_bf16_f32 v70, v44, v45\n"
"v_cvt_pk_bf16_f32 v71, v46, v47\n"
"v_mfma_f32_32x32x16_bf16 v[136:151], v[128:131], v[64:67], v[136:151]\n"
"v_exp_f32 v48, v48\n"
"v_exp_f32 v49, v49\n"
"v_exp_f32 v50, v50\n"
"v_exp_f32 v51, v51\n"
"v_cvt_pk_bf16_f32 v72, v48, v49\n"
"v_cvt_pk_bf16_f32 v73, v50, v51\n"
"v_mfma_f32_32x32x16_bf16 v[32:47], v[120:123], v[80:83], 0\n"
"v_exp_f32 v52, v52\n"
"v_exp_f32 v53, v53\n"
"v_exp_f32 v54, v54\n"
"v_exp_f32 v55, v55\n"
"v_cvt_pk_bf16_f32 v74, v52, v53\n"
"v_cvt_pk_bf16_f32 v75, v54, v55\n"
"v_mfma_f32_32x32x16_bf16 v[32:47], v[124:127], v[100:103], v[32:47]\n"
"v_exp_f32 v56, v56\n"
"v_exp_f32 v57, v57\n"
"v_exp_f32 v58, v58\n"
"v_exp_f32 v59, v59\n"
"v_cvt_pk_bf16_f32 v76, v56, v57\n"
"v_cvt_pk_bf16_f32 v77, v58, v59\n"
"v_mfma_f32_32x32x16_bf16 v[136:151], v[132:135], v[68:71], v[136:151]\n"
"v_exp_f32 v60, v60\n"
"v_exp_f32 v61, v61\n"
"v_exp_f32 v62, v62\n"
"v_exp_f32 v63, v63\n"
"v_cvt_pk_bf16_f32 v78, v60, v61\n"
"v_cvt_pk_bf16_f32 v79, v62, v63\n"
"s_waitcnt vmcnt(36)\n"
"s_waitcnt vmcnt(0)\n"
"s_barrier\n"
"s_lshl_b32 s56, s53, 4\n"
"s_add_u32 s56, s56, s54\n"
"s_lshl_b32 s56, s56, 3\n"
"s_add_u32 s56, s56, 12288\n"
"s_cmp_eq_u32 s52, 0\n"
"s_cselect_b32 s61, s56, 0x7f000000\n"
"v_mov_b32 v250, s62\n"
"v_mov_b32 v251, s63\n"
"v_mov_b32 v253, 0\n"
"buffer_store_dwordx2 v[250:251], v253, s[48:51], s61 offen sc1\n"
"v_mfma_f32_32x32x16_bf16 v[152:167], v[128:131], v[72:75], v[152:167]\n"
"ds_read_b128 v[128:131], v170 offset:16128\n"
"ds_read_b128 v[176:179], v170 offset:16768\n"
"v_exp_f32 v32, v32\n"
"v_exp_f32 v33, v33\n"
"v_exp_f32 v34, v34\n"
"v_exp_f32 v35, v35\n"
"v_cvt_pk_bf16_f32 v64, v32, v33\n"
"v_cvt_pk_bf16_f32 v65, v34, v35\n"
"v_mfma_f32_32x32x16_bf16 v[48:63], v[120:123], v[84:87], 0\n"
"v_exp_f32 v36, v36\n"
"v_exp_f32 v37, v37\n"
"v_exp_f32 v38, v38\n"
"v_exp_f32 v39, v39\n"
"v_cvt_pk_bf16_f32 v66, v36, v37\n"
"v_cvt_pk_bf16_f32 v67, v38, v39\n"
"v_mfma_f32_32x32x16_bf16 v[48:63], v[124:127], v[116:119], v[48:63]\n"
"ds_read_b128 v[120:123], v168 offset:18432\n"
"ds_read_b128 v[124:127], v169 offset:1024\n"
"v_exp_f32 v40, v40\n"
"v_exp_f32 v41, v41\n"
"v_exp_f32 v42, v42\n"
"v_exp_f32 v43, v43\n"
"v_cvt_pk_bf16_f32 v68, v40, v41\n"
"v_cvt_pk_bf16_f32 v69, v42, v43\n"
"v_mfma_f32_32x32x16_bf16 v[152:167], v[132:135], v[76:79], v[152:167]\n"
"v_exp_f32 v44, v44\n"
"v_exp_f32 v45, v45\n"
"v_exp_f32 v46, v46\n"
"s_waitcnt lgkmcnt(0)\n"
"v_exp_f32_e64 v47, v47\n"
"v_cvt_pk_bf16_f32 v70, v44, v45\n"
"v_cvt_pk_bf16_f32 v71, v46, v47\n"
"v_mfma_f32_32x32x16_bf16 v[136:151], v[128:131], v[64:67], v[136:151]\n"
"v_exp_f32 v48, v48\n"
"v_exp_f32 v49, v49\n"
"v_exp_f32 v50, v50\n"
"v_exp_f32 v51, v51\n"
"v_cvt_pk_bf16_f32 v72, v48, v49\n"
"v_cvt_pk_bf16_f32 v73, v50, v51\n"
"v_mfma_f32_32x32x16_bf16 v[32:47], v[120:123], v[80:83], 0\n"
"v_exp_f32 v52, v52\n"
"v_exp_f32 v53, v53\n"
"v_exp_f32 v54, v54\n"
"v_exp_f32 v55, v55\n"
"v_cvt_pk_bf16_f32 v74, v52, v53\n"
"v_cvt_pk_bf16_f32 v75, v54, v55\n"
"v_mfma_f32_32x32x16_bf16 v[32:47], v[124:127], v[88:91], v[32:47]\n"
"v_exp_f32 v56, v56\n"
"v_exp_f32 v57, v57\n"
"v_exp_f32 v58, v58\n"
"v_exp_f32 v59, v59\n"
"v_cvt_pk_bf16_f32 v76, v56, v57\n"
"v_cvt_pk_bf16_f32 v77, v58, v59\n"
"v_mfma_f32_32x32x16_bf16 v[136:151], v[176:179], v[68:71], v[136:151]\n"
"v_exp_f32 v60, v60\n"
"v_exp_f32 v61, v61\n"
"v_exp_f32 v62, v62\n"
"v_exp_f32 v63, v63\n"
"v_cvt_pk_bf16_f32 v78, v60, v61\n"
"v_cvt_pk_bf16_f32 v79, v62, v63\n"
"v_add_u32 v168, 18432, v168\n"
"v_add_u32 v170, 18432, v170\n"
"v_add_u32 v169, 1024, v169\n"
"v_and_b32 v214, 15, v228\n"
"s_lshl_b32 s56, s53, 4\n"
"v_add_u32 v214, s56, v214\n"
"v_lshlrev_b32 v214, 3, v214\n"
"v_add_u32 v253, 12288, v214\n"
"v_mfma_f32_32x32x16_bf16 v[152:167], v[128:131], v[72:75], v[152:167]\n"
"ds_read_b128 v[128:131], v170\n"
"ds_read_b128 v[132:135], v170 offset:640\n"
"v_exp_f32 v32, v32\n"
"v_exp_f32 v33, v33\n"
"v_exp_f32 v34, v34\n"
"v_exp_f32 v35, v35\n"
"v_cvt_pk_bf16_f32 v64, v32, v33\n"
"v_cvt_pk_bf16_f32 v65, v34, v35\n"
"v_mfma_f32_32x32x16_bf16 v[48:63], v[120:123], v[84:87], 0\n"
"v_exp_f32 v36, v36\n"
"v_exp_f32 v37, v37\n"
"v_exp_f32 v38, v38\n"
"v_exp_f32 v39, v39\n"
"v_cvt_pk_bf16_f32 v66, v36, v37\n"
"v_cvt_pk_bf16_f32 v67, v38, v39\n"
"v_mfma_f32_32x32x16_bf16 v[48:63], v[124:127], v[104:107], v[48:63]\n"
"ds_read_b128 v[120:123], v168 offset:2304\n"
"v_exp_f32 v40, v40\n"
"v_exp_f32 v41, v41\n"
"v_exp_f32 v42, v42\n"
"v_exp_f32 v43, v43\n"
"v_cvt_pk_bf16_f32 v68, v40, v41\n"
"v_cvt_pk_bf16_f32 v69, v42, v43\n"
"v_mfma_f32_32x32x16_bf16 v[152:167], v[176:179], v[76:79], v[152:167]\n"
"v_exp_f32 v44, v44\n"
"v_exp_f32 v45, v45\n"
"v_exp_f32 v46, v46\n"
"s_waitcnt lgkmcnt(0)\n"
"v_exp_f32_e64 v47, v47\n"
"v_cvt_pk_bf16_f32 v70, v44, v45\n"
"v_cvt_pk_bf16_f32 v71, v46, v47\n"
"v_mfma_f32_32x32x16_bf16 v[136:151], v[128:131], v[64:67], v[136:151]\n"
"v_exp_f32 v48, v48\n"
"v_exp_f32 v49, v49\n"
"v_exp_f32 v50, v50\n"
"v_exp_f32 v51, v51\n"
"v_cvt_pk_bf16_f32 v72, v48, v49\n"
"v_cvt_pk_bf16_f32 v73, v50, v51\n"
"v_mfma_f32_32x32x16_bf16 v[32:47], v[120:123], v[80:83], 0\n"
"v_exp_f32 v52, v52\n"
"v_exp_f32 v53, v53\n"
"v_exp_f32 v54, v54\n"
"v_exp_f32 v55, v55\n"
"v_cvt_pk_bf16_f32 v74, v52, v53\n"
"v_cvt_pk_bf16_f32 v75, v54, v55\n"
"v_mfma_f32_32x32x16_bf16 v[32:47], v[124:127], v[92:95], v[32:47]\n"
"v_exp_f32 v56, v56\n"
"v_exp_f32 v57, v57\n"
"v_exp_f32 v58, v58\n"
"v_exp_f32 v59, v59\n"
"v_cvt_pk_bf16_f32 v76, v56, v57\n"
"v_cvt_pk_bf16_f32 v77, v58, v59\n"
"v_mfma_f32_32x32x16_bf16 v[136:151], v[132:135], v[68:71], v[136:151]\n"
"v_exp_f32 v60, v60\n"
"v_exp_f32 v61, v61\n"
"v_exp_f32 v62, v62\n"
"v_exp_f32 v63, v63\n"
"v_cvt_pk_bf16_f32 v78, v60, v61\n"
"v_cvt_pk_bf16_f32 v79, v62, v63\n"
"v_mfma_f32_32x32x16_bf16 v[152:167], v[128:131], v[72:75], v[152:167]\n"
"ds_read_b128 v[128:131], v170 offset:2304\n"
"ds_read_b128 v[176:179], v170 offset:2944\n"
"v_exp_f32 v32, v32\n"
"v_exp_f32 v33, v33\n"
"v_exp_f32 v34, v34\n"
"v_exp_f32 v35, v35\n"
"v_cvt_pk_bf16_f32 v64, v32, v33\n"
"v_cvt_pk_bf16_f32 v65, v34, v35\n"
"v_mfma_f32_32x32x16_bf16 v[48:63], v[120:123], v[84:87], 0\n"
"v_exp_f32 v36, v36\n"
"v_exp_f32 v37, v37\n"
"v_exp_f32 v38, v38\n"
"v_exp_f32 v39, v39\n"
"v_cvt_pk_bf16_f32 v66, v36, v37\n"
"v_cvt_pk_bf16_f32 v67, v38, v39\n"
"v_mfma_f32_32x32x16_bf16 v[48:63], v[124:127], v[108:111], v[48:63]\n"
"ds_read_b128 v[120:123], v168 offset:4608\n"
"v_exp_f32 v40, v40\n"
"v_exp_f32 v41, v41\n"
"v_exp_f32 v42, v42\n"
"v_exp_f32 v43, v43\n"
"v_cvt_pk_bf16_f32 v68, v40, v41\n"
"v_cvt_pk_bf16_f32 v69, v42, v43\n"
"v_mfma_f32_32x32x16_bf16 v[152:167], v[132:135], v[76:79], v[152:167]\n"
"v_exp_f32 v44, v44\n"
"v_exp_f32 v45, v45\n"
"v_exp_f32 v46, v46\n"
"s_waitcnt lgkmcnt(0)\n"
"v_exp_f32_e64 v47, v47\n"
"v_cvt_pk_bf16_f32 v70, v44, v45\n"
"v_cvt_pk_bf16_f32 v71, v46, v47\n"
"v_mfma_f32_32x32x16_bf16 v[136:151], v[128:131], v[64:67], v[136:151]\n"
"v_exp_f32 v48, v48\n"
"v_exp_f32 v49, v49\n"
"v_exp_f32 v50, v50\n"
"v_exp_f32 v51, v51\n"
"v_cvt_pk_bf16_f32 v72, v48, v49\n"
"v_cvt_pk_bf16_f32 v73, v50, v51\n"
"v_mfma_f32_32x32x16_bf16 v[32:47], v[120:123], v[80:83], 0\n"
"v_exp_f32 v52, v52\n"
"v_exp_f32 v53, v53\n"
"v_exp_f32 v54, v54\n"
"v_exp_f32 v55, v55\n"
"v_cvt_pk_bf16_f32 v74, v52, v53\n"
"v_cvt_pk_bf16_f32 v75, v54, v55\n"
"v_mfma_f32_32x32x16_bf16 v[32:47], v[124:127], v[96:99], v[32:47]\n"
"v_exp_f32 v56, v56\n"
"v_exp_f32 v57, v57\n"
"v_exp_f32 v58, v58\n"
"v_exp_f32 v59, v59\n"
"v_cvt_pk_bf16_f32 v76, v56, v57\n"
"v_cvt_pk_bf16_f32 v77, v58, v59\n"
"v_mfma_f32_32x32x16_bf16 v[136:151], v[176:179], v[68:71], v[136:151]\n"
"v_exp_f32 v60, v60\n"
"v_exp_f32 v61, v61\n"
"v_exp_f32 v62, v62\n"
"v_exp_f32 v63, v63\n"
"v_cvt_pk_bf16_f32 v78, v60, v61\n"
"v_cvt_pk_bf16_f32 v79, v62, v63\n"
"v_mfma_f32_32x32x16_bf16 v[152:167], v[128:131], v[72:75], v[152:167]\n"
"ds_read_b128 v[128:131], v170 offset:4608\n"
"ds_read_b128 v[132:135], v170 offset:5248\n"
"v_exp_f32 v32, v32\n"
"v_exp_f32 v33, v33\n"
"v_exp_f32 v34, v34\n"
"v_exp_f32 v35, v35\n"
"v_cvt_pk_bf16_f32 v64, v32, v33\n"
"v_cvt_pk_bf16_f32 v65, v34, v35\n"
"v_mfma_f32_32x32x16_bf16 v[48:63], v[120:123], v[84:87], 0\n"
"v_exp_f32 v36, v36\n"
"v_exp_f32 v37, v37\n"
"v_exp_f32 v38, v38\n"
"v_exp_f32 v39, v39\n"
"v_cvt_pk_bf16_f32 v66, v36, v37\n"
"v_cvt_pk_bf16_f32 v67, v38, v39\n"
"v_mfma_f32_32x32x16_bf16 v[48:63], v[124:127], v[112:115], v[48:63]\n"
"ds_read_b128 v[120:123], v168 offset:6912\n"
"v_exp_f32 v40, v40\n"
"v_exp_f32 v41, v41\n"
"v_exp_f32 v42, v42\n"
"v_exp_f32 v43, v43\n"
"v_cvt_pk_bf16_f32 v68, v40, v41\n"
"v_cvt_pk_bf16_f32 v69, v42, v43\n"
"v_mfma_f32_32x32x16_bf16 v[152:167], v[176:179], v[76:79], v[152:167]\n"
"v_exp_f32 v44, v44\n"
"v_exp_f32 v45, v45\n"
"v_exp_f32 v46, v46\n"
"s_waitcnt lgkmcnt(0)\n"
"v_exp_f32_e64 v47, v47\n"
"v_cvt_pk_bf16_f32 v70, v44, v45\n"
"v_cvt_pk_bf16_f32 v71, v46, v47\n"
"v_mfma_f32_32x32x16_bf16 v[136:151], v[128:131], v[64:67], v[136:151]\n"
"v_exp_f32 v48, v48\n"
"v_exp_f32 v49, v49\n"
"v_exp_f32 v50, v50\n"
"v_exp_f32 v51, v51\n"
"v_cvt_pk_bf16_f32 v72, v48, v49\n"
"v_cvt_pk_bf16_f32 v73, v50, v51\n"
"v_mfma_f32_32x32x16_bf16 v[32:47], v[120:123], v[80:83], 0\n"
"v_exp_f32 v52, v52\n"
"v_exp_f32 v53, v53\n"
"v_exp_f32 v54, v54\n"
"v_exp_f32 v55, v55\n"
"v_cvt_pk_bf16_f32 v74, v52, v53\n"
"v_cvt_pk_bf16_f32 v75, v54, v55\n"
"v_mfma_f32_32x32x16_bf16 v[32:47], v[124:127], v[100:103], v[32:47]\n"
"v_exp_f32 v56, v56\n"
"v_exp_f32 v57, v57\n"
"v_exp_f32 v58, v58\n"
"v_exp_f32 v59, v59\n"
"v_cvt_pk_bf16_f32 v76, v56, v57\n"
"v_cvt_pk_bf16_f32 v77, v58, v59\n"
"v_mfma_f32_32x32x16_bf16 v[136:151], v[132:135], v[68:71], v[136:151]\n"
"v_exp_f32 v60, v60\n"
"v_exp_f32 v61, v61\n"
"v_exp_f32 v62, v62\n"
"v_exp_f32 v63, v63\n"
"v_cvt_pk_bf16_f32 v78, v60, v61\n"
"v_cvt_pk_bf16_f32 v79, v62, v63\n"
"buffer_load_dwordx2 v[250:251], v253, s[48:51], 0 offen sc1\n"
"s_or_b32 s56, s53, s54\n"
"s_or_b32 s56, s56, s52\n"
"s_cmp_eq_u32 s56, 0\n"
"s_cbranch_scc0 LGI_%=\n"
"v_lshlrev_b32 v220, 3, v228\n"
"v_add_u32 v220, 1024, v220\n"
"buffer_load_dwordx2 v[212:213], v220, s[48:51], 0 offen sc1\n"
"v_lshlrev_b32 v221, 3, v228\n"
"v_add_u32 v221, 1536, v221\n"
"buffer_load_dwordx2 v[214:215], v221, s[48:51], 0 offen sc1\n"
"v_lshlrev_b32 v222, 3, v228\n"
"v_add_u32 v222, 2048, v222\n"
"buffer_load_dwordx2 v[216:217], v222, s[48:51], 0 offen sc1\n"
"v_lshlrev_b32 v223, 3, v228\n"
"v_add_u32 v223, 2560, v223\n"
"buffer_load_dwordx2 v[218:219], v223, s[48:51], 0 offen sc1\n"
"LGI_%=:\n"
"v_mfma_f32_32x32x16_bf16 v[152:167], v[128:131], v[72:75], v[152:167]\n"
"ds_read_b128 v[128:131], v170 offset:6912\n"
"ds_read_b128 v[176:179], v170 offset:7552\n"
"v_exp_f32 v32, v32\n"
"v_exp_f32 v33, v33\n"
"v_exp_f32 v34, v34\n"
"v_exp_f32 v35, v35\n"
"v_cvt_pk_bf16_f32 v64, v32, v33\n"
"v_cvt_pk_bf16_f32 v65, v34, v35\n"
"v_mfma_f32_32x32x16_bf16 v[48:63], v[120:123], v[84:87], 0\n"
"v_exp_f32 v36, v36\n"
"v_exp_f32 v37, v37\n"
"v_exp_f32 v38, v38\n"
"v_exp_f32 v39, v39\n"
"v_cvt_pk_bf16_f32 v66, v36, v37\n"
"v_cvt_pk_bf16_f32 v67, v38, v39\n"
"v_mfma_f32_32x32x16_bf16 v[48:63], v[124:127], v[116:119], v[48:63]\n"
"ds_read_b128 v[120:123], v168 offset:9216\n"
"ds_read_b128 v[124:127], v169 offset:512\n"
"v_exp_f32 v40, v40\n"
"v_exp_f32 v41, v41\n"
"v_exp_f32 v42, v42\n"
"v_exp_f32 v43, v43\n"
"v_cvt_pk_bf16_f32 v68, v40, v41\n"
"v_cvt_pk_bf16_f32 v69, v42, v43\n"
"v_mfma_f32_32x32x16_bf16 v[152:167], v[132:135], v[76:79], v[152:167]\n"
"v_exp_f32 v44, v44\n"
"v_exp_f32 v45, v45\n"
"v_exp_f32 v46, v46\n"
"s_waitcnt lgkmcnt(0)\n"
"v_exp_f32_e64 v47, v47\n"
"v_cvt_pk_bf16_f32 v70, v44, v45\n"
"v_cvt_pk_bf16_f32 v71, v46, v47\n"
"v_mfma_f32_32x32x16_bf16 v[136:151], v[128:131], v[64:67], v[136:151]\n"
"v_exp_f32 v48, v48\n"
"v_exp_f32 v49, v49\n"
"v_exp_f32 v50, v50\n"
"v_exp_f32 v51, v51\n"
"v_cvt_pk_bf16_f32 v72, v48, v49\n"
"v_cvt_pk_bf16_f32 v73, v50, v51\n"
"v_mfma_f32_32x32x16_bf16 v[32:47], v[120:123], v[80:83], 0\n"
"v_exp_f32 v52, v52\n"
"v_exp_f32 v53, v53\n"
"v_exp_f32 v54, v54\n"
"v_exp_f32 v55, v55\n"
"v_cvt_pk_bf16_f32 v74, v52, v53\n"
"v_cvt_pk_bf16_f32 v75, v54, v55\n"
"v_mfma_f32_32x32x16_bf16 v[32:47], v[124:127], v[88:91], v[32:47]\n"
"v_exp_f32 v56, v56\n"
"v_exp_f32 v57, v57\n"
"v_exp_f32 v58, v58\n"
"v_exp_f32 v59, v59\n"
"v_cvt_pk_bf16_f32 v76, v56, v57\n"
"v_cvt_pk_bf16_f32 v77, v58, v59\n"
"v_mfma_f32_32x32x16_bf16 v[136:151], v[176:179], v[68:71], v[136:151]\n"
"v_exp_f32 v60, v60\n"
"v_exp_f32 v61, v61\n"
"v_exp_f32 v62, v62\n"
"v_exp_f32 v63, v63\n"
"v_cvt_pk_bf16_f32 v78, v60, v61\n"
"v_cvt_pk_bf16_f32 v79, v62, v63\n"
"v_mfma_f32_32x32x16_bf16 v[152:167], v[128:131], v[72:75], v[152:167]\n"
"ds_read_b128 v[128:131], v170 offset:9216\n"
"ds_read_b128 v[132:135], v170 offset:9856\n"
"v_exp_f32 v32, v32\n"
"v_exp_f32 v33, v33\n"
"v_exp_f32 v34, v34\n"
"v_exp_f32 v35, v35\n"
"v_cvt_pk_bf16_f32 v64, v32, v33\n"
"v_cvt_pk_bf16_f32 v65, v34, v35\n"
"v_mfma_f32_32x32x16_bf16 v[48:63], v[120:123], v[84:87], 0\n"
"v_exp_f32 v36, v36\n"
"v_exp_f32 v37, v37\n"
"v_exp_f32 v38, v38\n"
"v_exp_f32 v39, v39\n"
"v_cvt_pk_bf16_f32 v66, v36, v37\n"
"v_cvt_pk_bf16_f32 v67, v38, v39\n"
"v_mfma_f32_32x32x16_bf16 v[48:63], v[124:127], v[104:107], v[48:63]\n"
"ds_read_b128 v[120:123], v168 offset:11520\n"
"v_exp_f32 v40, v40\n"
"v_exp_f32 v41, v41\n"
"v_exp_f32 v42, v42\n"
"v_exp_f32 v43, v43\n"
"v_cvt_pk_bf16_f32 v68, v40, v41\n"
"v_cvt_pk_bf16_f32 v69, v42, v43\n"
"v_mfma_f32_32x32x16_bf16 v[152:167], v[176:179], v[76:79], v[152:167]\n"
"v_exp_f32 v44, v44\n"
"v_exp_f32 v45, v45\n"
"v_exp_f32 v46, v46\n"
"s_waitcnt lgkmcnt(0)\n"
"v_exp_f32_e64 v47, v47\n"
"v_cvt_pk_bf16_f32 v70, v44, v45\n"
"v_cvt_pk_bf16_f32 v71, v46, v47\n"
"v_mfma_f32_32x32x16_bf16 v[136:151], v[128:131], v[64:67], v[136:151]\n"
"v_exp_f32 v48, v48\n"
"v_exp_f32 v49, v49\n"
"v_exp_f32 v50, v50\n"
"v_exp_f32 v51, v51\n"
"v_cvt_pk_bf16_f32 v72, v48, v49\n"
"v_cvt_pk_bf16_f32 v73, v50, v51\n"
"v_mfma_f32_32x32x16_bf16 v[32:47], v[120:123], v[80:83], 0\n"
"v_exp_f32 v52, v52\n"
"v_exp_f32 v53, v53\n"
"v_exp_f32 v54, v54\n"
"v_exp_f32 v55, v55\n"
"v_cvt_pk_bf16_f32 v74, v52, v53\n"
"v_cvt_pk_bf16_f32 v75, v54, v55\n"
"v_mfma_f32_32x32x16_bf16 v[32:47], v[124:127], v[92:95], v[32:47]\n"
"v_exp_f32 v56, v56\n"
"v_exp_f32 v57, v57\n"
"v_exp_f32 v58, v58\n"
"v_exp_f32 v59, v59\n"
"v_cvt_pk_bf16_f32 v76, v56, v57\n"
"v_cvt_pk_bf16_f32 v77, v58, v59\n"
"v_mfma_f32_32x32x16_bf16 v[136:151], v[132:135], v[68:71], v[136:151]\n"
"v_exp_f32 v60, v60\n"
"v_exp_f32 v61, v61\n"
"v_exp_f32 v62, v62\n"
"v_exp_f32 v63, v63\n"
"v_cvt_pk_bf16_f32 v78, v60, v61\n"
"v_cvt_pk_bf16_f32 v79, v62, v63\n"
"v_mfma_f32_32x32x16_bf16 v[152:167], v[128:131], v[72:75], v[152:167]\n"
"ds_read_b128 v[128:131], v170 offset:11520\n"
"ds_read_b128 v[176:179], v170 offset:12160\n"
"v_exp_f32 v32, v32\n"
"v_exp_f32 v33, v33\n"
"v_exp_f32 v34, v34\n"
"v_exp_f32 v35, v35\n"
"v_cvt_pk_bf16_f32 v64, v32, v33\n"
"v_cvt_pk_bf16_f32 v65, v34, v35\n"
"v_mfma_f32_32x32x16_bf16 v[48:63], v[120:123], v[84:87], 0\n"
"v_exp_f32 v36, v36\n"
"v_exp_f32 v37, v37\n"
"v_exp_f32 v38, v38\n"
"v_exp_f32 v39, v39\n"
"v_cvt_pk_bf16_f32 v66, v36, v37\n"
"v_cvt_pk_bf16_f32 v67, v38, v39\n"
"v_mfma_f32_32x32x16_bf16 v[48:63], v[124:127], v[108:111], v[48:63]\n"
"ds_read_b128 v[120:123], v168 offset:13824\n"
"v_exp_f32 v40, v40\n"
"v_exp_f32 v41, v41\n"
"v_exp_f32 v42, v42\n"
"v_exp_f32 v43, v43\n"
"v_cvt_pk_bf16_f32 v68, v40, v41\n"
"v_cvt_pk_bf16_f32 v69, v42, v43\n"
"v_mfma_f32_32x32x16_bf16 v[152:167], v[132:135], v[76:79], v[152:167]\n"
"v_exp_f32 v44, v44\n"
"v_exp_f32 v45, v45\n"
"v_exp_f32 v46, v46\n"
"s_waitcnt lgkmcnt(0)\n"
"v_exp_f32_e64 v47, v47\n"
"v_cvt_pk_bf16_f32 v70, v44, v45\n"
"v_cvt_pk_bf16_f32 v71, v46, v47\n"
"v_mfma_f32_32x32x16_bf16 v[136:151], v[128:131], v[64:67], v[136:151]\n"
"v_exp_f32 v48, v48\n"
"v_exp_f32 v49, v49\n"
"v_exp_f32 v50, v50\n"
"v_exp_f32 v51, v51\n"
"v_cvt_pk_bf16_f32 v72, v48, v49\n"
"v_cvt_pk_bf16_f32 v73, v50, v51\n"
"v_mfma_f32_32x32x16_bf16 v[32:47], v[120:123], v[80:83], 0\n"
"v_exp_f32 v52, v52\n"
"v_exp_f32 v53, v53\n"
"v_exp_f32 v54, v54\n"
"v_exp_f32 v55, v55\n"
"v_cvt_pk_bf16_f32 v74, v52, v53\n"
"v_cvt_pk_bf16_f32 v75, v54, v55\n"
"v_mfma_f32_32x32x16_bf16 v[32:47], v[124:127], v[96:99], v[32:47]\n"
"v_exp_f32 v56, v56\n"
"v_exp_f32 v57, v57\n"
"v_exp_f32 v58, v58\n"
"v_exp_f32 v59, v59\n"
"v_cvt_pk_bf16_f32 v76, v56, v57\n"
"v_cvt_pk_bf16_f32 v77, v58, v59\n"
"v_mfma_f32_32x32x16_bf16 v[136:151], v[176:179], v[68:71], v[136:151]\n"
"v_exp_f32 v60, v60\n"
"v_exp_f32 v61, v61\n"
"v_exp_f32 v62, v62\n"
"v_exp_f32 v63, v63\n"
"v_cvt_pk_bf16_f32 v78, v60, v61\n"
"v_cvt_pk_bf16_f32 v79, v62, v63\n"
"v_mfma_f32_32x32x16_bf16 v[152:167], v[128:131], v[72:75], v[152:167]\n"
"ds_read_b128 v[128:131], v170 offset:13824\n"
"ds_read_b128 v[132:135], v170 offset:14464\n"
"v_exp_f32 v32, v32\n"
"v_exp_f32 v33, v33\n"
"v_exp_f32 v34, v34\n"
"v_exp_f32 v35, v35\n"
"v_cvt_pk_bf16_f32 v64, v32, v33\n"
"v_cvt_pk_bf16_f32 v65, v34, v35\n"
"v_mfma_f32_32x32x16_bf16 v[48:63], v[120:123], v[84:87], 0\n"
"v_exp_f32 v36, v36\n"
"v_exp_f32 v37, v37\n"
"v_exp_f32 v38, v38\n"
"v_exp_f32 v39, v39\n"
"v_cvt_pk_bf16_f32 v66, v36, v37\n"
"v_cvt_pk_bf16_f32 v67, v38, v39\n"
"v_mfma_f32_32x32x16_bf16 v[48:63], v[124:127], v[112:115], v[48:63]\n"
"ds_read_b128 v[120:123], v168 offset:16128\n"
"v_exp_f32 v40, v40\n"
"v_exp_f32 v41, v41\n"
"v_exp_f32 v42, v42\n"
"v_exp_f32 v43, v43\n"
"v_cvt_pk_bf16_f32 v68, v40, v41\n"
"v_cvt_pk_bf16_f32 v69, v42, v43\n"
"v_mfma_f32_32x32x16_bf16 v[152:167], v[176:179], v[76:79], v[152:167]\n"
"v_exp_f32 v44, v44\n"
"v_exp_f32 v45, v45\n"
"v_exp_f32 v46, v46\n"
"s_waitcnt lgkmcnt(0)\n"
"v_exp_f32_e64 v47, v47\n"
"v_cvt_pk_bf16_f32 v70, v44, v45\n"
"v_cvt_pk_bf16_f32 v71, v46, v47\n"
"v_mfma_f32_32x32x16_bf16 v[136:151], v[128:131], v[64:67], v[136:151]\n"
"v_exp_f32 v48, v48\n"
"v_exp_f32 v49, v49\n"
"v_exp_f32 v50, v50\n"
"v_exp_f32 v51, v51\n"
"v_cvt_pk_bf16_f32 v72, v48, v49\n"
"v_cvt_pk_bf16_f32 v73, v50, v51\n"
"v_mfma_f32_32x32x16_bf16 v[32:47], v[120:123], v[80:83], 0\n"
"v_exp_f32 v52, v52\n"
"v_exp_f32 v53, v53\n"
"v_exp_f32 v54, v54\n"
"v_exp_f32 v55, v55\n"
"v_cvt_pk_bf16_f32 v74, v52, v53\n"
"v_cvt_pk_bf16_f32 v75, v54, v55\n"
"v_mfma_f32_32x32x16_bf16 v[32:47], v[124:127], v[100:103], v[32:47]\n"
"v_exp_f32 v56, v56\n"
"v_exp_f32 v57, v57\n"
"v_exp_f32 v58, v58\n"
"v_exp_f32 v59, v59\n"
"v_cvt_pk_bf16_f32 v76, v56, v57\n"
"v_cvt_pk_bf16_f32 v77, v58, v59\n"
"v_mfma_f32_32x32x16_bf16 v[136:151], v[132:135], v[68:71], v[136:151]\n"
"v_exp_f32 v60, v60\n"
"v_exp_f32 v61, v61\n"
"v_exp_f32 v62, v62\n"
"v_exp_f32 v63, v63\n"
"v_cvt_pk_bf16_f32 v78, v60, v61\n"
"v_cvt_pk_bf16_f32 v79, v62, v63\n"
"v_mfma_f32_32x32x16_bf16 v[152:167], v[128:131], v[72:75], v[152:167]\n"
"ds_read_b128 v[128:131], v170 offset:16128\n"
"ds_read_b128 v[176:179], v170 offset:16768\n"
"v_exp_f32 v32, v32\n"
"v_exp_f32 v33, v33\n"
"v_exp_f32 v34, v34\n"
"v_exp_f32 v35, v35\n"
"v_cvt_pk_bf16_f32 v64, v32, v33\n"
"v_cvt_pk_bf16_f32 v65, v34, v35\n"
"v_mfma_f32_32x32x16_bf16 v[48:63], v[120:123], v[84:87], 0\n"
"v_exp_f32 v36, v36\n"
"v_exp_f32 v37, v37\n"
"v_exp_f32 v38, v38\n"
"v_exp_f32 v39, v39\n"
"v_cvt_pk_bf16_f32 v66, v36, v37\n"
"v_cvt_pk_bf16_f32 v67, v38, v39\n"
"v_mfma_f32_32x32x16_bf16 v[48:63], v[124:127], v[116:119], v[48:63]\n"
"ds_read_b128 v[120:123], v168 offset:18432\n"
"ds_read_b128 v[124:127], v169 offset:1024\n"
"v_exp_f32 v40, v40\n"
"v_exp_f32 v41, v41\n"
"v_exp_f32 v42, v42\n"
"v_exp_f32 v43, v43\n"
"v_cvt_pk_bf16_f32 v68, v40, v41\n"
"v_cvt_pk_bf16_f32 v69, v42, v43\n"
"v_mfma_f32_32x32x16_bf16 v[152:167], v[132:135], v[76:79], v[152:167]\n"
"v_exp_f32 v44, v44\n"
"v_exp_f32 v45, v45\n"
"v_exp_f32 v46, v46\n"
"s_waitcnt lgkmcnt(0)\n"
"v_exp_f32_e64 v47, v47\n"
"v_cvt_pk_bf16_f32 v70, v44, v45\n"
"v_cvt_pk_bf16_f32 v71, v46, v47\n"
"v_mfma_f32_32x32x16_bf16 v[136:151], v[128:131], v[64:67], v[136:151]\n"
"v_exp_f32 v48, v48\n"
"v_exp_f32 v49, v49\n"
"v_exp_f32 v50, v50\n"
"v_exp_f32 v51, v51\n"
"v_cvt_pk_bf16_f32 v72, v48, v49\n"
"v_cvt_pk_bf16_f32 v73, v50, v51\n"
"v_mfma_f32_32x32x16_bf16 v[32:47], v[120:123], v[80:83], 0\n"
"v_exp_f32 v52, v52\n"
"v_exp_f32 v53, v53\n"
"v_exp_f32 v54, v54\n"
"v_exp_f32 v55, v55\n"
"v_cvt_pk_bf16_f32 v74, v52, v53\n"
"v_cvt_pk_bf16_f32 v75, v54, v55\n"
"v_mfma_f32_32x32x16_bf16 v[32:47], v[124:127], v[88:91], v[32:47]\n"
"v_exp_f32 v56, v56\n"
"v_exp_f32 v57, v57\n"
"v_exp_f32 v58, v58\n"
"v_exp_f32 v59, v59\n"
"v_cvt_pk_bf16_f32 v76, v56, v57\n"
"v_cvt_pk_bf16_f32 v77, v58, v59\n"
"v_mfma_f32_32x32x16_bf16 v[136:151], v[176:179], v[68:71], v[136:151]\n"
"v_exp_f32 v60, v60\n"
"v_exp_f32 v61, v61\n"
"v_exp_f32 v62, v62\n"
"v_exp_f32 v63, v63\n"
"v_cvt_pk_bf16_f32 v78, v60, v61\n"
"v_cvt_pk_bf16_f32 v79, v62, v63\n"
"v_add_u32 v168, 18432, v168\n"
"v_add_u32 v170, 18432, v170\n"
"v_add_u32 v169, 1024, v169\n"
"s_waitcnt vmcnt(0)\n"
"s_or_b32 s56, s53, s54\n"
"s_or_b32 s56, s56, s52\n"
"s_cmp_eq_u32 s56, 0\n"
"s_cbranch_scc0 LGC_%=\n"
"v_cmp_eq_u64 vcc, s[62:63], v[212:213]\n"
"s_nop 3\n"
"s_cmp_eq_u64 vcc, exec\n"
"s_cbranch_scc1 LGC0_OK_%=\n"
"s_mov_b32 s56, 0x4000\n"
"LGC0_%=:\n"
"s_sleep 8\n"
"buffer_load_dwordx2 v[212:213], v220, s[48:51], 0 offen sc1\n"
"s_waitcnt vmcnt(0)\n"
"v_cmp_eq_u64 vcc, s[62:63], v[212:213]\n"
"s_nop 3\n"
"s_cmp_eq_u64 vcc, exec\n"
"s_cbranch_scc1 LGC0_OK_%=\n"
"s_sub_u32 s56, s56, 1\n"
"s_cmp_lg_u32 s56, 0\n"
"s_cbranch_scc1 LGC0_%=\n"
"s_or_b32 s75, s75, 2\n"
"LGC0_OK_%=:\n"
"v_cmp_eq_u64 vcc, s[62:63], v[214:215]\n"
"s_nop 3\n"
"s_cmp_eq_u64 vcc, exec\n"
"s_cbranch_scc1 LGC1_OK_%=\n"
"s_mov_b32 s56, 0x4000\n"
"LGC1_%=:\n"
"s_sleep 8\n"
"buffer_load_dwordx2 v[214:215], v221, s[48:51], 0 offen sc1\n"
"s_waitcnt vmcnt(0)\n"
"v_cmp_eq_u64 vcc, s[62:63], v[214:215]\n"
"s_nop 3\n"
"s_cmp_eq_u64 vcc, exec\n"
"s_cbranch_scc1 LGC1_OK_%=\n"
"s_sub_u32 s56, s56, 1\n"
"s_cmp_lg_u32 s56, 0\n"
"s_cbranch_scc1 LGC1_%=\n"
"s_or_b32 s75, s75, 2\n"
"LGC1_OK_%=:\n"
"v_cmp_eq_u64 vcc, s[62:63], v[216:217]\n"
"s_nop 3\n"
"s_cmp_eq_u64 vcc, exec\n"
"s_cbranch_scc1 LGC2_OK_%=\n"
"s_mov_b32 s56, 0x4000\n"
"LGC2_%=:\n"
"s_sleep 8\n"
"buffer_load_dwordx2 v[216:217], v222, s[48:51], 0 offen sc1\n"
"s_waitcnt vmcnt(0)\n"
"v_cmp_eq_u64 vcc, s[62:63], v[216:217]\n"
"s_nop 3\n"
"s_cmp_eq_u64 vcc, exec\n"
"s_cbranch_scc1 LGC2_OK_%=\n"
"s_sub_u32 s56, s56, 1\n"
"s_cmp_lg_u32 s56, 0\n"
"s_cbranch_scc1 LGC2_%=\n"
"s_or_b32 s75, s75, 2\n"
"LGC2_OK_%=:\n"
"v_cmp_eq_u64 vcc, s[62:63], v[218:219]\n"
"s_nop 3\n"
"s_cmp_eq_u64 vcc, exec\n"
"s_cbranch_scc1 LGC3_OK_%=\n"
"s_mov_b32 s56, 0x4000\n"
"LGC3_%=:\n"
"s_sleep 8\n"
"buffer_load_dwordx2 v[218:219], v223, s[48:51], 0 offen sc1\n"
"s_waitcnt vmcnt(0)\n"
"v_cmp_eq_u64 vcc, s[62:63], v[218:219]\n"
"s_nop 3\n"
"s_cmp_eq_u64 vcc, exec\n"
"s_cbranch_scc1 LGC3_OK_%=\n"
"s_sub_u32 s56, s56, 1\n"
"s_cmp_lg_u32 s56, 0\n"
"s_cbranch_scc1 LGC3_%=\n"
"s_or_b32 s75, s75, 2\n"
"LGC3_OK_%=:\n"
"v_mov_b32 v212, s62\n"
"v_mov_b32 v213, s63\n"
"v_mov_b32 v220, 0\n"
"buffer_store_dwordx2 v[212:213], v220, s[48:51], 0 offen sc1\n"
"s_waitcnt vmcnt(0)\n"
"LGC_%=:\n"
"v_cmp_eq_u64 vcc, s[62:63], v[250:251]\n"
"s_nop 3\n"
"s_cmp_eq_u64 vcc, exec\n"
"s_cbranch_scc1 LPOLL2_OK_%=\n"
"s_mov_b32 s56, 0x4000\n"
"LPOLL2_%=:\n"
"s_sleep 8\n"
"buffer_load_dwordx2 v[250:251], v253, s[48:51], 0 offen sc1\n"
"s_waitcnt vmcnt(0)\n"
"v_cmp_eq_u64 vcc, s[62:63], v[250:251]\n"
"s_nop 3\n"
"s_cmp_eq_u64 vcc, exec\n"
"s_cbranch_scc1 LPOLL2_OK_%=\n"
"s_sub_u32 s56, s56, 1\n"
"s_cmp_lg_u32 s56, 0\n"
"s_cbranch_scc1 LPOLL2_%=\n"
"s_or_b32 s75, s75, 2\n"
"LPOLL2_OK_%=:\n"
"s_mov_b32 s64, 0\n"
"s_mov_b32 s68, 0x7f000000\n"
"s_mov_b32 s74, 4\n"
"LRED_%=:\n"
"s_mov_b32 s66, s68\n"
"s_lshl_b32 s56, s64, 2\n"
"s_add_u32 s56, s56, s52\n"
"s_mul_i32 s57, s54, 9\n"
"s_add_u32 s57, s57, s56\n"
"s_mul_i32 s58, s53, 2359296\n"
"s_lshl_b32 s59, s57, 10\n"
"s_add_u32 s58, s58, s59\n"
"s_add_u32 s58, s58, 9715712\n"
"s_mul_i32 s59, s53, 144\n"
"s_add_u32 s59, s59, s57\n"
"s_lshl_b32 s59, s59, 11\n"
"s_add_u32 s59, s59, 47464448\n"
"s_cmp_lt_u32 s56, 9\n"
"s_cselect_b32 s65, s58, 0x7f000000\n"
"s_cselect_b32 s68, s59, 0x7f000000\n"
"s_add_u32 s64, s64, 1\n"
"v_mfma_f32_32x32x16_bf16 v[152:167], v[128:131], v[72:75], v[152:167]\n"
"ds_read_b128 v[128:131], v170\n"
"ds_read_b128 v[132:135], v170 offset:640\n"
"s_waitcnt vmcnt(16)\n"
"v_exp_f32 v32, v32\n"
"v_exp_f32_e64 v33, v33\n"
"v_mfma_f32_32x32x16_bf16 v[212:227], v[230:233], v[0:3], 0\n"
"v_exp_f32 v34, v34\n"
"v_exp_f32 v35, v35\n"
"v_cvt_pk_bf16_f32 v64, v32, v33\n"
"v_cvt_pk_bf16_f32 v65, v34, v35\n"
"v_mfma_f32_32x32x16_bf16 v[48:63], v[120:123], v[84:87], 0\n"
"buffer_load_dwordx4 v[0:3], v236, s[48:51], s65 offen sc1\n"
"v_exp_f32 v36, v36\n"
"v_exp_f32 v37, v37\n"
"v_exp_f32 v38, v38\n"
"v_exp_f32 v39, v39\n"
"v_cvt_pk_bf16_f32 v66, v36, v37\n"
"v_cvt_pk_bf16_f32 v67, v38, v39\n"
"v_mfma_f32_32x32x16_bf16 v[48:63], v[124:127], v[104:107], v[48:63]\n"
"ds_read_b128 v[120:123], v168 offset:2304\n"
"v_exp_f32 v40, v40\n"
"v_exp_f32 v41, v41\n"
"v_mfma_f32_32x32x16_bf16 v[212:227], v[230:233], v[4:7], v[212:227]\n"
"v_exp_f32 v42, v42\n"
"v_exp_f32 v43, v43\n"
"v_cvt_pk_bf16_f32 v68, v40, v41\n"
"v_cvt_pk_bf16_f32 v69, v42, v43\n"
"v_mfma_f32_32x32x16_bf16 v[152:167], v[176:179], v[76:79], v[152:167]\n"
"buffer_load_dwordx4 v[4:7], v237, s[48:51], s65 offen sc1\n"
"s_add_u32 s65, s65, 294912\n"
"v_exp_f32 v44, v44\n"
"v_exp_f32 v45, v45\n"
"v_exp_f32 v46, v46\n"
"s_waitcnt lgkmcnt(0)\n"
"v_exp_f32_e64 v47, v47\n"
"v_cvt_pk_bf16_f32 v70, v44, v45\n"
"v_cvt_pk_bf16_f32 v71, v46, v47\n"
"v_mfma_f32_32x32x16_bf16 v[136:151], v[128:131], v[64:67], v[136:151]\n"
"v_exp_f32 v48, v48\n"
"v_exp_f32 v49, v49\n"
"v_exp_f32 v50, v50\n"
"v_exp_f32 v51, v51\n"
"v_cvt_pk_bf16_f32 v72, v48, v49\n"
"v_cvt_pk_bf16_f32 v73, v50, v51\n"
"v_mfma_f32_32x32x16_bf16 v[32:47], v[120:123], v[80:83], 0\n"
"v_exp_f32 v52, v52\n"
"v_exp_f32 v53, v53\n"
"v_exp_f32 v54, v54\n"
"v_exp_f32 v55, v55\n"
"v_cvt_pk_bf16_f32 v74, v52, v53\n"
"v_cvt_pk_bf16_f32 v75, v54, v55\n"
"v_mfma_f32_32x32x16_bf16 v[32:47], v[124:127], v[92:95], v[32:47]\n"
"v_exp_f32 v56, v56\n"
"v_exp_f32 v57, v57\n"
"v_exp_f32 v58, v58\n"
"v_exp_f32 v59, v59\n"
"v_cvt_pk_bf16_f32 v76, v56, v57\n"
"v_cvt_pk_bf16_f32 v77, v58, v59\n"
"v_mfma_f32_32x32x16_bf16 v[136:151], v[132:135], v[68:71], v[136:151]\n"
"v_exp_f32 v60, v60\n"
"v_exp_f32 v61, v61\n"
"v_exp_f32 v62, v62\n"
"v_exp_f32 v63, v63\n"
"v_cvt_pk_bf16_f32 v78, v60, v61\n"
"v_cvt_pk_bf16_f32 v79, v62, v63\n"
"v_mfma_f32_32x32x16_bf16 v[152:167], v[128:131], v[72:75], v[152:167]\n"
"ds_read_b128 v[128:131], v170 offset:2304\n"
"ds_read_b128 v[176:179], v170 offset:2944\n"
"s_waitcnt vmcnt(16)\n"
"v_exp_f32 v32, v32\n"
"v_exp_f32_e64 v33, v33\n"
"v_mfma_f32_32x32x16_bf16 v[212:227], v[230:233], v[8:11], v[212:227]\n"
"v_exp_f32 v34, v34\n"
"v_exp_f32 v35, v35\n"
"v_cvt_pk_bf16_f32 v64, v32, v33\n"
"v_cvt_pk_bf16_f32 v65, v34, v35\n"
"v_mfma_f32_32x32x16_bf16 v[48:63], v[120:123], v[84:87], 0\n"
"buffer_load_dwordx4 v[8:11], v236, s[48:51], s65 offen sc1\n"
"v_exp_f32 v36, v36\n"
"v_exp_f32 v37, v37\n"
"v_exp_f32 v38, v38\n"
"v_exp_f32 v39, v39\n"
"v_cvt_pk_bf16_f32 v66, v36, v37\n"
"v_cvt_pk_bf16_f32 v67, v38, v39\n"
"v_mfma_f32_32x32x16_bf16 v[48:63], v[124:127], v[108:111], v[48:63]\n"
"ds_read_b128 v[120:123], v168 offset:4608\n"
"v_exp_f32 v40, v40\n"
"v_exp_f32 v41, v41\n"
"v_mfma_f32_32x32x16_bf16 v[212:227], v[230:233], v[12:15], v[212:227]\n"
"v_exp_f32 v42, v42\n"
"v_exp_f32 v43, v43\n"
"v_cvt_pk_bf16_f32 v68, v40, v41\n"
"v_cvt_pk_bf16_f32 v69, v42, v43\n"
"v_mfma_f32_32x32x16_bf16 v[152:167], v[132:135], v[76:79], v[152:167]\n"
"buffer_load_dwordx4 v[12:15], v237, s[48:51], s65 offen sc1\n"
"s_add_u32 s65, s65, 294912\n"
"v_exp_f32 v44, v44\n"
"v_exp_f32 v45, v45\n"
"v_exp_f32 v46, v46\n"
"s_waitcnt lgkmcnt(0)\n"
"v_exp_f32_e64 v47, v47\n"
"v_cvt_pk_bf16_f32 v70, v44, v45\n"
"v_cvt_pk_bf16_f32 v71, v46, v47\n"
"v_mfma_f32_32x32x16_bf16 v[136:151], v[128:131], v[64:67], v[136:151]\n"
"v_exp_f32 v48, v48\n"
"v_exp_f32 v49, v49\n"
"v_exp_f32 v50, v50\n"
"v_exp_f32 v51, v51\n"
"v_cvt_pk_bf16_f32 v72, v48, v49\n"
"v_cvt_pk_bf16_f32 v73, v50, v51\n"
"v_mfma_f32_32x32x16_bf16 v[32:47], v[120:123], v[80:83], 0\n"
"v_exp_f32 v52, v52\n"
"v_exp_f32 v53, v53\n"
"v_exp_f32 v54, v54\n"
"v_exp_f32 v55, v55\n"
"v_cvt_pk_bf16_f32 v74, v52, v53\n"
"v_cvt_pk_bf16_f32 v75, v54, v55\n"
"v_mfma_f32_32x32x16_bf16 v[32:47], v[124:127], v[96:99], v[32:47]\n"
"v_exp_f32 v56, v56\n"
"v_exp_f32 v57, v57\n"
"v_exp_f32 v58, v58\n"
"v_exp_f32 v59, v59\n"
"v_cvt_pk_bf16_f32 v76, v56, v57\n"
"v_cvt_pk_bf16_f32 v77, v58, v59\n"
"v_mfma_f32_32x32x16_bf16 v[136:151], v[176:179], v[68:71], v[136:151]\n"
"v_exp_f32 v60, v60\n"
"v_exp_f32 v61, v61\n"
"v_exp_f32 v62, v62\n"
"v_exp_f32 v63, v63\n"
"v_cvt_pk_bf16_f32 v78, v60, v61\n"
"v_cvt_pk_bf16_f32 v79, v62, v63\n"
"v_mfma_f32_32x32x16_bf16 v[152:167], v[128:131], v[72:75], v[152:167]\n"
"ds_read_b128 v[128:131], v170 offset:4608\n"
"ds_read_b128 v[132:135], v170 offset:5248\n"
"s_waitcnt vmcnt(16)\n"
"v_exp_f32 v32, v32\n"
"v_exp_f32_e64 v33, v33\n"
"v_mfma_f32_32x32x16_bf16 v[212:227], v[230:233], v[16:19], v[212:227]\n"
"v_exp_f32 v34, v34\n"
"v_exp_f32 v35, v35\n"
"v_cvt_pk_bf16_f32 v64, v32, v33\n"
"v_cvt_pk_bf16_f32 v65, v34, v35\n"
"v_mfma_f32_32x32x16_bf16 v[48:63], v[120:123], v[84:87], 0\n"
"buffer_load_dwordx4 v[16:19], v236, s[48:51], s65 offen sc1\n"
"v_exp_f32 v36, v36\n"
"v_exp_f32 v37, v37\n"
"v_exp_f32 v38, v38\n"
"v_exp_f32 v39, v39\n"
"v_cvt_pk_bf16_f32 v66, v36, v37\n"
"v_cvt_pk_bf16_f32 v67, v38, v39\n"
"v_mfma_f32_32x32x16_bf16 v[48:63], v[124:127], v[112:115], v[48:63]\n"
"ds_read_b128 v[120:123], v168 offset:6912\n"
"v_exp_f32 v40, v40\n"
"v_exp_f32 v41, v41\n"
"v_mfma_f32_32x32x16_bf16 v[212:227], v[230:233], v[20:23], v[212:227]\n"
"v_exp_f32 v42, v42\n"
"v_exp_f32 v43, v43\n"
"v_cvt_pk_bf16_f32 v68, v40, v41\n"
"v_cvt_pk_bf16_f32 v69, v42, v43\n"
"v_mfma_f32_32x32x16_bf16 v[152:167], v[176:179], v[76:79], v[152:167]\n"
"buffer_load_dwordx4 v[20:23], v237, s[48:51], s65 offen sc1\n"
"s_add_u32 s65, s65, 294912\n"
"v_exp_f32 v44, v44\n"
"v_exp_f32 v45, v45\n"
"v_exp_f32 v46, v46\n"
"s_waitcnt lgkmcnt(0)\n"
"v_exp_f32_e64 v47, v47\n"
"v_cvt_pk_bf16_f32 v70, v44, v45\n"
"v_cvt_pk_bf16_f32 v71, v46, v47\n"
"v_mfma_f32_32x32x16_bf16 v[136:151], v[128:131], v[64:67], v[136:151]\n"
"v_exp_f32 v48, v48\n"
"v_exp_f32 v49, v49\n"
"v_exp_f32 v50, v50\n"
"v_exp_f32 v51, v51\n"
"v_cvt_pk_bf16_f32 v72, v48, v49\n"
"v_cvt_pk_bf16_f32 v73, v50, v51\n"
"v_mfma_f32_32x32x16_bf16 v[32:47], v[120:123], v[80:83], 0\n"
"v_exp_f32 v52, v52\n"
"v_exp_f32 v53, v53\n"
"v_exp_f32 v54, v54\n"
"v_exp_f32 v55, v55\n"
"v_cvt_pk_bf16_f32 v74, v52, v53\n"
"v_cvt_pk_bf16_f32 v75, v54, v55\n"
"v_mfma_f32_32x32x16_bf16 v[32:47], v[124:127], v[100:103], v[32:47]\n"
"v_exp_f32 v56, v56\n"
"v_exp_f32 v57, v57\n"
"v_exp_f32 v58, v58\n"
"v_exp_f32 v59, v59\n"
"v_cvt_pk_bf16_f32 v76, v56, v57\n"
"v_cvt_pk_bf16_f32 v77, v58, v59\n"
"v_mfma_f32_32x32x16_bf16 v[136:151], v[132:135], v[68:71], v[136:151]\n"
"v_exp_f32 v60, v60\n"
"v_exp_f32 v61, v61\n"
"v_exp_f32 v62, v62\n"
"v_exp_f32 v63, v63\n"
"v_cvt_pk_bf16_f32 v78, v60, v61\n"
"v_cvt_pk_bf16_f32 v79, v62, v63\n"
"v_mfma_f32_32x32x16_bf16 v[152:167], v[128:131], v[72:75], v[152:167]\n"
"ds_read_b128 v[128:131], v170 offset:6912\n"
"ds_read_b128 v[176:179], v170 offset:7552\n"
"s_waitcnt vmcnt(16)\n"
"v_exp_f32 v32, v32\n"
"v_exp_f32_e64 v33, v33\n"
"v_mfma_f32_32x32x16_bf16 v[212:227], v[230:233], v[24:27], v[212:227]\n"
"v_exp_f32 v34, v34\n"
"v_exp_f32 v35, v35\n"
"v_cvt_pk_bf16_f32 v64, v32, v33\n"
"v_cvt_pk_bf16_f32 v65, v34, v35\n"
"v_mfma_f32_32x32x16_bf16 v[48:63], v[120:123], v[84:87], 0\n"
"buffer_load_dwordx4 v[24:27], v236, s[48:51], s65 offen sc1\n"
"v_exp_f32 v36, v36\n"
"v_exp_f32 v37, v37\n"
"v_exp_f32 v38, v38\n"
"v_exp_f32 v39, v39\n"
"v_cvt_pk_bf16_f32 v66, v36, v37\n"
"v_cvt_pk_bf16_f32 v67, v38, v39\n"
"v_mfma_f32_32x32x16_bf16 v[48:63], v[124:127], v[116:119], v[48:63]\n"
"ds_read_b128 v[120:123], v168 offset:9216\n"
"ds_read_b128 v[124:127], v169 offset:512\n"
"v_exp_f32 v40, v40\n"
"v_exp_f32 v41, v41\n"
"v_mfma_f32_32x32x16_bf16 v[212:227], v[230:233], v[28:31], v[212:227]\n"
"v_exp_f32 v42, v42\n"
"v_exp_f32 v43, v43\n"
"v_cvt_pk_bf16_f32 v68, v40, v41\n"
"v_cvt_pk_bf16_f32 v69, v42, v43\n"
"v_mfma_f32_32x32x16_bf16 v[152:167], v[132:135], v[76:79], v[152:167]\n"
"buffer_load_dwordx4 v[28:31], v237, s[48:51], s65 offen sc1\n"
"s_add_u32 s65, s65, 294912\n"
"v_exp_f32 v44, v44\n"
"v_exp_f32 v45, v45\n"
"v_exp_f32 v46, v46\n"
"s_waitcnt lgkmcnt(0)\n"
"v_exp_f32_e64 v47, v47\n"
"v_cvt_pk_bf16_f32 v70, v44, v45\n"
"v_cvt_pk_bf16_f32 v71, v46, v47\n"
"v_mfma_f32_32x32x16_bf16 v[136:151], v[128:131], v[64:67], v[136:151]\n"
"v_exp_f32 v48, v48\n"
"v_exp_f32 v49, v49\n"
"v_exp_f32 v50, v50\n"
"v_exp_f32 v51, v51\n"
"v_cvt_pk_bf16_f32 v72, v48, v49\n"
"v_cvt_pk_bf16_f32 v73, v50, v51\n"
"v_mfma_f32_32x32x16_bf16 v[32:47], v[120:123], v[80:83], 0\n"
"v_exp_f32 v52, v52\n"
"v_exp_f32 v53, v53\n"
"v_exp_f32 v54, v54\n"
"v_exp_f32 v55, v55\n"
"v_cvt_pk_bf16_f32 v74, v52, v53\n"
"v_cvt_pk_bf16_f32 v75, v54, v55\n"
"v_mfma_f32_32x32x16_bf16 v[32:47], v[124:127], v[88:91], v[32:47]\n"
"v_exp_f32 v56, v56\n"
"v_exp_f32 v57, v57\n"
"v_exp_f32 v58, v58\n"
"v_exp_f32 v59, v59\n"
"v_cvt_pk_bf16_f32 v76, v56, v57\n"
"v_cvt_pk_bf16_f32 v77, v58, v59\n"
"v_mfma_f32_32x32x16_bf16 v[136:151], v[176:179], v[68:71], v[136:151]\n"
"v_exp_f32 v60, v60\n"
"v_exp_f32 v61, v61\n"
"v_exp_f32 v62, v62\n"
"v_exp_f32 v63, v63\n"
"v_cvt_pk_bf16_f32 v78, v60, v61\n"
"v_cvt_pk_bf16_f32 v79, v62, v63\n"
"v_mfma_f32_32x32x16_bf16 v[152:167], v[128:131], v[72:75], v[152:167]\n"
"ds_read_b128 v[128:131], v170 offset:9216\n"
"ds_read_b128 v[132:135], v170 offset:9856\n"
"s_waitcnt vmcnt(16)\n"
"v_exp_f32 v32, v32\n"
"v_exp_f32_e64 v33, v33\n"
"v_mfma_f32_32x32x16_bf16 v[212:227], v[230:233], v[180:183], v[212:227]\n"
"v_exp_f32 v34, v34\n"
"v_exp_f32 v35, v35\n"
"v_cvt_pk_bf16_f32 v64, v32, v33\n"
"v_cvt_pk_bf16_f32 v65, v34, v35\n"
"v_mfma_f32_32x32x16_bf16 v[48:63], v[120:123], v[84:87], 0\n"
"buffer_load_dwordx4 v[180:183], v236, s[48:51], s65 offen sc1\n"
"v_exp_f32 v36, v36\n"
"v_exp_f32 v37, v37\n"
"v_exp_f32 v38, v38\n"
"v_exp_f32 v39, v39\n"
"v_cvt_pk_bf16_f32 v66, v36, v37\n"
"v_cvt_pk_bf16_f32 v67, v38, v39\n"
"v_mfma_f32_32x32x16_bf16 v[48:63], v[124:127], v[104:107], v[48:63]\n"
"ds_read_b128 v[120:123], v168 offset:11520\n"
"v_exp_f32 v40, v40\n"
"v_exp_f32 v41, v41\n"
"v_mfma_f32_32x32x16_bf16 v[212:227], v[230:233], v[184:187], v[212:227]\n"
"v_exp_f32 v42, v42\n"
"v_exp_f32 v43, v43\n"
"v_cvt_pk_bf16_f32 v68, v40, v41\n"
"v_cvt_pk_bf16_f32 v69, v42, v43\n"
"v_mfma_f32_32x32x16_bf16 v[152:167], v[176:179], v[76:79], v[152:167]\n"
"buffer_load_dwordx4 v[184:187], v237, s[48:51], s65 offen sc1\n"
"s_add_u32 s65, s65, 294912\n"
"v_exp_f32 v44, v44\n"
"v_exp_f32 v45, v45\n"
"v_exp_f32 v46, v46\n"
"s_waitcnt lgkmcnt(0)\n"
"v_exp_f32_e64 v47, v47\n"
"v_cvt_pk_bf16_f32 v70, v44, v45\n"
"v_cvt_pk_bf16_f32 v71, v46, v47\n"
"v_mfma_f32_32x32x16_bf16 v[136:151], v[128:131], v[64:67], v[136:151]\n"
"v_exp_f32 v48, v48\n"
"v_exp_f32 v49, v49\n"
"v_exp_f32 v50, v50\n"
"v_exp_f32 v51, v51\n"
"v_cvt_pk_bf16_f32 v72, v48, v49\n"
"v_cvt_pk_bf16_f32 v73, v50, v51\n"
"v_mfma_f32_32x32x16_bf16 v[32:47], v[120:123], v[80:83], 0\n"
"v_exp_f32 v52, v52\n"
"v_exp_f32 v53, v53\n"
"v_exp_f32 v54, v54\n"
"v_exp_f32 v55, v55\n"
"v_cvt_pk_bf16_f32 v74, v52, v53\n"
"v_cvt_pk_bf16_f32 v75, v54, v55\n"
"v_mfma_f32_32x32x16_bf16 v[32:47], v[124:127], v[92:95], v[32:47]\n"
"v_exp_f32 v56, v56\n"
"v_exp_f32 v57, v57\n"
"v_exp_f32 v58, v58\n"
"v_exp_f32 v59, v59\n"
"v_cvt_pk_bf16_f32 v76, v56, v57\n"
"v_cvt_pk_bf16_f32 v77, v58, v59\n"
"v_mfma_f32_32x32x16_bf16 v[136:151], v[132:135], v[68:71], v[136:151]\n"
"v_exp_f32 v60, v60\n"
"v_exp_f32 v61, v61\n"
"v_exp_f32 v62, v62\n"
"v_exp_f32 v63, v63\n"
"v_cvt_pk_bf16_f32 v78, v60, v61\n"
"v_cvt_pk_bf16_f32 v79, v62, v63\n"
"v_mfma_f32_32x32x16_bf16 v[152:167], v[128:131], v[72:75], v[152:167]\n"
"ds_read_b128 v[128:131], v170 offset:11520\n"
"ds_read_b128 v[176:179], v170 offset:12160\n"
"s_waitcnt vmcnt(16)\n"
"v_exp_f32 v32, v32\n"
"v_exp_f32_e64 v33, v33\n"
"v_mfma_f32_32x32x16_bf16 v[212:227], v[230:233], v[188:191], v[212:227]\n"
"v_exp_f32 v34, v34\n"
"v_exp_f32 v35, v35\n"
"v_cvt_pk_bf16_f32 v64, v32, v33\n"
"v_cvt_pk_bf16_f32 v65, v34, v35\n"
"v_mfma_f32_32x32x16_bf16 v[48:63], v[120:123], v[84:87], 0\n"
"buffer_load_dwordx4 v[188:191], v236, s[48:51], s65 offen sc1\n"
"v_exp_f32 v36, v36\n"
"v_exp_f32 v37, v37\n"
"v_exp_f32 v38, v38\n"
"v_exp_f32 v39, v39\n"
"v_cvt_pk_bf16_f32 v66, v36, v37\n"
"v_cvt_pk_bf16_f32 v67, v38, v39\n"
"v_mfma_f32_32x32x16_bf16 v[48:63], v[124:127], v[108:111], v[48:63]\n"
"ds_read_b128 v[120:123], v168 offset:13824\n"
"v_exp_f32 v40, v40\n"
"v_exp_f32 v41, v41\n"
"v_mfma_f32_32x32x16_bf16 v[212:227], v[230:233], v[192:195], v[212:227]\n"
"v_exp_f32 v42, v42\n"
"v_exp_f32 v43, v43\n"
"v_cvt_pk_bf16_f32 v68, v40, v41\n"
"v_cvt_pk_bf16_f32 v69, v42, v43\n"
"v_mfma_f32_32x32x16_bf16 v[152:167], v[132:135], v[76:79], v[152:167]\n"
"buffer_load_dwordx4 v[192:195], v237, s[48:51], s65 offen sc1\n"
"s_add_u32 s65, s65, 294912\n"
"v_exp_f32 v44, v44\n"
"v_exp_f32 v45, v45\n"
"v_exp_f32 v46, v46\n"
"s_waitcnt lgkmcnt(0)\n"
"v_exp_f32_e64 v47, v47\n"
"v_cvt_pk_bf16_f32 v70, v44, v45\n"
"v_cvt_pk_bf16_f32 v71, v46, v47\n"
"v_mfma_f32_32x32x16_bf16 v[136:151], v[128:131], v[64:67], v[136:151]\n"
"v_exp_f32 v48, v48\n"
"v_exp_f32 v49, v49\n"
"v_exp_f32 v50, v50\n"
"v_exp_f32 v51, v51\n"
"v_cvt_pk_bf16_f32 v72, v48, v49\n"
"v_cvt_pk_bf16_f32 v73, v50, v51\n"
"v_mfma_f32_32x32x16_bf16 v[32:47], v[120:123], v[80:83], 0\n"
"v_exp_f32 v52, v52\n"
"v_exp_f32 v53, v53\n"
"v_exp_f32 v54, v54\n"
"v_exp_f32 v55, v55\n"
"v_cvt_pk_bf16_f32 v74, v52, v53\n"
"v_cvt_pk_bf16_f32 v75, v54, v55\n"
"v_mfma_f32_32x32x16_bf16 v[32:47], v[124:127], v[96:99], v[32:47]\n"
"v_exp_f32 v56, v56\n"
"v_exp_f32 v57, v57\n"
"v_exp_f32 v58, v58\n"
"v_exp_f32 v59, v59\n"
"v_cvt_pk_bf16_f32 v76, v56, v57\n"
"v_cvt_pk_bf16_f32 v77, v58, v59\n"
"v_mfma_f32_32x32x16_bf16 v[136:151], v[176:179], v[68:71], v[136:151]\n"
"v_exp_f32 v60, v60\n"
"v_exp_f32 v61, v61\n"
"v_exp_f32 v62, v62\n"
"v_exp_f32 v63, v63\n"
"v_cvt_pk_bf16_f32 v78, v60, v61\n"
"v_cvt_pk_bf16_f32 v79, v62, v63\n"
"v_mfma_f32_32x32x16_bf16 v[152:167], v[128:131], v[72:75], v[152:167]\n"
"ds_read_b128 v[128:131], v170 offset:13824\n"
"ds_read_b128 v[132:135], v170 offset:14464\n"
"s_waitcnt vmcnt(16)\n"
"v_exp_f32 v32, v32\n"
"v_exp_f32_e64 v33, v33\n"
"v_mfma_f32_32x32x16_bf16 v[212:227], v[230:233], v[196:199], v[212:227]\n"
"v_exp_f32 v34, v34\n"
"v_exp_f32 v35, v35\n"
"v_cvt_pk_bf16_f32 v64, v32, v33\n"
"v_cvt_pk_bf16_f32 v65, v34, v35\n"
"v_mfma_f32_32x32x16_bf16 v[48:63], v[120:123], v[84:87], 0\n"
"buffer_load_dwordx4 v[196:199], v236, s[48:51], s65 offen sc1\n"
"v_exp_f32 v36, v36\n"
"v_exp_f32 v37, v37\n"
"v_exp_f32 v38, v38\n"
"v_exp_f32 v39, v39\n"
"v_cvt_pk_bf16_f32 v66, v36, v37\n"
"v_cvt_pk_bf16_f32 v67, v38, v39\n"
"v_mfma_f32_32x32x16_bf16 v[48:63], v[124:127], v[112:115], v[48:63]\n"
"ds_read_b128 v[120:123], v168 offset:16128\n"
"v_exp_f32 v40, v40\n"
"v_exp_f32 v41, v41\n"
"v_mfma_f32_32x32x16_bf16 v[212:227], v[230:233], v[200:203], v[212:227]\n"
"v_exp_f32 v42, v42\n"
"v_exp_f32 v43, v43\n"
"v_cvt_pk_bf16_f32 v68, v40, v41\n"
"v_cvt_pk_bf16_f32 v69, v42, v43\n"
"v_mfma_f32_32x32x16_bf16 v[152:167], v[176:179], v[76:79], v[152:167]\n"
"buffer_load_dwordx4 v[200:203], v237, s[48:51], s65 offen sc1\n"
"s_add_u32 s65, s65, 294912\n"
"v_exp_f32 v44, v44\n"
"v_exp_f32 v45, v45\n"
"v_exp_f32 v46, v46\n"
"s_waitcnt lgkmcnt(0)\n"
"v_exp_f32_e64 v47, v47\n"
"v_cvt_pk_bf16_f32 v70, v44, v45\n"
"v_cvt_pk_bf16_f32 v71, v46, v47\n"
"v_mfma_f32_32x32x16_bf16 v[136:151], v[128:131], v[64:67], v[136:151]\n"
"v_exp_f32 v48, v48\n"
"v_exp_f32 v49, v49\n"
"v_exp_f32 v50, v50\n"
"v_exp_f32 v51, v51\n"
"v_cvt_pk_bf16_f32 v72, v48, v49\n"
"v_cvt_pk_bf16_f32 v73, v50, v51\n"
"v_mfma_f32_32x32x16_bf16 v[32:47], v[120:123], v[80:83], 0\n"
"v_exp_f32 v52, v52\n"
"v_exp_f32 v53, v53\n"
"v_exp_f32 v54, v54\n"
"v_exp_f32 v55, v55\n"
"v_cvt_pk_bf16_f32 v74, v52, v53\n"
"v_cvt_pk_bf16_f32 v75, v54, v55\n"
"v_mfma_f32_32x32x16_bf16 v[32:47], v[124:127], v[100:103], v[32:47]\n"
"v_exp_f32 v56, v56\n"
"v_exp_f32 v57, v57\n"
"v_exp_f32 v58, v58\n"
"v_exp_f32 v59, v59\n"
"v_cvt_pk_bf16_f32 v76, v56, v57\n"
"v_cvt_pk_bf16_f32 v77, v58, v59\n"
"v_mfma_f32_32x32x16_bf16 v[136:151], v[132:135], v[68:71], v[136:151]\n"
"v_exp_f32 v60, v60\n"
"v_exp_f32 v61, v61\n"
"v_exp_f32 v62, v62\n"
"v_exp_f32 v63, v63\n"
"v_cvt_pk_bf16_f32 v78, v60, v61\n"
"v_cvt_pk_bf16_f32 v79, v62, v63\n"
"v_mfma_f32_32x32x16_bf16 v[152:167], v[128:131], v[72:75], v[152:167]\n"
"ds_read_b128 v[128:131], v170 offset:16128\n"
"ds_read_b128 v[176:179], v170 offset:16768\n"
"s_waitcnt vmcnt(16)\n"
"v_exp_f32 v32, v32\n"
"v_exp_f32_e64 v33, v33\n"
"v_mfma_f32_32x32x16_bf16 v[212:227], v[230:233], v[204:207], v[212:227]\n"
"v_exp_f32 v34, v34\n"
"v_exp_f32 v35, v35\n"
"v_cvt_pk_bf16_f32 v64, v32, v33\n"
"v_cvt_pk_bf16_f32 v65, v34, v35\n"
"v_mfma_f32_32x32x16_bf16 v[48:63], v[120:123], v[84:87], 0\n"
"buffer_load_dwordx4 v[204:207], v236, s[48:51], s65 offen sc1\n"
"v_exp_f32 v36, v36\n"
"v_exp_f32 v37, v37\n"
"v_exp_f32 v38, v38\n"
"v_exp_f32 v39, v39\n"
"v_cvt_pk_bf16_f32 v66, v36, v37\n"
"v_cvt_pk_bf16_f32 v67, v38, v39\n"
"v_mfma_f32_32x32x16_bf16 v[48:63], v[124:127], v[116:119], v[48:63]\n"
"ds_read_b128 v[120:123], v168 offset:18432\n"
"ds_read_b128 v[124:127], v169 offset:1024\n"
"v_exp_f32 v40, v40\n"
"v_exp_f32 v41, v41\n"
"v_mfma_f32_32x32x16_bf16 v[212:227], v[230:233], v[208:211], v[212:227]\n"
"v_exp_f32 v42, v42\n"
"v_exp_f32 v43, v43\n"
"v_cvt_pk_bf16_f32 v68, v40, v41\n"
"v_cvt_pk_bf16_f32 v69, v42, v43\n"
"v_mfma_f32_32x32x16_bf16 v[152:167], v[132:135], v[76:79], v[152:167]\n"
"buffer_load_dwordx4 v[208:211], v237, s[48:51], s65 offen sc1\n"
"v_exp_f32 v44, v44\n"
"v_exp_f32 v45, v45\n"
"v_exp_f32 v46, v46\n"
"s_waitcnt lgkmcnt(0)\n"
"v_exp_f32_e64 v47, v47\n"
"v_cvt_pk_bf16_f32 v70, v44, v45\n"
"v_cvt_pk_bf16_f32 v71, v46, v47\n"
"v_mfma_f32_32x32x16_bf16 v[136:151], v[128:131], v[64:67], v[136:151]\n"
"v_exp_f32 v48, v48\n"
"v_exp_f32 v49, v49\n"
"v_exp_f32 v50, v50\n"
"v_exp_f32 v51, v51\n"
"v_cvt_pk_bf16_f32 v72, v48, v49\n"
"v_cvt_pk_bf16_f32 v73, v50, v51\n"
"v_mfma_f32_32x32x16_bf16 v[32:47], v[120:123], v[80:83], 0\n"
"v_exp_f32 v52, v52\n"
"v_exp_f32 v53, v53\n"
"v_exp_f32 v54, v54\n"
"v_exp_f32 v55, v55\n"
"v_cvt_pk_bf16_f32 v74, v52, v53\n"
"v_cvt_pk_bf16_f32 v75, v54, v55\n"
"v_mfma_f32_32x32x16_bf16 v[32:47], v[124:127], v[88:91], v[32:47]\n"
"v_exp_f32 v56, v56\n"
"v_exp_f32 v57, v57\n"
"v_exp_f32 v58, v58\n"
"v_exp_f32 v59, v59\n"
"v_cvt_pk_bf16_f32 v76, v56, v57\n"
"v_cvt_pk_bf16_f32 v77, v58, v59\n"
"v_mfma_f32_32x32x16_bf16 v[136:151], v[176:179], v[68:71], v[136:151]\n"
"v_exp_f32 v60, v60\n"
"v_exp_f32 v61, v61\n"
"v_exp_f32 v62, v62\n"
"v_exp_f32 v63, v63\n"
"v_cvt_pk_bf16_f32 v78, v60, v61\n"
"v_cvt_pk_bf16_f32 v79, v62, v63\n"
"v_add_u32 v168, 18432, v168\n"
"v_add_u32 v170, 18432, v170\n"
"v_add_u32 v169, 1024, v169\n"
"buffer_store_dwordx4 v[212:215], v235, s[48:51], s66 offen sc1\n"
"buffer_store_dwordx4 v[216:219], v235, s[48:51], s66 offen offset:1024 sc1\n"
"s_sub_u32 s74, s74, 1\n"
"s_cmp_lg_u32 s74, 0\n"
"s_cbranch_scc1 LRED_%=\n"
"v_mfma_f32_32x32x16_bf16 v[152:167], v[128:131], v[72:75], v[152:167]\n"
"v_mfma_f32_32x32x16_bf16 v[152:167], v[176:179], v[76:79], v[152:167]\n"
"s_nop 11\n"
"v_and_b32_e64 v174, 31, v228\n"
"v_lshrrev_b32 v175, 5, v228\n"
"s_mul_i32 s56, s71, 18\n"
"v_lshlrev_b32 v40, 2, v175\n"
"v_add_u32 v40, 0, v40\n"
"v_add_u32 v40, s56, v40\n"
"v_lshrrev_b32 v41, 3, v40\n"
"v_lshlrev_b32 v41, 9, v41\n"
"v_and_b32 v40, 7, v40\n"
"v_lshl_add_u32 v41, v40, 1, v41\n"
"v_lshl_add_u32 v32, v174, 4, v41\n"
"v_lshlrev_b32 v40, 2, v175\n"
"v_add_u32 v40, 2, v40\n"
"v_add_u32 v40, s56, v40\n"
"v_lshrrev_b32 v41, 3, v40\n"
"v_lshlrev_b32 v41, 9, v41\n"
"v_and_b32 v40, 7, v40\n"
"v_lshl_add_u32 v41, v40, 1, v41\n"
"v_lshl_add_u32 v33, v174, 4, v41\n"
"v_lshlrev_b32 v40, 2, v175\n"
"v_add_u32 v40, 8, v40\n"
"v_add_u32 v40, s56, v40\n"
"v_lshrrev_b32 v41, 3, v40\n"
"v_lshlrev_b32 v41, 9, v41\n"
"v_and_b32 v40, 7, v40\n"
"v_lshl_add_u32 v41, v40, 1, v41\n"
"v_lshl_add_u32 v34, v174, 4, v41\n"
"v_lshlrev_b32 v40, 2, v175\n"
"v_add_u32 v40, 10, v40\n"
"v_add_u32 v40, s56, v40\n"
"v_lshrrev_b32 v41, 3, v40\n"
"v_lshlrev_b32 v41, 9, v41\n"
"v_and_b32 v40, 7, v40\n"
"v_lshl_add_u32 v41, v40, 1, v41\n"
"v_lshl_add_u32 v35, v174, 4, v41\n"
"v_lshlrev_b32 v40, 2, v175\n"
"v_add_u32 v40, 16, v40\n"
"v_add_u32 v40, s56, v40\n"
"v_lshrrev_b32 v41, 3, v40\n"
"v_lshlrev_b32 v41, 9, v41\n"
"v_and_b32 v40, 7, v40\n"
"v_lshl_add_u32 v41, v40, 1, v41\n"
"v_lshl_add_u32 v36, v174, 4, v41\n"
"v_cmp_gt_u32 vcc, 32, v228\n"
"v_mov_b32 v41, 0x7f000000\n"
"s_nop 0\n"
"v_cndmask_b32 v36, v41, v36\n"
"s_lshl_b32 s56, s72, 3\n"
"s_lshl_b32 s57, s52, 1\n"
"s_add_u32 s56, s56, s57\n"
"s_mul_i32 s56, s56, 36864\n"
"s_add_u32 s67, s56, 7356416\n"
"v_mov_b32 v44, v146\n"
"v_mov_b32 v45, v146\n"
"s_nop 1\n"
"v_permlane32_swap_b32 v44, v45\n"
"v_rcp_f32 v46, v44\n"
"s_nop 0\n"
"v_mul_f32 v136, v46, v136\n"
"v_mul_f32 v137, v46, v137\n"
"v_mul_f32 v138, v46, v138\n"
"v_mul_f32 v139, v46, v139\n"
"v_mul_f32 v140, v46, v140\n"
"v_mul_f32 v141, v46, v141\n"
"v_mul_f32 v142, v46, v142\n"
"v_mul_f32 v143, v46, v143\n"
"v_mul_f32 v144, v46, v144\n"
"v_mul_f32 v145, v46, v145\n"
"v_add_f32 v47, |v136|, |v137|\n"
"v_add_f32 v47, v47, |v138|\n"
"v_add_f32 v47, v47, |v139|\n"
"v_add_f32 v47, v47, |v140|\n"
"v_add_f32 v47, v47, |v141|\n"
"v_add_f32 v47, v47, |v142|\n"
"v_add_f32 v47, v47, |v143|\n"
"v_add_f32 v45, |v144|, |v145|\n"
"v_cmp_gt_u32 vcc, 32, v228\n"
"s_nop 1\n"
"v_cndmask_b32 v45, 0, v45\n"
"v_add_f32 v47, v47, v45\n"
"v_mov_b32 v45, 0x21800000\n"
"v_cmp_lt_f32 s[56:57], v45, v44\n"
"v_mov_b32 v45, 0x5d800000\n"
"v_cmp_gt_f32 s[58:59], v45, v44\n"
"v_mov_b32 v45, 0x7b800000\n"
"v_cmp_gt_f32 vcc, v45, v47\n"
"s_nop 3\n"
"s_and_b64 s[56:57], s[56:57], s[58:59]\n"
"s_and_b64 s[56:57], s[56:57], vcc\n"
"s_cmp_eq_u64 s[56:57], exec\n"
"s_cselect_b32 s56, 0, 1\n"
"s_or_b32 s75, s75, s56\n"
"v_cvt_pk_bf16_f32 v48, v136, v137\n"
"v_cvt_pk_bf16_f32 v49, v138, v139\n"
"v_cvt_pk_bf16_f32 v50, v140, v141\n"
"v_cvt_pk_bf16_f32 v51, v142, v143\n"
"v_cvt_pk_bf16_f32 v52, v144, v145\n"
"s_add_u32 s60, s67, 0\n"
"buffer_store_dword v48, v32, s[48:51], s60 offen sc1\n"
"buffer_store_dword v49, v33, s[48:51], s60 offen sc1\n"
"buffer_store_dword v50, v34, s[48:51], s60 offen sc1\n"
"buffer_store_dword v51, v35, s[48:51], s60 offen sc1\n"
"buffer_store_dword v52, v36, s[48:51], s60 offen sc1\n"
"v_mov_b32 v44, v162\n"
"v_mov_b32 v45, v162\n"
"s_nop 1\n"
"v_permlane32_swap_b32 v44, v45\n"
"v_rcp_f32 v46, v44\n"
"s_nop 0\n"
"v_mul_f32 v152, v46, v152\n"
"v_mul_f32 v153, v46, v153\n"
"v_mul_f32 v154, v46, v154\n"
"v_mul_f32 v155, v46, v155\n"
"v_mul_f32 v156, v46, v156\n"
"v_mul_f32 v157, v46, v157\n"
"v_mul_f32 v158, v46, v158\n"
"v_mul_f32 v159, v46, v159\n"
"v_mul_f32 v160, v46, v160\n"
"v_mul_f32 v161, v46, v161\n"
"v_add_f32 v47, |v152|, |v153|\n"
"v_add_f32 v47, v47, |v154|\n"
"v_add_f32 v47, v47, |v155|\n"
"v_add_f32 v47, v47, |v156|\n"
"v_add_f32 v47, v47, |v157|\n"
"v_add_f32 v47, v47, |v158|\n"
"v_add_f32 v47, v47, |v159|\n"
"v_add_f32 v45, |v160|, |v161|\n"
"v_cmp_gt_u32 vcc, 32, v228\n"
"s_nop 1\n"
"v_cndmask_b32 v45, 0, v45\n"
"v_add_f32 v47, v47, v45\n"
"v_mov_b32 v45, 0x21800000\n"
"v_cmp_lt_f32 s[56:57], v45, v44\n"
"v_mov_b32 v45, 0x5d800000\n"
"v_cmp_gt_f32 s[58:59], v45, v44\n"
"v_mov_b32 v45, 0x7b800000\n"
"v_cmp_gt_f32 vcc, v45, v47\n"
"s_nop 3\n"
"s_and_b64 s[56:57], s[56:57], s[58:59]\n"
"s_and_b64 s[56:57], s[56:57], vcc\n"
"s_cmp_eq_u64 s[56:57], exec\n"
"s_cselect_b32 s56, 0, 1\n"
"s_or_b32 s75, s75, s56\n"
"v_cvt_pk_bf16_f32 v48, v152, v153\n"
"v_cvt_pk_bf16_f32 v49, v154, v155\n"
"v_cvt_pk_bf16_f32 v50, v156, v157\n"
"v_cvt_pk_bf16_f32 v51, v158, v159\n"
"v_cvt_pk_bf16_f32 v52, v160, v161\n"
"s_add_u32 s60, s67, 36864\n"
"buffer_store_dword v48, v32, s[48:51], s60 offen sc1\n"
"buffer_store_dword v49, v33, s[48:51], s60 offen sc1\n"
"buffer_store_dword v50, v34, s[48:51], s60 offen sc1\n"
"buffer_store_dword v51, v35, s[48:51], s60 offen sc1\n"
"buffer_store_dword v52, v36, s[48:51], s60 offen sc1\n"
"s_waitcnt vmcnt(0)\n"
"s_mov_b32 %0, s75\n"
"LEND_%=:\n"
    : "=s"(bad) : "s"(__builtin_amdgcn_kernarg_segment_ptr()), "s"(blockIdx.x), "v"(threadIdx.x)
    : "v0", "v1", "v2", "v3", "v4", "v5", "v6", "v7", "v8", "v9", "v10", "v11", "v12", "v13", "v14", "v15", "v16", "v17", "v18", "v19", "v20", "v21", "v22", "v23", "v24", "v25", "v26", "v27", "v28", "v29", "v30", "v31", "v32", "v33", "v34", "v35", "v36", "v37", "v38", "v39", "v40", "v41", "v42", "v43", "v44", "v45", "v46", "v47", "v48", "v49", "v50", "v51", "v52", "v53", "v54", "v55", "v56", "v57", "v58", "v59", "v60", "v61", "v62", "v63", "v64", "v65", "v66", "v67", "v68", "v69", "v70", "v71", "v72", "v73", "v74", "v75", "v76", "v77", "v78", "v79", "v80", "v81", "v82", "v83", "v84", "v85", "v86", "v87", "v88", "v89", "v90", "v91", "v92", "v93", "v94", "v95", "v96", "v97", "v98", "v99", "v100", "v101", "v102", "v103", "v104", "v105", "v106", "v107", "v108", "v109", "v110", "v111", "v112", "v113", "v114", "v115", "v116", "v117", "v118", "v119", "v120", "v121", "v122", "v123", "v124", "v125", "v126", "v127", "v128", "v129", "v130", "v131", "v132", "v133", "v134", "v135", "v136", "v137", "v138", "v139", "v140", "v141", "v142", "v143", "v144", "v145", "v146", "v147", "v148", "v149", "v150", "v151", "v152", "v153", "v154", "v155", "v156", "v157", "v158", "v159", "v160", "v161", "v162", "v163", "v164", "v165", "v166", "v167", "v168", "v169", "v170", "v171", "v172", "v173", "v174", "v175", "v176", "v177", "v178", "v179", "v180", "v181", "v182", "v183", "v184", "v185", "v186", "v187", "v188", "v189", "v190", "v191", "v192", "v193", "v194", "v195", "v196", "v197", "v198", "v199", "v200", "v201", "v202", "v203", "v204", "v205", "v206", "v207", "v208", "v209", "v210", "v211", "v212", "v213", "v214", "v215", "v216", "v217", "v218", "v219", "v220", "v221", "v222", "v223", "v224", "v225", "v226", "v227", "v228", "v229", "v230", "v231", "v232", "v233", "v234", "v235", "v236", "v237", "v238", "v239", "v240", "v241", "v242", "v243", "v244", "v245", "v246", "v247", "v248", "v249", "v250", "v251", "v252", "v253", "a0", "a1", "a2", "a3", "a4", "a5", "a6", "a7", "a8", "a9", "a10", "a11", "a12", "a13", "a14", "a15", "a16", "a17", "a18", "a19", "a20", "a21", "a22", "a23", "a24", "a25", "a26", "a27", "a28", "a29", "a30", "a31", "a32", "a33", "a34", "a35", "a36", "a37", "a38", "a39", "a40", "a41", "a42", "a43", "a44", "a45", "a46", "a47", "a48", "a49", "a50", "a51", "a52", "a53", "a54", "a55", "a56", "a57", "a58", "a59", "a60", "a61", "a62", "a63", "a64", "a65", "a66", "a67", "a68", "a69", "a70", "a71", "a72", "a73", "a74", "a75", "a76", "a77", "a78", "a79", "a80", "a81", "a82", "a83", "a84", "a85", "a86", "a87", "a88", "a89", "a90", "a91", "a92", "a93", "a94", "a95", "a96", "a97", "a98", "a99", "a100", "a101", "a102", "a103", "a104", "a105", "a106", "a107", "a108", "a109", "a110", "a111", "a112", "a113", "a114", "a115", "a116", "a117", "a118", "a119", "a120", "a121", "a122", "a123", "a124", "a125", "a126", "a127", "a128", "a129", "a130", "a131", "a132", "a133", "a134", "a135", "a136", "a137", "a138", "a139", "a140", "a141", "a142", "a143", "a144", "a145", "a146", "a147", "a148", "a149", "a150", "a151", "a152", "a153", "a154", "a155", "a156", "a157", "a158", "a159", "a160", "a161", "a162", "a163", "a164", "a165", "a166", "a167", "a168", "a169", "a170", "a171", "a172", "a173", "a174", "a175", "a176", "a177", "a178", "a179", "a180", "a181", "a182", "a183", "a184", "a185", "a186", "a187", "a188", "a189", "a190", "a191", "a192", "a193", "a194", "a195", "a196", "a197", "a198", "a199", "a200", "a201", "a202", "a203", "a204", "a205", "a206", "a207", "a208", "a209", "a210", "a211", "a212", "a213", "a214", "a215", "a216", "a217", "a218", "a219", "a220", "a221", "a222", "a223", "a224", "a225", "a226", "a227", "a228", "a229", "a230", "a231", "a232", "a233", "a234", "a235", "a236", "a237", "a238", "a239", "a240", "a241", "a242", "a243", "a244", "a245", "a246", "a247", "a248", "a249", "a250", "a251", "a252", "a253", "a254", "a255", "s16", "s17", "s18", "s19", "s20", "s21", "s22", "s23", "s24", "s25", "s26", "s27", "s28", "s29", "s30", "s31", "s33", "s34", "s35", "s36", "s37", "s38", "s39", "s40", "s41", "s42", "s43", "s44", "s45", "s46", "s47", "s48", "s49", "s50", "s51", "s52", "s53", "s54", "s55", "s56", "s57", "s58", "s59", "s60", "s61", "s62", "s63", "s64", "s65", "s66", "s67", "s68", "s69", "s70", "s71", "s72", "s73", "s74", "s75", "s76", "s77", "s78", "s79", "s80", "s81", "s82", "s83", "s84", "s85", "s86", "s87", "s88", "s89", "s90", "s91", "s92", "s93", "s94", "s95", "s96", "s97", "s98", "s99", "vcc", "memory", "scc");
    if (bad & 1) {
        const int b = blockIdx.x, rb = (b & 1) * 8 + ((b >> 3) & 7), sp = ((b >> 1) & 3) * 4 + (b >> 6);
        attention_true_max(lds, (char*)prm.ws, 2 * sp + (rb & 1), (rb >> 1) * 8 + 2 * (int)(threadIdx.x >> 6), threadIdx.x & 63);
    }
}

extern "C" void kernel_launch(void* const* d_in, const int* in_sizes, int n_in, void* d_out, int out_size, void* d_ws, size_t ws_size, hipStream_t stream) {
    (void)in_sizes; (void)n_in; (void)out_size;
    d_ws = (char*)d_ws + ((ws_size - WS_END - 262144) & ~(size_t)0x1FFFFF);
    (void)hipFuncSetAttribute((const void*)k1_main, hipFuncAttributeMaxDynamicSharedMemorySize, 163840);
    K1Params p1; p1.x = d_in[0]; p1.wqkv = d_in[1]; p1.w1 = d_in[4]; p1.b1 = d_in[5]; p1.w2 = d_in[6]; p1.ws = d_ws; p1.dbg = d_ws; p1.wo = d_in[2];
    k1_main<<<256, 256, 163840, stream>>>(p1);
    K2Params p2; p2.x = d_in[0]; p2.wo = d_in[2]; p2.b2 = d_in[7]; p2.ws = d_ws; p2.out = d_out;
    (void)hipFuncSetAttribute((const void*)k2_final, hipFuncAttributeMaxDynamicSharedMemorySize, 73728);
    k2_final<<<144, 256, 73728, stream>>>(p2);
}
```
